```python
import torch

def _softmax(x, axis=-1):
    return torch.softmax(x, dim=axis)
def _gelu(x):
    return torch.nn.functional.gelu(x, approximate="tanh")
def _silu(x):
    return torch.nn.functional.silu(x)
def _sigmoid(x):
    return torch.sigmoid(x)
def _layernorm(x, axis=-1, eps=1e-5):
    m = x.mean(dim=axis, keepdim=True)
    v = x.var(dim=axis, keepdim=True, unbiased=False)
    return (x - m) / torch.sqrt(v + eps)
def _scatter_add(tgt, idx, src, axis=0):
    return tgt.clone().index_add_(axis, idx, src)
def _segment_sum(src, seg, n):
    out = torch.zeros((n,) + src.shape[1:], dtype=src.dtype)
    return out.index_add_(0, seg, src)
def _pad(x, pw):
    flat = tuple(v for pair in reversed(pw) for v in pair)
    return torch.nn.functional.pad(x, flat)

kernel_name = 'gather_scatter__cheap_equivariant_egnn__graph'

def setup_inputs():

    _w = (lambda a: (lambda t: t.to(torch.bfloat16) if t.is_floating_point() else t)(torch.as_tensor(a)))
    N, deg, D, De = 10000, 64, 64, max(0, 1)
    E = N * deg
    return {
        "h": _w((lambda *s: torch.randn(*s, dtype=torch.bfloat16) * 0.1)(N, D)),
        "pos": _w((lambda *s: torch.randn(*s, dtype=torch.bfloat16) * 0.8)(N, 3)),
        "e_attr": _w((lambda *s: torch.randn(*s, dtype=torch.bfloat16) * 0.8)(E, De)),
        "src": _w((lambda lo, hi, *s: torch.randint(lo, hi, s))(0, N, E).to(torch.int64)),
        "dst": _w((lambda lo, hi, *s: torch.randint(lo, hi, s))(0, N, E).to(torch.int64)),
        "w_e": _w(0.1 * (lambda *s: torch.randn(*s, dtype=torch.bfloat16) * 0.8)(2 * D + 1 + De, D)),
        "w_x": _w(0.1 * (lambda *s: torch.randn(*s, dtype=torch.bfloat16) * 0.1)(D, 1)),
        "w_h": _w(0.1 * (lambda *s: torch.randn(*s, dtype=torch.bfloat16) * 0.1)(2 * D, D)),
    }

def reference(h, pos, e_attr, src, dst, w_e, w_x, w_h):
    N, D = h.shape[0], h.shape[1]
    diff = pos[dst] - pos[src]
    dist2 = torch.sum(diff * diff, axis=-1, keepdims=True)
    e_in = torch.concatenate([h[dst], h[src], dist2, e_attr], axis=-1)
    m_ij = _silu(e_in @ w_e)
    coord_scale = m_ij @ w_x
    pos_new = pos + _segment_sum(diff * coord_scale, dst, N)
    m_i = _segment_sum(m_ij, dst, N)
    h_new = h + _silu(torch.concatenate([h, m_i], axis=-1) @ w_h)
    return h_new, pos_new
```

```cpp
#include <hip/hip_runtime.h>
#include <hip/hip_bf16.h>
#include <stdint.h>

#define DI __device__ __forceinline__
#define AS3 __attribute__((address_space(3)))

typedef short bf16x8 __attribute__((ext_vector_type(8)));
typedef short s16x4 __attribute__((ext_vector_type(4)));
typedef float f32x16 __attribute__((ext_vector_type(16)));
typedef float f32x4 __attribute__((ext_vector_type(4)));
typedef float f32x2 __attribute__((ext_vector_type(2)));
typedef unsigned u32x4 __attribute__((ext_vector_type(4)));
typedef unsigned u32x2 __attribute__((ext_vector_type(2)));
typedef int i32x4 __attribute__((ext_vector_type(4)));
typedef __bf16 bf16x2v __attribute__((ext_vector_type(2)));
typedef unsigned short u16;

namespace {

constexpr int NN = 10000;
constexpr int NE = 640000;
constexpr int NP = 256;
constexpr int CH = NE / NP;
constexpr int BN = 40;
constexpr int NBK = NN / BN;
constexpr int NTILE = (NN + 1 + 31) / 32;
constexpr int K1_THREADS = 640;
constexpr int K1_TABLE_BLOCKS = (NTILE * 4 + K1_THREADS / 64 - 1) / (K1_THREADS / 64);
constexpr float NLOG2E = -1.4426950408889634f;
constexpr float NLN2 = -0.6931471805599453f;

constexpr size_t WS_RECS = 0;
constexpr size_t WS_ENT = WS_RECS + (size_t)NP * CH * 8;
constexpr size_t WS_QTAB = WS_ENT + (size_t)NP * 256 * 16;
constexpr size_t WS_PTAB = WS_QTAB + (size_t)(NN + 2) * 128;
constexpr size_t WS_POS4 = WS_PTAB + (size_t)NN * 128;

constexpr int CAP = 2880;
constexpr int L_S = 0;
constexpr int S_WAVE = 2560;
constexpr int L_PD = 20480;
constexpr int L_WT = 21248;
constexpr int L_WX = 21504;
constexpr int L_TBL = 21760;
constexpr int L_RAW = L_TBL + 4096;
constexpr int L_PROWS = L_RAW + (CAP + 64) * 8;
constexpr int L_RING = L_PROWS + 5120;
constexpr int L_ACC = L_RING + 8 * 4096;
constexpr int ACC_NODE = 1040;
constexpr int L_END = L_ACC + BN * ACC_NODE;
static_assert(L_PD % 1024 == 0, "group id is read from bits 7..9 of the pos address");
static_assert(L_RAW + (CAP + 64) * 8 < 65536 && L_RAW >= 8 * CH, "run table holds 16-bit LDS addresses");
static_assert(L_PROWS % 16 == 0 && L_RING % 16 == 0 && L_ACC % 16 == 0 && L_END <= 163840, "LDS map");

DI unsigned cvt_pk(float a, float b) { f32x2 v = {a, b}; return __builtin_bit_cast(unsigned, __builtin_convertvector(v, bf16x2v)); }
DI float as_f(unsigned u) { return __builtin_bit_cast(float, u); }
DI float bf_lo(unsigned u) { return __builtin_bit_cast(float, u << 16); }
DI float bf_hi(unsigned u) { return __builtin_bit_cast(float, u & 0xffff0000u); }
DI float bf_round(float x) { return bf_lo(cvt_pk(x, x)); }
DI float bf_ld(const u16* p) { return __builtin_bit_cast(float, (unsigned)*p << 16); }
DI f32x16 mfma(bf16x8 a, bf16x8 b, f32x16 c) { return __builtin_amdgcn_mfma_f32_32x32x16_bf16(a, b, c, 0, 0, 0); }
DI bf16x8 as_frag(u32x4 v) { return __builtin_bit_cast(bf16x8, v); }

DI unsigned wave_incl_scan(unsigned x) {
  x += __builtin_amdgcn_update_dpp(0, (int)x, 0x111, 0xf, 0xf, true);
  x += __builtin_amdgcn_update_dpp(0, (int)x, 0x112, 0xf, 0xf, true);
  x += __builtin_amdgcn_update_dpp(0, (int)x, 0x114, 0xf, 0xf, true);
  x += __builtin_amdgcn_update_dpp(0, (int)x, 0x118, 0xf, 0xf, true);
  x += __builtin_amdgcn_update_dpp(0, (int)x, 0x142, 0xa, 0xf, false);
  x += __builtin_amdgcn_update_dpp(0, (int)x, 0x143, 0xc, 0xf, false);
  return x;
}

template <typename T> DI T lds_ld(AS3 char* L, unsigned off) { return *(AS3 T*)(L + off); }
template <typename T> DI void lds_st(AS3 char* L, unsigned off, T v) { *(AS3 T*)(L + off) = v; }
DI u32x2 lds_tr(AS3 char* L, unsigned off) {
  return __builtin_bit_cast(u32x2, __builtin_amdgcn_ds_read_tr16_b64_v4i16((AS3 s16x4*)(L + off)));
}

DI void dma_piece(const char* base, unsigned voff, unsigned ldsaddr) {
  unsigned keep;
  asm volatile("s_mov_b32 %0, m0\n\ts_mov_b32 m0, %3\n\ts_nop 0\n\tglobal_load_lds_dwordx4 %1, %2\n\ts_mov_b32 m0, %0"
               : "=&s"(keep) : "v"(voff), "s"(base), "s"(ldsaddr) : "memory");
}
DI void dma_tile(const char* base, unsigned v0, unsigned v1, unsigned v2, unsigned v3, unsigned ldsaddr) {
  unsigned keep;
  asm volatile("s_waitcnt lgkmcnt(0)\n\ts_mov_b32 %0, m0\n\ts_mov_b32 m0, %9\n\ts_nop 0\n\t"
               "global_load_lds_dwordx4 %1, %5\n\t"
               "global_load_lds_dwordx4 %2, %6 offset:1024\n\t"
               "global_load_lds_dwordx4 %3, %7 offset:2048\n\t"
               "global_load_lds_dwordx4 %4, %8 offset:3072\n\t"
               "s_mov_b32 m0, %0"
               : "=&s"(keep)
               : "v"(v0), "v"(v1), "v"(v2), "v"(v3), "s"(base), "s"(base - 1024), "s"(base - 2048), "s"(base - 3072), "s"(ldsaddr)
               : "memory");
}
DI void wait_vm0() { asm volatile("s_waitcnt vmcnt(0)" ::: "memory"); }

DI void table_task(int task, int lane, const u16* __restrict__ h, const u16* __restrict__ pos, const u16* __restrict__ we, char* ws) {
  const int T = task >> 2, m = (task >> 1) & 1, t = task & 1;
  const int r = lane & 31, hh = lane >> 5;
  const int node = 32 * T + r, nodec = node < NN ? node : NN - 1;
  f32x16 acc;
#pragma unroll
  for (int i = 0; i < 16; ++i) acc[i] = 0.f;
  bf16x8 hb[4], wa[4];
#pragma unroll
  for (int s = 0; s < 4; ++s) hb[s] = *(const bf16x8*)(h + nodec * 64 + 16 * s + 8 * hh);
#pragma unroll
  for (int s = 0; s < 4; ++s)
#pragma unroll
    for (int j = 0; j < 8; ++j) wa[s][j] = (short)we[(64 * m + 16 * s + 8 * hh + j) * 64 + 32 * t + r];
  u16 px = 0, py = 0, pz = 0;
  const bool do_pos = (m == 0 && t == 0 && hh == 0 && node <= NN);
  if (do_pos && node < NN) { px = pos[node * 3]; py = pos[node * 3 + 1]; pz = pos[node * 3 + 2]; }
#pragma unroll
  for (int s = 0; s < 4; ++s) acc = mfma(wa[s], hb[s], acc);
  u32x4 o0, o1;
#pragma unroll
  for (int i = 0; i < 4; ++i) {
    o0[i] = cvt_pk(acc[2 * i] * NLOG2E, acc[2 * i + 1] * NLOG2E);
    o1[i] = cvt_pk(acc[8 + 2 * i] * NLOG2E, acc[8 + 2 * i + 1] * NLOG2E);
  }
  char* tab = ws + (m ? WS_QTAB : WS_PTAB);
  if (node == NN) { o0 = u32x4{0, 0, 0, 0}; o1 = o0; }
  if (node < NN || (node == NN && m == 1)) {
    u32x4* dstp = (u32x4*)(tab + (size_t)node * 128 + 64 * t + 32 * hh);
    dstp[0] = o0;
    dstp[1] = o1;
  }
  if (do_pos) *(u32x2*)(ws + WS_POS4 + (size_t)node * 8) = u32x2{(unsigned)px | ((unsigned)py << 16), (unsigned)pz << 16};
}

__global__ __launch_bounds__(K1_THREADS) void sort_kernel(const int* __restrict__ src, const int* __restrict__ dst,
                                                          const u16* __restrict__ ea, const u16* __restrict__ h,
                                                          const u16* __restrict__ pos, const u16* __restrict__ we, char* ws) {
  __shared__ __attribute__((aligned(16))) char lds[4096 + 64 + CH * 8];
  AS3 char* L = (AS3 char*)lds;
  constexpr int C_CNT = 0, C_WT = 4096, C_SORT = 4160;
  const int tid = threadIdx.x, lane = tid & 63;
  const int w = __builtin_amdgcn_readfirstlane(tid >> 6);
  if (blockIdx.x >= NP) {
    const int task = (blockIdx.x - NP) * (K1_THREADS / 64) + w;
    if (task < NTILE * 4) table_task(task, lane, h, pos, we, ws);
    return;
  }
  const int p = blockIdx.x;
  const bool act = tid < CH / 4;
  i32x4 d4 = {0, 0, 0, 0}, s4 = {0, 0, 0, 0};
  u32x2 a2 = {0, 0};
  if (act) {
    const int e0 = p * CH + 4 * tid;
    d4 = *(const i32x4*)(dst + e0);
    s4 = *(const i32x4*)(src + e0);
    a2 = *(const u32x2*)(ea + e0);
  }
  if (tid < 256) lds_st<u32x4>(L, C_CNT + 16 * tid, u32x4{0, 0, 0, 0});
  __syncthreads();
  unsigned idx[4], rank[4], bk[4];
#pragma unroll
  for (int i = 0; i < 4; ++i) {
    const unsigned G = (unsigned)d4[i] >> 3;
    bk[i] = (G * 52429u) >> 18;
    idx[i] = G + 3 * bk[i];
    rank[i] = 0;
    if (act) {
      const unsigned sh = (idx[i] & 1) << 4;
      const unsigned old = __hip_atomic_fetch_add((AS3 unsigned*)(L + C_CNT + 4 * (idx[i] >> 1)), 1u << sh, __ATOMIC_RELAXED,
                                                  __HIP_MEMORY_SCOPE_WORKGROUP);
      rank[i] = (old >> sh) & 0xffffu;
    }
  }
  __syncthreads();
  u32x4 c = {0, 0, 0, 0};
  unsigned tot = 0, incl = 0;
  if (tid < 256) {
    c = lds_ld<u32x4>(L, C_CNT + 16 * tid);
    tot = (c[0] & 0xffffu) + (c[0] >> 16) + (c[1] & 0xffffu) + (c[1] >> 16) + (c[2] & 0xffffu);
    incl = wave_incl_scan(tot);
    if (lane == 63) lds_st<unsigned>(L, C_WT + 4 * w, incl);
  }
  __syncthreads();
  if (tid < 256) {
    const u32x4 wt = lds_ld<u32x4>(L, C_WT);
    const unsigned s0 = incl - tot + (w > 0 ? wt[0] : 0) + (w > 1 ? wt[1] : 0) + (w > 2 ? wt[2] : 0);
    const unsigned s1 = s0 + (c[0] & 0xffffu), s2 = s1 + (c[0] >> 16), s3 = s2 + (c[1] & 0xffffu), s4_ = s3 + (c[1] >> 16);
    const unsigned s5 = s0 + tot;
    const u32x4 e = {s0 | (s1 << 16), s2 | (s3 << 16), s4_ | (s5 << 16), s0 | (tot << 16)};
    lds_st<u32x4>(L, C_CNT + 16 * tid, e);
    *(u32x4*)(ws + WS_ENT + ((size_t)p * 256 + tid) * 16) = e;
  }
  __syncthreads();
  if (act) {
#pragma unroll
    for (int i = 0; i < 4; ++i) {
      const unsigned ps = lds_ld<u16>(L, C_CNT + 2 * idx[i]) + rank[i];
      const unsigned d = (unsigned)d4[i], dl = d - BN * bk[i];
      const unsigned eav = (i & 1) ? (a2[i >> 1] & 0xffff0000u) : (a2[i >> 1] << 16);
      const u32x2 rec = {(unsigned)s4[i] | ((L_PD + 16 * dl) << 16), ((d & 7) * 320) | eav};
      lds_st<u32x2>(L, C_SORT + 8 * ps, rec);
    }
  }
  __syncthreads();
  char* out = ws + WS_RECS + (size_t)p * (CH * 8);
#pragma unroll
  for (int i = 0; i < 2; ++i) {
    const int k = tid + i * K1_THREADS;
    if (k < CH / 2) *(u32x4*)(out + 16 * k) = lds_ld<u32x4>(L, C_SORT + 16 * k);
  }
}

struct Prep {
  unsigned sdA, sdB;
  unsigned saA, saB;
  unsigned de;
};

DI bf16x8 silu8(const f32x16& y, int base) {
  u32x4 o;
#pragma unroll
  for (int k = 0; k < 4; ++k) {
    const f32x2 yy = {y[base + 2 * k], y[base + 2 * k + 1]};
    const f32x2 e = {__builtin_amdgcn_exp2f(yy[0]), __builtin_amdgcn_exp2f(yy[1])};
    const f32x2 d = e + 1.0f;
    const f32x2 rr = {__builtin_amdgcn_rcpf(d[0]), __builtin_amdgcn_rcpf(d[1])};
    const f32x2 mm = yy * rr;
    o[k] = cvt_pk(mm[0], mm[1]);
  }
  return as_frag(o);
}

DI float silu_f(float x) { return x * __builtin_amdgcn_rcpf(1.0f + __builtin_amdgcn_exp2f(x * NLOG2E)); }

__global__ __launch_bounds__(512) void edge_kernel(const u16* __restrict__ h, const u16* __restrict__ pos, const u16* __restrict__ we,
                                                   const u16* __restrict__ wx, const u16* __restrict__ wh,
                                                   const char* __restrict__ ws, u16* __restrict__ out) {
  __shared__ __attribute__((aligned(1024))) char lds[L_END];
  AS3 char* L = (AS3 char*)lds;
  const unsigned LB = (unsigned)(size_t)L;
  const int tid = threadIdx.x, lane = tid & 63;
  const int w = __builtin_amdgcn_readfirstlane(tid >> 6);
  const int r = lane & 31, hf = lane >> 5;
  const int xl = blockIdx.x & 7;
  const int b = xl * 31 + (xl < 2 ? xl : 2) + (blockIdx.x >> 3);
  const int n0 = b * BN;
  const char* recs = ws + WS_RECS;
  const char* ents = ws + WS_ENT + (size_t)b * 16;
  const char* qtab = ws + WS_QTAB;
  const char* pos4 = ws + WS_POS4;

  const unsigned ringb = L_RING + w * 4096;
  unsigned aq[4];
#pragma unroll
  for (int s = 0; s < 4; ++s) aq[s] = ringb + r * 128 + (((2 * s + hf) ^ ((r >> 1) & 7)) << 4);
  const unsigned dc0 = ((lane & 7) ^ ((lane >> 4) & 7)) << 4, dc1 = ((lane & 7) ^ ((4 + (lane >> 4)) & 7)) << 4;
  const unsigned Sb = L_S + w * S_WAVE;
  const unsigned pi_e = (r & 0x13) | ((r & 4) << 1) | ((r & 8) >> 1);
  const unsigned sa_c = Sb + 2 * pi_e;
  const unsigned s_rd = Sb + r * 80 + 16 * hf;
  const int g16 = lane >> 4, tq = (lane & 15) >> 2, tp = lane & 3, tu = g16 & 1, th = g16 >> 1;
  const unsigned s_tr = Sb + (4 * (4 * th + tq) + 3) * 80 + 2 * (16 * tu + 8 * (tp & 1) + 4 * (tp >> 1));
  const unsigned p_tr = L_PROWS + (4 * th + tq) * 128 + 32 * (tp & 1) + 8 * (2 * tu + (tp >> 1));
  const unsigned acc_l = L_ACC + hf * ACC_NODE + 4 * r;

  bf16x8 bid[2];
#pragma unroll
  for (int u = 0; u < 2; ++u)
#pragma unroll
    for (int j = 0; j < 8; ++j) bid[u][j] = (r == (j & 3) + 8 * (2 * hf + (j >> 2)) + 4 * u) ? (short)0x3f80 : (short)0;

  f32x16 acc0, acc1;
#pragma unroll
  for (int i = 0; i < 16; ++i) { acc0[i] = 0.f; acc1[i] = 0.f; }
  unsigned cw[2][2];

  int pa = 0, np = NP;
  bool staged = false;
  for (;;) {
    unsigned e3[8];
#pragma unroll
    for (int k = 0; k < 8; ++k) e3[k] = *(const unsigned*)(ents + (size_t)(32 * k + (tid >> 4)) * 4096 + 12);
    u32x4 ent = {0, 0, 0, 0};
    if (tid < 256) ent = *(const u32x4*)(ents + (size_t)tid * 4096);

    if (!staged) {
      staged = true;
      if (w < 5) dma_piece(ws + WS_PTAB + (size_t)n0 * 128 + w * 1024, lane * 16, LB + L_PROWS + w * 1024);
      if (tid < 41) {
        f32x4 v = {0.f, 0.f, 0.f, 0.f};
        if (tid < BN) { const u16* pp = pos + (n0 + tid) * 3; v = f32x4{bf_ld(pp), bf_ld(pp + 1), bf_ld(pp + 2), 1.0f}; }
        lds_st<f32x4>(L, L_PD + 16 * tid, v);
      } else if (tid >= 64 && tid < 128) {
        lds_st<float>(L, L_WX + 4 * (tid - 64), bf_ld(wx + tid - 64));
      }
#pragma unroll
      for (int t = 0; t < 2; ++t) {
        const float vd = bf_ld(we + 128 * 64 + 32 * t + r) * NLOG2E, va = bf_ld(we + 129 * 64 + 32 * t + r) * NLOG2E;
        const unsigned hi = cvt_pk(vd, va);
        cw[t][0] = hi;
        cw[t][1] = cvt_pk(vd - bf_lo(hi), va - bf_hi(hi));
      }
      for (int o = tid * 16; o < 8 * S_WAVE; o += 512 * 16) lds_st<u32x4>(L, L_S + o, u32x4{0, 0, 0, 0});
      for (int o = tid * 16; o < BN * ACC_NODE; o += 512 * 16) lds_st<u32x4>(L, L_ACC + o, u32x4{0, 0, 0, 0});
    }

    const int j = tid & 15;
    u32x2 ra[8], rb[8];
    unsigned cnt[8];
#pragma unroll
    for (int k = 0; k < 8; ++k) {
      const int p = 32 * k + (tid >> 4);
      cnt[k] = ((unsigned)(p - pa) < (unsigned)np) ? (e3[k] >> 16) : 0;
      const char* sp = recs + ((size_t)p * CH + (e3[k] & 0xffffu) + j) * 8;
      ra[k] = u32x2{0, 0};
      rb[k] = u32x2{0, 0};
      if ((unsigned)j < cnt[k]) ra[k] = *(const u32x2*)sp;
      if ((unsigned)j + 16 < cnt[k]) rb[k] = *(const u32x2*)(sp + 128);
    }

    unsigned cA = 0, cB = 0, cC = 0, cD = 0, iA = 0, iB = 0, iC = 0, iD = 0;
    if (tid < 256) {
      if ((unsigned)(tid - pa) < (unsigned)np) {
        const unsigned s0 = ent[0] & 0xffffu, s1 = ent[0] >> 16, s2 = ent[1] & 0xffffu, s3 = ent[1] >> 16;
        const unsigned s4 = ent[2] & 0xffffu, s5 = ent[2] >> 16;
        cA = (s1 - s0) | ((s2 - s1) << 16);
        cB = (s3 - s2) | ((s4 - s3) << 16);
        cC = s5 - s4;
        cD = s5 - s0;
      }
      iA = wave_incl_scan(cA); iB = wave_incl_scan(cB); iC = wave_incl_scan(cC); iD = wave_incl_scan(cD);
      if (lane == 63) {
        lds_st<unsigned>(L, L_WT + 4 * w, iA); lds_st<unsigned>(L, L_WT + 16 + 4 * w, iB);
        lds_st<unsigned>(L, L_WT + 32 + 4 * w, iC); lds_st<unsigned>(L, L_WT + 48 + 4 * w, iD);
      }
    }
    __syncthreads();
    const u32x4 wA = lds_ld<u32x4>(L, L_WT), wB = lds_ld<u32x4>(L, L_WT + 16), wC = lds_ld<u32x4>(L, L_WT + 32), wD = lds_ld<u32x4>(L, L_WT + 48);
    const unsigned tA = wA[0] + wA[1] + wA[2] + wA[3], tB = wB[0] + wB[1] + wB[2] + wB[3];
    const unsigned tC = wC[0] + wC[1] + wC[2] + wC[3], tD = wD[0] + wD[1] + wD[2] + wD[3];
    unsigned tot[5] = {tA & 0xffffu, tA >> 16, tB & 0xffffu, tB >> 16, tC};
    unsigned base[6];
    base[0] = 0;
#pragma unroll
    for (int g = 0; g < 5; ++g) base[g + 1] = base[g] + ((tot[g] + 31) & ~31u);
    const bool fits = __builtin_amdgcn_readfirstlane((int)(tD <= (unsigned)CAP && base[5] <= (unsigned)CAP)) != 0;
    if (!fits) {
      np >>= 1;
      __syncthreads();
      continue;
    }
    if (tid < 256) {
      const unsigned pA = iA - cA + (w > 0 ? wA[0] : 0) + (w > 1 ? wA[1] : 0) + (w > 2 ? wA[2] : 0);
      const unsigned pB = iB - cB + (w > 0 ? wB[0] : 0) + (w > 1 ? wB[1] : 0) + (w > 2 ? wB[2] : 0);
      const unsigned pC = iC - cC + (w > 0 ? wC[0] : 0) + (w > 1 ? wC[1] : 0) + (w > 2 ? wC[2] : 0);
      const unsigned s0 = ent[0] & 0xffffu, s1 = ent[0] >> 16, s2 = ent[1] & 0xffffu, s3 = ent[1] >> 16, s4 = ent[2] & 0xffffu;
      const unsigned v0 = L_RAW + 8 * (base[0] + (pA & 0xffffu));
      const unsigned v1 = L_RAW + 8 * (base[1] + (pA >> 16) - (s1 - s0));
      const unsigned v2 = L_RAW + 8 * (base[2] + (pB & 0xffffu) - (s2 - s0));
      const unsigned v3 = L_RAW + 8 * (base[3] + (pB >> 16) - (s3 - s0));
      const unsigned v4 = L_RAW + 8 * (base[4] + pC - (s4 - s0));
      lds_st<u32x4>(L, L_TBL + 16 * tid, u32x4{(v0 & 0xffffu) | (v1 << 16), (v2 & 0xffffu) | (v3 << 16), v4 & 0xffffu, 0});
    } else {
      const int t = tid - 256;
      const u32x2 pad = {(unsigned)NN | ((unsigned)(L_PD + 16 * BN) << 16), 0};
      if (t < 160) {
        const int g = t >> 5, k = t & 31;
        const unsigned bg = g == 0 ? base[0] : g == 1 ? base[1] : g == 2 ? base[2] : g == 3 ? base[3] : base[4];
        const unsigned bn = g == 0 ? base[1] : g == 1 ? base[2] : g == 2 ? base[3] : g == 3 ? base[4] : base[5];
        const unsigned tg = g == 0 ? tot[0] : g == 1 ? tot[1] : g == 2 ? tot[2] : g == 3 ? tot[3] : tot[4];
        const unsigned slot = bg + tg + k;
        if (slot < bn) lds_st<u32x2>(L, L_RAW + 8 * slot, pad);
      } else if (t < 224) {
        lds_st<u32x2>(L, L_RAW + 8 * (base[5] + t - 160), pad);
      }
    }
    __syncthreads();

#pragma unroll
    for (int k = 0; k < 8; ++k) {
      const unsigned tb = L_TBL + 16 * (32 * k + (tid >> 4));
      if ((unsigned)j < cnt[k]) {
        const unsigned a = lds_ld<u16>(L, tb + 2 * ((ra[k][0] >> 23) & 7));
        lds_st<u32x2>(L, a + 8 * j, ra[k]);
      }
      if ((unsigned)j + 16 < cnt[k]) {
        const unsigned a = lds_ld<u16>(L, tb + 2 * ((rb[k][0] >> 23) & 7));
        lds_st<u32x2>(L, a + 8 * (j + 16), rb[k]);
      }
      for (unsigned jj = j + 32; jj < cnt[k]; jj += 16) {
        const int p = 32 * k + (tid >> 4);
        const u32x2 rec = *(const u32x2*)(recs + ((size_t)p * CH + (e3[k] & 0xffffu) + jj) * 8);
        const unsigned a = lds_ld<u16>(L, tb + 2 * ((rec[0] >> 23) & 7));
        lds_st<u32x2>(L, a + 8 * jj, rec);
      }
    }
    wait_vm0();
    __syncthreads();

    const int T = __builtin_amdgcn_readfirstlane((int)(base[5] >> 5));
    const int tq8 = T >> 3, trem = T & 7;
    const int ta = w * tq8 + (w < trem ? w : trem);
    const int tb = ta + tq8 + (w < trem ? 1 : 0);
    const int tg1 = __builtin_amdgcn_readfirstlane((int)(base[1] >> 5)), tg2 = __builtin_amdgcn_readfirstlane((int)(base[2] >> 5));
    const int tg3 = __builtin_amdgcn_readfirstlane((int)(base[3] >> 5)), tg4 = __builtin_amdgcn_readfirstlane((int)(base[4] >> 5));

    if (ta < tb) {
      int g = (ta >= tg1) + (ta >= tg2) + (ta >= tg3) + (ta >= tg4);
      int gend = g == 0 ? tg1 : g == 1 ? tg2 : g == 2 ? tg3 : g == 3 ? tg4 : T;
      bf16x8 bxA[2], bxB[2];
      auto load_bx = [&](int gg) {
#pragma unroll
        for (int t = 0; t < 2; ++t) {
          const u32x2 pv = lds_tr(L, p_tr + gg * 1024 + 64 * t);
          bxA[t] = as_frag(u32x4{pv[0], pv[1], hf ? 0u : cw[t][0], hf ? 0u : cw[t][1]});
          bxB[t] = as_frag(u32x4{pv[0], pv[1], hf ? cw[t][0] : 0u, hf ? cw[t][1] : 0u});
        }
      };
      auto flush = [&](int gg) {
        const unsigned a = acc_l + gg * (8 * ACC_NODE);
#pragma unroll
        for (int i = 0; i < 16; ++i) {
          const unsigned o = a + (i >> 2) * (2 * ACC_NODE) + (i & 3) * 256;
          __hip_atomic_fetch_add((AS3 float*)(L + o), acc0[i], __ATOMIC_RELAXED, __HIP_MEMORY_SCOPE_WORKGROUP);
          __hip_atomic_fetch_add((AS3 float*)(L + o + 128), acc1[i], __ATOMIC_RELAXED, __HIP_MEMORY_SCOPE_WORKGROUP);
          acc0[i] = 0.f;
          acc1[i] = 0.f;
        }
      };
      auto issue_rows = [&](int t) {
        const unsigned a = L_RAW + t * 256 + (lane >> 3) * 8;
        const unsigned i0 = lds_ld<u16>(L, a), i1 = lds_ld<u16>(L, a + 64), i2 = lds_ld<u16>(L, a + 128), i3 = lds_ld<u16>(L, a + 192);
        dma_tile(qtab, i0 * 128 + dc0, i1 * 128 + dc1, i2 * 128 + dc0, i3 * 128 + dc1, LB + ringb);
      };
      auto issue_wh = [&]() {
        asm volatile("s_waitcnt lgkmcnt(0)" ::: "memory");
        dma_piece((const char*)wh + w * 2048, lane * 16, LB + ringb);
        dma_piece((const char*)wh + w * 2048 + 1024, lane * 16, LB + ringb + 1024);
      };
      struct Raw { unsigned pda, w1, n320; u32x2 ps; };
      auto raw_load = [&](int t) {
        const unsigned a = L_RAW + t * 256 + lane * 8;
        Raw x;
        const unsigned s = lds_ld<u16>(L, a);
        x.pda = lds_ld<u16>(L, a + 2);
        x.w1 = lds_ld<unsigned>(L, a + 4);
        x.n320 = lds_ld<u16>(L, a + 4);
        x.ps = *(const u32x2*)(pos4 + s * 8u);
        return x;
      };
      auto prep = [&](const Raw& x) {
        const f32x4 pd = lds_ld<f32x4>(L, x.pda);
        const float dx = pd[0] - bf_lo(x.ps[0]), dy = pd[1] - bf_hi(x.ps[0]), dz = pd[2] - as_f(x.ps[1]);
        const unsigned wa = cvt_pk(dx, dy), wb = cvt_pk(dz, pd[3]);
        const float dzr = bf_lo(wb);
        const bf16x2v wav = __builtin_bit_cast(bf16x2v, wa);
        const float d2 = __builtin_amdgcn_fdot2_f32_bf16(wav, wav, dzr * dzr, false);
        Prep o;
        o.de = cvt_pk(d2, as_f(x.w1));
        const unsigned P = x.n320 + sa_c, Q = P + 160;
        const auto sa = __builtin_amdgcn_permlane32_swap(P, Q, false, false);
        const auto sd = __builtin_amdgcn_permlane32_swap(wa, wb, false, false);
        o.saA = sa[0]; o.saB = sa[1]; o.sdA = sd[0]; o.sdB = sd[1];
        return o;
      };
      f32x16 y0, y1;
      bf16x8 as0, as1;
      auto tile_head = [&](unsigned sa, unsigned sd, unsigned de, const bf16x8* bx) {
        const bf16x8 q0 = lds_ld<bf16x8>(L, aq[0]), q1 = lds_ld<bf16x8>(L, aq[1]), q2 = lds_ld<bf16x8>(L, aq[2]), q3 = lds_ld<bf16x8>(L, aq[3]);
        lds_st<u16>(L, sa, (u16)sd);
        lds_st<u16>(L, sa + 80, (u16)(sd >> 16));
        as0 = lds_ld<bf16x8>(L, s_rd);
        as1 = lds_ld<bf16x8>(L, s_rd + 32);
        const u32x2 oh = lds_tr(L, s_tr);
        lds_st<u16>(L, sa, (u16)0);
        lds_st<u16>(L, sa + 80, (u16)0);
        const bf16x8 ax = as_frag(u32x4{oh[0], oh[1], de, de});
        f32x16 z;
#pragma unroll
        for (int i = 0; i < 16; ++i) z[i] = 0.f;
        y0 = mfma(ax, bx[0], z);
        y1 = mfma(ax, bx[1], z);
        y0 = mfma(q0, bid[0], y0);
        y1 = mfma(q2, bid[0], y1);
        y0 = mfma(q1, bid[1], y0);
        y1 = mfma(q3, bid[1], y1);
      };
      auto tile_tail = [&]() {
        acc0 = mfma(as0, silu8(y0, 0), acc0);
        acc0 = mfma(as1, silu8(y0, 8), acc0);
        acc1 = mfma(as0, silu8(y1, 0), acc1);
        acc1 = mfma(as1, silu8(y1, 8), acc1);
      };
      auto next_group = [&](int t) {
        flush(g);
        g = (t >= tg1) + (t >= tg2) + (t >= tg3) + (t >= tg4);
        gend = g == 0 ? tg1 : g == 1 ? tg2 : g == 2 ? tg3 : g == 3 ? tg4 : T;
        load_bx(g);
      };

      load_bx(g);
      issue_rows(ta);
      Prep pr = prep(raw_load(ta));
      for (int t = ta; t < tb; t += 2) {
        if (t == gend) next_group(t);
        wait_vm0();
        tile_head(pr.saA, pr.sdA, pr.de, bxA);
        const bool hasB = t + 1 < tb;
        if (hasB) issue_rows(t + 1); else issue_wh();
        const Raw nx = raw_load(t + 2 < tb ? t + 2 : t);
        tile_tail();
        if (!hasB) break;
        if (t + 1 == gend) next_group(t + 1);
        wait_vm0();
        tile_head(pr.saB, pr.sdB, pr.de, bxB);
        pr = prep(nx);
        if (t + 2 < tb) issue_rows(t + 2); else issue_wh();
        tile_tail();
      }
      flush(g);
    } else {
      dma_piece((const char*)wh + w * 2048, lane * 16, LB + ringb);
      dma_piece((const char*)wh + w * 2048 + 1024, lane * 16, LB + ringb + 1024);
    }
    pa += np;
    if (pa >= NP) break;
    wait_vm0();
    __syncthreads();
  }
  wait_vm0();
  __syncthreads();

  if (w < 4) {
    const int u = w >> 1, t = w & 1;
    const int nl = 32 * u + r, nlc = nl < BN ? nl : BN - 1;
    const u16* hrow = h + (size_t)(n0 + nlc) * 64;
    f32x16 d;
#pragma unroll
    for (int i = 0; i < 16; ++i) d[i] = 0.f;
    const unsigned w_tr = L_RING + (8 * th + tq) * 128 + (32 * t + 16 * tu + 4 * tp) * 2;
#pragma unroll
    for (int s = 0; s < 8; ++s) {
      const u32x2 a0 = lds_tr(L, w_tr + s * 4096), a1 = lds_tr(L, w_tr + s * 4096 + 512);
      const bf16x8 a = as_frag(u32x4{a0[0], a0[1], a1[0], a1[1]});
      bf16x8 bb;
      if (s < 4) {
        bb = *(const bf16x8*)(hrow + 16 * s + 8 * hf);
      } else {
        const unsigned o = L_ACC + nlc * ACC_NODE + 3 * 256 + (16 * (s - 4) + 8 * hf) * 4;
        const f32x4 m0 = lds_ld<f32x4>(L, o) * NLN2, m1 = lds_ld<f32x4>(L, o + 16) * NLN2;
        bb = as_frag(u32x4{cvt_pk(m0[0], m0[1]), cvt_pk(m0[2], m0[3]), cvt_pk(m1[0], m1[1]), cvt_pk(m1[2], m1[3])});
      }
      d = mfma(a, bb, d);
    }
    if (nl < BN) {
#pragma unroll
      for (int q = 0; q < 4; ++q) {
        const int f = 32 * t + 8 * q + 4 * hf;
        const u32x2 hv = *(const u32x2*)(hrow + f);
        float o4[4];
#pragma unroll
        for (int k = 0; k < 4; ++k) {
          const float act = bf_round(silu_f(bf_round(d[4 * q + k])));
          const float hres = (k & 1) ? bf_hi(hv[k >> 1]) : bf_lo(hv[k >> 1]);
          o4[k] = hres + act;
        }
        *(u32x2*)(out + (size_t)(n0 + nl) * 64 + f) = u32x2{cvt_pk(o4[0], o4[1]), cvt_pk(o4[2], o4[3])};
      }
    }
  } else {
    const int idx = tid - 256, nc = idx >> 1, half = idx & 1;
    const int ncc = nc < 3 * BN ? nc : 3 * BN - 1;
    const int n = ncc / 3, c = ncc - 3 * n;
    const unsigned o = L_ACC + n * ACC_NODE + c * 256 + half * 128;
    float s = 0.f;
#pragma unroll
    for (int k = 0; k < 8; ++k) {
      const f32x4 v = lds_ld<f32x4>(L, o + 16 * k), x = lds_ld<f32x4>(L, L_WX + half * 128 + 16 * k);
      s += v[0] * x[0] + v[1] * x[1] + v[2] * x[2] + v[3] * x[3];
    }
    s += __builtin_bit_cast(float, __builtin_amdgcn_update_dpp(0, __builtin_bit_cast(int, s), 0xb1, 0xf, 0xf, true));
    if (nc < 3 * BN && half == 0) {
      const float p0 = lds_ld<float>(L, L_PD + 16 * n + 4 * c);
      out[(size_t)NN * 64 + (size_t)n0 * 3 + nc] = (u16)cvt_pk(p0 + bf_round(s * NLN2), 0.f);
    }
  }
}

}

extern "C" void kernel_launch(void* const* d_in, const int* in_sizes, int n_in, void* d_out, int out_size, void* d_ws, size_t ws_size,
                              hipStream_t stream) {
  const u16* h = (const u16*)d_in[0];
  const u16* pos = (const u16*)d_in[1];
  const u16* ea = (const u16*)d_in[2];
  const int* src = (const int*)d_in[3];
  const int* dst = (const int*)d_in[4];
  const u16* we = (const u16*)d_in[5];
  const u16* wx = (const u16*)d_in[6];
  const u16* wh = (const u16*)d_in[7];
  sort_kernel<<<NP + K1_TABLE_BLOCKS, K1_THREADS, 0, stream>>>(src, dst, ea, h, pos, we, (char*)d_ws);
  edge_kernel<<<NBK, 512, 0, stream>>>(h, pos, we, wx, wh, (const char*)d_ws, (u16*)d_out);
}
```

```cpp
#include <hip/hip_runtime.h>
#include <hip/hip_bf16.h>
#include <stdint.h>

#define DI __device__ __forceinline__
#define AS3 __attribute__((address_space(3)))
#define SB() __builtin_amdgcn_sched_barrier(0)

typedef short bf16x8 __attribute__((ext_vector_type(8)));
typedef short s16x4 __attribute__((ext_vector_type(4)));
typedef float f32x16 __attribute__((ext_vector_type(16)));
typedef float f32x4 __attribute__((ext_vector_type(4)));
typedef float f32x2 __attribute__((ext_vector_type(2)));
typedef unsigned u32x4 __attribute__((ext_vector_type(4)));
typedef unsigned u32x2 __attribute__((ext_vector_type(2)));
typedef int i32x4 __attribute__((ext_vector_type(4)));
typedef __bf16 bf16x2v __attribute__((ext_vector_type(2)));
typedef unsigned short u16;

namespace {

constexpr int NN = 10000;
constexpr int NE = 640000;
constexpr int NP = 256;
constexpr int CH = NE / NP;
constexpr int BN = 40;
constexpr int NBK = NN / BN;
constexpr int NTILE = (NN + 1 + 31) / 32;
constexpr int K1_THREADS = 640;
constexpr int K1_TABLE_BLOCKS = (NTILE * 2 + K1_THREADS / 64 - 1) / (K1_THREADS / 64);
constexpr float NLOG2E = -1.4426950408889634f;
constexpr float NLN2 = -0.6931471805599453f;

constexpr size_t WS_RECS = 0;
constexpr size_t WS_ENT = WS_RECS + (size_t)NP * CH * 8;
constexpr size_t WS_QTAB = WS_ENT + (size_t)NP * 256 * 16;
constexpr size_t WS_POS4 = WS_QTAB + (size_t)(NN + 2) * 128;

constexpr int CAP = 2880;
constexpr int L_S = 0;
constexpr int S_WAVE = 2560;
constexpr int L_PD = 20480;
constexpr int L_WT = 21248;
constexpr int L_LOCK = 21312;
constexpr int L_WX = 21504;
constexpr int L_TBL = 21760;
constexpr int L_RAW = L_TBL + 4096;
constexpr int L_PFR = L_RAW + (CAP + 96) * 8;
constexpr int L_RING = L_PFR + 5120;
constexpr int L_ACC = L_RING + 8 * 4096;
constexpr int ACC_NODE = 1040;
constexpr int L_END = L_ACC + BN * ACC_NODE;
static_assert(L_PD % 1024 == 0, "group id is read from bits 7..9 of the pos address");
static_assert(L_RAW + (CAP + 96) * 8 < 65536 && L_RAW >= 8 * CH, "run table holds 16-bit LDS addresses");
static_assert(L_PFR % 16 == 0 && L_RING % 16 == 0 && L_ACC % 16 == 0 && L_END <= 163840, "LDS map");

DI unsigned cvt_pk(float a, float b) { f32x2 v = {a, b}; return __builtin_bit_cast(unsigned, __builtin_convertvector(v, bf16x2v)); }
DI float as_f(unsigned u) { return __builtin_bit_cast(float, u); }
DI float bf_lo(unsigned u) { return __builtin_bit_cast(float, u << 16); }
DI float bf_hi(unsigned u) { return __builtin_bit_cast(float, u & 0xffff0000u); }
DI float bf_round(float x) { return bf_lo(cvt_pk(x, x)); }
DI float bf_ld(const u16* p) { return __builtin_bit_cast(float, (unsigned)*p << 16); }
DI f32x16 mfma(bf16x8 a, bf16x8 b, f32x16 c) { return __builtin_amdgcn_mfma_f32_32x32x16_bf16(a, b, c, 0, 0, 0); }
DI bf16x8 as_frag(u32x4 v) { return __builtin_bit_cast(bf16x8, v); }

DI unsigned wave_incl_scan(unsigned x) {
  x += __builtin_amdgcn_update_dpp(0, (int)x, 0x111, 0xf, 0xf, true);
  x += __builtin_amdgcn_update_dpp(0, (int)x, 0x112, 0xf, 0xf, true);
  x += __builtin_amdgcn_update_dpp(0, (int)x, 0x114, 0xf, 0xf, true);
  x += __builtin_amdgcn_update_dpp(0, (int)x, 0x118, 0xf, 0xf, true);
  x += __builtin_amdgcn_update_dpp(0, (int)x, 0x142, 0xa, 0xf, false);
  x += __builtin_amdgcn_update_dpp(0, (int)x, 0x143, 0xc, 0xf, false);
  return x;
}

template <typename T> DI T lds_ld(AS3 char* L, unsigned off) { return *(AS3 T*)(L + off); }
template <typename T> DI void lds_st(AS3 char* L, unsigned off, T v) { *(AS3 T*)(L + off) = v; }
DI u32x2 lds_tr(AS3 char* L, unsigned off) {
  return __builtin_bit_cast(u32x2, __builtin_amdgcn_ds_read_tr16_b64_v4i16((AS3 s16x4*)(L + off)));
}

DI const char* uni_ptr(const char* p) {
  const unsigned long long v = (unsigned long long)p;
  const unsigned lo = __builtin_amdgcn_readfirstlane((int)(unsigned)v), hi = __builtin_amdgcn_readfirstlane((int)(unsigned)(v >> 32));
  return (const char*)(((unsigned long long)hi << 32) | lo);
}
DI void dma_piece(const char* base_, unsigned voff, unsigned ldsaddr_) {
  const char* base = uni_ptr(base_);
  const unsigned ldsaddr = __builtin_amdgcn_readfirstlane((int)ldsaddr_);
  unsigned keep;
  asm volatile("s_mov_b32 %0, m0\n\ts_mov_b32 m0, %3\n\ts_nop 0\n\tglobal_load_lds_dwordx4 %1, %2\n\ts_mov_b32 m0, %0"
               : "=&s"(keep) : "v"(voff), "s"(base), "s"(ldsaddr) : "memory");
}
DI void set_m0(unsigned ldsaddr) { asm volatile("s_mov_b32 m0, %0\n\ts_nop 0" :: "s"(ldsaddr) : "memory"); }
DI void dma_tile(const char* base, unsigned v0, unsigned v1, unsigned v2, unsigned v3) {
  asm volatile("global_load_lds_dwordx4 %0, %4\n\t"
               "global_load_lds_dwordx4 %1, %5 offset:1024\n\t"
               "global_load_lds_dwordx4 %2, %6 offset:2048\n\t"
               "global_load_lds_dwordx4 %3, %7 offset:3072"
               :: "v"(v0), "v"(v1), "v"(v2), "v"(v3), "s"(base), "s"(base - 1024), "s"(base - 2048), "s"(base - 3072)
               : "memory");
}
#define STAMP(k, kern)
#define CSTAMP(k, kern)
DI void st16_wt(void* p, u32x4 v) {
  *(u32x4*)p = v;
}
DI void wait_vm0() { asm volatile("s_waitcnt vmcnt(0)" ::: "memory"); }

DI bf16x8 w_frag(const u16* __restrict__ we, int k0, int f) {
  bf16x8 x;
#pragma unroll
  for (int j = 0; j < 8; ++j) x[j] = (short)we[(k0 + j) * 64 + f];
  return x;
}

DI void table_task(int task, int lane, const u16* __restrict__ h, const u16* __restrict__ pos, const u16* __restrict__ we, char* ws) {
  const int T = task >> 1, t = task & 1;
  const int r = lane & 31, hh = lane >> 5;
  const int node = 32 * T + r, nodec = node < NN ? node : NN - 1;
  f32x16 acc;
#pragma unroll
  for (int i = 0; i < 16; ++i) acc[i] = 0.f;
  bf16x8 hb[4], wa[4];
#pragma unroll
  for (int s = 0; s < 4; ++s) hb[s] = *(const bf16x8*)(h + nodec * 64 + 16 * s + 8 * hh);
#pragma unroll
  for (int s = 0; s < 4; ++s) wa[s] = w_frag(we, 64 + 16 * s + 8 * hh, 32 * t + r);
  u16 px = 0, py = 0, pz = 0;
  const bool do_pos = (t == 0 && hh == 0 && node <= NN);
  if (do_pos && node < NN) { px = pos[node * 3]; py = pos[node * 3 + 1]; pz = pos[node * 3 + 2]; }
#pragma unroll
  for (int s = 0; s < 4; ++s) acc = mfma(wa[s], hb[s], acc);
  u32x4 o0, o1;
#pragma unroll
  for (int i = 0; i < 4; ++i) {
    o0[i] = cvt_pk(acc[2 * i] * NLOG2E, acc[2 * i + 1] * NLOG2E);
    o1[i] = cvt_pk(acc[8 + 2 * i] * NLOG2E, acc[8 + 2 * i + 1] * NLOG2E);
  }
  if (node == NN) { o0 = u32x4{0, 0, 0, 0}; o1 = o0; }
  if (node <= NN) {
    u32x4* dstp = (u32x4*)(ws + WS_QTAB + (size_t)node * 128 + 64 * t + 32 * hh);
    st16_wt(dstp, o0);
    st16_wt(dstp + 1, o1);
  }
  if (do_pos) *(u32x2*)(ws + WS_POS4 + (size_t)node * 8) = u32x2{(unsigned)px | ((unsigned)py << 16), (unsigned)pz << 16};
}

__global__ __launch_bounds__(K1_THREADS) void sort_kernel(const int* __restrict__ src, const int* __restrict__ dst,
                                                          const u16* __restrict__ ea, const u16* __restrict__ h,
                                                          const u16* __restrict__ pos, const u16* __restrict__ we, char* ws) {
  __shared__ __attribute__((aligned(16))) char lds[4096 + 64 + CH * 8];
  AS3 char* L = (AS3 char*)lds;
  constexpr int C_CNT = 0, C_WT = 4096, C_SORT = 4160;
  const int tid = threadIdx.x, lane = tid & 63;
  const int w = __builtin_amdgcn_readfirstlane(tid >> 6);
  if (blockIdx.x >= NP) {
    const int task = (blockIdx.x - NP) * (K1_THREADS / 64) + w;
    STAMP(8, 0);
    if (task < NTILE * 2) table_task(task, lane, h, pos, we, ws);
    STAMP(9, 0);
    return;
  }
  const int p = blockIdx.x;
  STAMP(0, 0);
  const bool act = tid < CH / 4;
  i32x4 d4 = {0, 0, 0, 0}, s4 = {0, 0, 0, 0};
  u32x2 a2 = {0, 0};
  if (act) {
    const int e0 = p * CH + 4 * tid;
    d4 = *(const i32x4*)(dst + e0);
    s4 = *(const i32x4*)(src + e0);
    a2 = *(const u32x2*)(ea + e0);
  }
  if (tid < 256) lds_st<u32x4>(L, C_CNT + 16 * tid, u32x4{0, 0, 0, 0});
  __syncthreads();
  STAMP(1, 0);
  unsigned idx[4], rank[4], bk[4];
#pragma unroll
  for (int i = 0; i < 4; ++i) {
    const unsigned G = (unsigned)d4[i] >> 3;
    bk[i] = (G * 52429u) >> 18;
    idx[i] = G + 3 * bk[i];
    rank[i] = 0;
    if (act) {
      const unsigned sh = (idx[i] & 1) << 4;
      const unsigned old = __hip_atomic_fetch_add((AS3 unsigned*)(L + C_CNT + 4 * (idx[i] >> 1)), 1u << sh, __ATOMIC_RELAXED,
                                                  __HIP_MEMORY_SCOPE_WORKGROUP);
      rank[i] = (old >> sh) & 0xffffu;
    }
  }
  STAMP(2, 0);
  __syncthreads();
  STAMP(3, 0);
  u32x4 c = {0, 0, 0, 0};
  unsigned tot = 0, incl = 0;
  if (tid < 256) {
    c = lds_ld<u32x4>(L, C_CNT + 16 * tid);
    tot = (c[0] & 0xffffu) + (c[0] >> 16) + (c[1] & 0xffffu) + (c[1] >> 16) + (c[2] & 0xffffu);
    incl = wave_incl_scan(tot);
    if (lane == 63) lds_st<unsigned>(L, C_WT + 4 * w, incl);
  }
  __syncthreads();
  if (tid < 256) {
    const u32x4 wt = lds_ld<u32x4>(L, C_WT);
    const unsigned s0 = incl - tot + (w > 0 ? wt[0] : 0) + (w > 1 ? wt[1] : 0) + (w > 2 ? wt[2] : 0);
    const unsigned s1 = s0 + (c[0] & 0xffffu), s2 = s1 + (c[0] >> 16), s3 = s2 + (c[1] & 0xffffu), s4_ = s3 + (c[1] >> 16);
    const unsigned s5 = s0 + tot;
    const u32x4 e = {s0 | (s1 << 16), s2 | (s3 << 16), s4_ | (s5 << 16), s0 | (tot << 16)};
    lds_st<u32x4>(L, C_CNT + 16 * tid, e);
    st16_wt(ws + WS_ENT + ((size_t)p * 256 + tid) * 16, e);
  }
  __syncthreads();
  STAMP(4, 0);
  if (act) {
    unsigned ps[4];
#pragma unroll
    for (int i = 0; i < 4; ++i) ps[i] = lds_ld<u16>(L, C_CNT + 2 * idx[i]) + rank[i];
#pragma unroll
    for (int i = 0; i < 4; ++i) {
      const unsigned d = (unsigned)d4[i], dl = d - BN * bk[i];
      const unsigned eav = (i & 1) ? (a2[i >> 1] & 0xffff0000u) : (a2[i >> 1] << 16);
      const u32x2 rec = {(unsigned)s4[i] | ((L_PD + 16 * dl) << 16), ((d & 7) * 320) | eav};
      lds_st<u32x2>(L, C_SORT + 8 * ps[i], rec);
    }
  }
  __syncthreads();
  STAMP(5, 0);
  char* out = ws + WS_RECS + (size_t)p * (CH * 8);
#pragma unroll
  for (int i = 0; i < 2; ++i) {
    const int k = tid + i * K1_THREADS;
    if (k < CH / 2) st16_wt(out + 16 * k, lds_ld<u32x4>(L, C_SORT + 16 * k));
  }
  STAMP(6, 0);
  STAMP(7, 0);
}

struct Prep {
  unsigned sdA, sdB;
  unsigned saA, saB;
  unsigned de;
};
struct Raw { unsigned pda, w1, n320; u32x2 ps; };

DI float silu_f(float x) { return x * __builtin_amdgcn_rcpf(1.0f + __builtin_amdgcn_exp2f(x * NLOG2E)); }

#pragma clang diagnostic ignored "-Winline-asm"
#define T4(op, d, s0, s1, s2, s3, d1, d2, d3) op " v" #d ", v" #s0 "\n\t" op " v" #d1 ", v" #s1 "\n\t" op " v" #d2 ", v" #s2 "\n\t" op " v" #d3 ", v" #s3 "\n\t"
#define EXP_LO(a, b, c, d) T4("v_exp_f32", 184, a, b, c, d, 185, 186, 187)
#define EXP_HI(a, b, c, d) T4("v_exp_f32", 188, a, b, c, d, 189, 190, 191)
#define RCP_LO T4("v_rcp_f32", 184, 184, 185, 186, 187, 185, 186, 187)
#define RCP_HI T4("v_rcp_f32", 188, 188, 189, 190, 191, 189, 190, 191)
#define PKA(a, b) "v_pk_add_f32 v[" #a ":" #b "], v[" #a ":" #b "], 1.0 op_sel_hi:[1,0]\n\t"
#define ADD1 PKA(184, 185) PKA(186, 187) PKA(188, 189) PKA(190, 191)
#define PKM(a, b, c, d) "v_pk_mul_f32 v[" #a ":" #b "], v[" #a ":" #b "], v[" #c ":" #d "]\n\t"
#define MULY(a0, a1, a2, a3, a4, a5, a6, a7) PKM(184, 185, a0, a1) PKM(186, 187, a2, a3) PKM(188, 189, a4, a5) PKM(190, 191, a6, a7)
#define CVT(d, a, b) "v_cvt_pk_bf16_f32 v" #d ", v" #a ", v" #b "\n\t"
#define PACK(d0, d1, d2, d3) CVT(d0, 184, 185) CVT(d1, 186, 187) CVT(d2, 188, 189) CVT(d3, 190, 191)
#define MF(acc, a, b) "v_mfma_f32_32x32x16_bf16 " acc ", " a ", " b ", " acc "\n\t"
#define MF0(acc, a, b) "v_mfma_f32_32x32x16_bf16 " acc ", " a ", " b ", 0\n\t"
#define Y0 "v[192:207]"
#define Y1 "v[208:223]"
#define ACC0 "v[224:239]"
#define ACC1 "v[240:255]"
#define X0 "v[176:179]"
#define X1 "v[180:183]"

DI void tiles_init() {
  asm volatile(
               "v_mov_b64 v[224:225], 0\n\t"
               "v_mov_b64 v[226:227], 0\n\t"
               "v_mov_b64 v[228:229], 0\n\t"
               "v_mov_b64 v[230:231], 0\n\t"
               "v_mov_b64 v[232:233], 0\n\t"
               "v_mov_b64 v[234:235], 0\n\t"
               "v_mov_b64 v[236:237], 0\n\t"
               "v_mov_b64 v[238:239], 0\n\t"
               "v_mov_b64 v[240:241], 0\n\t"
               "v_mov_b64 v[242:243], 0\n\t"
               "v_mov_b64 v[244:245], 0\n\t"
               "v_mov_b64 v[246:247], 0\n\t"
               "v_mov_b64 v[248:249], 0\n\t"
               "v_mov_b64 v[250:251], 0\n\t"
               "v_mov_b64 v[252:253], 0\n\t"
               "v_mov_b64 v[254:255], 0\n\t"
               "v_mov_b64 v[180:181], 0\n\t"
               "v_mov_b64 v[182:183], 0\n\t"
               "s_nop 1" ::: "v176", "v255");
}
DI void alpha1(const bf16x8& asl) {
  asm volatile(EXP_LO(192, 193, 194, 195) MF(ACC1, "%[asl]", X1) EXP_HI(196, 197, 198, 199) :: [asl] "v"(asl));
}
DI void alpha2(const bf16x8& ax, const bf16x8& bx, const bf16x8& q2, const bf16x8& i0) {
  asm volatile(ADD1 MF0(Y1, "%[ax]", "%[bx]") RCP_LO MF(Y1, "%[q]", "%[i]") RCP_HI
               MULY(192, 193, 194, 195, 196, 197, 198, 199) PACK(176, 177, 178, 179)
               :: [ax] "v"(ax), [bx] "v"(bx), [q] "v"(q2), [i] "v"(i0));
}
DI void alpha3(const bf16x8& q3, const bf16x8& i1, const bf16x8& as0) {
  asm volatile(EXP_LO(200, 201, 202, 203) MF(Y1, "%[q]", "%[i]") EXP_HI(204, 205, 206, 207) ADD1 MF(ACC0, "%[as]", X0) RCP_LO RCP_HI
               MULY(200, 201, 202, 203, 204, 205, 206, 207) PACK(180, 181, 182, 183)
               :: [q] "v"(q3), [i] "v"(i1), [as] "v"(as0));
}
DI void beta1(const bf16x8& as1) {
  asm volatile(EXP_LO(208, 209, 210, 211) MF(ACC0, "%[as]", X1) EXP_HI(212, 213, 214, 215) ADD1 :: [as] "v"(as1));
}
DI void beta2(const bf16x8& ax, const bf16x8& bx, const bf16x8& q0, const bf16x8& i0) {
  asm volatile(MF0(Y0, "%[ax]", "%[bx]") RCP_LO MF(Y0, "%[q]", "%[i]") RCP_HI
               MULY(208, 209, 210, 211, 212, 213, 214, 215) PACK(176, 177, 178, 179)
               :: [ax] "v"(ax), [bx] "v"(bx), [q] "v"(q0), [i] "v"(i0));
}
DI void beta3(const bf16x8& q1, const bf16x8& i1, const bf16x8& as0) {
  asm volatile(EXP_LO(216, 217, 218, 219) MF(Y0, "%[q]", "%[i]") EXP_HI(220, 221, 222, 223) ADD1 MF(ACC1, "%[as]", X0) RCP_LO RCP_HI
               MULY(216, 217, 218, 219, 220, 221, 222, 223) PACK(180, 181, 182, 183)
               :: [q] "v"(q1), [i] "v"(i1), [as] "v"(as0));
}
DI void chain_y0(const bf16x8& ax, const bf16x8& bx, const bf16x8& q0, const bf16x8& q1, const bf16x8& i0, const bf16x8& i1) {
  asm volatile("s_nop 1\n\t" MF0(Y0, "%[ax]", "%[bx]") MF(Y0, "%[q0]", "%[i0]") MF(Y0, "%[q1]", "%[i1]") "s_nop 15"
               :: [ax] "v"(ax), [bx] "v"(bx), [q0] "v"(q0), [i0] "v"(i0), [q1] "v"(q1), [i1] "v"(i1));
}
DI void last_block(const bf16x8& as1) { asm volatile("s_nop 1\n\t" MF(ACC1, "%[as]", X1) :: [as] "v"(as1)); }
DI void acc_gather(unsigned a0, unsigned a1, unsigned a2, unsigned a3) {
  f32x2 t0, t1, t2, t3, t4, t5, t6, t7;
  asm volatile(
               "ds_read2_b32 %[t0], %[a0] offset0:0 offset1:64\n\t"
               "ds_read2_b32 %[t1], %[a0] offset0:128 offset1:192\n\t"
               "ds_read2_b32 %[t2], %[a0] offset0:32 offset1:96\n\t"
               "ds_read2_b32 %[t3], %[a0] offset0:160 offset1:224\n\t"
               "ds_read2_b32 %[t4], %[a1] offset0:0 offset1:64\n\t"
               "ds_read2_b32 %[t5], %[a1] offset0:128 offset1:192\n\t"
               "ds_read2_b32 %[t6], %[a1] offset0:32 offset1:96\n\t"
               "ds_read2_b32 %[t7], %[a1] offset0:160 offset1:224\n\t"
               "s_waitcnt lgkmcnt(4)\n\t"
               "v_pk_add_f32 v[224:225], v[224:225], %[t0]\n\t"
               "v_pk_add_f32 v[226:227], v[226:227], %[t1]\n\t"
               "v_pk_add_f32 v[240:241], v[240:241], %[t2]\n\t"
               "v_pk_add_f32 v[242:243], v[242:243], %[t3]\n\t"
               "s_waitcnt lgkmcnt(0)\n\t"
               "v_pk_add_f32 v[228:229], v[228:229], %[t4]\n\t"
               "v_pk_add_f32 v[230:231], v[230:231], %[t5]\n\t"
               "v_pk_add_f32 v[244:245], v[244:245], %[t6]\n\t"
               "v_pk_add_f32 v[246:247], v[246:247], %[t7]\n\t"
               "ds_read2_b32 %[t0], %[a2] offset0:0 offset1:64\n\t"
               "ds_read2_b32 %[t1], %[a2] offset0:128 offset1:192\n\t"
               "ds_read2_b32 %[t2], %[a2] offset0:32 offset1:96\n\t"
               "ds_read2_b32 %[t3], %[a2] offset0:160 offset1:224\n\t"
               "ds_read2_b32 %[t4], %[a3] offset0:0 offset1:64\n\t"
               "ds_read2_b32 %[t5], %[a3] offset0:128 offset1:192\n\t"
               "ds_read2_b32 %[t6], %[a3] offset0:32 offset1:96\n\t"
               "ds_read2_b32 %[t7], %[a3] offset0:160 offset1:224\n\t"
               "s_waitcnt lgkmcnt(4)\n\t"
               "v_pk_add_f32 v[232:233], v[232:233], %[t0]\n\t"
               "v_pk_add_f32 v[234:235], v[234:235], %[t1]\n\t"
               "v_pk_add_f32 v[248:249], v[248:249], %[t2]\n\t"
               "v_pk_add_f32 v[250:251], v[250:251], %[t3]\n\t"
               "s_waitcnt lgkmcnt(0)\n\t"
               "v_pk_add_f32 v[236:237], v[236:237], %[t4]\n\t"
               "v_pk_add_f32 v[238:239], v[238:239], %[t5]\n\t"
               "v_pk_add_f32 v[252:253], v[252:253], %[t6]\n\t"
               "v_pk_add_f32 v[254:255], v[254:255], %[t7]\n\t"
               "s_nop 0"
               : [t0] "=&v"(t0), [t1] "=&v"(t1), [t2] "=&v"(t2), [t3] "=&v"(t3), [t4] "=&v"(t4), [t5] "=&v"(t5), [t6] "=&v"(t6), [t7] "=&v"(t7)
               : [a0] "v"(a0), [a1] "v"(a1), [a2] "v"(a2), [a3] "v"(a3) : "memory");
}
DI void acc_store(unsigned a0, unsigned a1, unsigned a2, unsigned a3) {
  asm volatile(
               "ds_write2_b32 %[a0], v224, v225 offset0:0 offset1:64\n\t"
               "ds_write2_b32 %[a0], v226, v227 offset0:128 offset1:192\n\t"
               "ds_write2_b32 %[a0], v240, v241 offset0:32 offset1:96\n\t"
               "ds_write2_b32 %[a0], v242, v243 offset0:160 offset1:224\n\t"
               "ds_write2_b32 %[a1], v228, v229 offset0:0 offset1:64\n\t"
               "ds_write2_b32 %[a1], v230, v231 offset0:128 offset1:192\n\t"
               "ds_write2_b32 %[a1], v244, v245 offset0:32 offset1:96\n\t"
               "ds_write2_b32 %[a1], v246, v247 offset0:160 offset1:224\n\t"
               "ds_write2_b32 %[a2], v232, v233 offset0:0 offset1:64\n\t"
               "ds_write2_b32 %[a2], v234, v235 offset0:128 offset1:192\n\t"
               "ds_write2_b32 %[a2], v248, v249 offset0:32 offset1:96\n\t"
               "ds_write2_b32 %[a2], v250, v251 offset0:160 offset1:224\n\t"
               "ds_write2_b32 %[a3], v236, v237 offset0:0 offset1:64\n\t"
               "ds_write2_b32 %[a3], v238, v239 offset0:128 offset1:192\n\t"
               "ds_write2_b32 %[a3], v252, v253 offset0:32 offset1:96\n\t"
               "ds_write2_b32 %[a3], v254, v255 offset0:160 offset1:224\n\t"
               "s_waitcnt lgkmcnt(0)\n\t"
               "v_mov_b64 v[224:225], 0\n\t"
               "v_mov_b64 v[226:227], 0\n\t"
               "v_mov_b64 v[228:229], 0\n\t"
               "v_mov_b64 v[230:231], 0\n\t"
               "v_mov_b64 v[232:233], 0\n\t"
               "v_mov_b64 v[234:235], 0\n\t"
               "v_mov_b64 v[236:237], 0\n\t"
               "v_mov_b64 v[238:239], 0\n\t"
               "v_mov_b64 v[240:241], 0\n\t"
               "v_mov_b64 v[242:243], 0\n\t"
               "v_mov_b64 v[244:245], 0\n\t"
               "v_mov_b64 v[246:247], 0\n\t"
               "v_mov_b64 v[248:249], 0\n\t"
               "v_mov_b64 v[250:251], 0\n\t"
               "v_mov_b64 v[252:253], 0\n\t"
               "v_mov_b64 v[254:255], 0\n\t"
               "s_nop 7"
               :: [a0] "v"(a0), [a1] "v"(a1), [a2] "v"(a2), [a3] "v"(a3) : "memory");
}
DI unsigned lds_lock(unsigned addr) {
  unsigned t, old;
  unsigned long long sv;
  asm volatile("s_mov_b64 %2, exec\n\ts_mov_b64 exec, 1\n"
               "1:\n\tds_wrxchg_rtn_b32 %0, %3, %4\n\ts_waitcnt lgkmcnt(0)\n\tv_cmp_eq_u32 vcc, 1, %0\n\ts_cbranch_vccz 2f\n\t"
               "s_sleep 1\n\ts_branch 1b\n"
               "2:\n\tv_readfirstlane_b32 %1, %0\n\ts_mov_b64 exec, %2\n\ts_nop 15"
               : "=&v"(t), "=&s"(old), "=&s"(sv) : "v"(addr), "v"(1u) : "vcc", "memory");
  return old;
}

template <typename T> DI T* karg(int k) {
  typedef __attribute__((address_space(4))) const unsigned long long* kp_t;
  kp_t kp = (kp_t)__builtin_amdgcn_kernarg_segment_ptr();
  asm volatile("" : "+s"(kp));
  return (T*)kp[k];
}

__global__ __launch_bounds__(512) __attribute__((amdgpu_num_vgpr(88))) void edge_kernel(const u16* __restrict__ h_, const u16* __restrict__ pos_, const u16* __restrict__ we_,
                                                   const u16* __restrict__ wx_, const u16* __restrict__ wh_,
                                                   const char* __restrict__ ws_, u16* __restrict__ out_) {
  __shared__ __attribute__((aligned(1024))) char lds[L_END];
  AS3 char* L = (AS3 char*)lds;
  const unsigned LB = (unsigned)(size_t)L;
  const int tid_ = threadIdx.x, lane = tid_ & 63;
  const int w = __builtin_amdgcn_readfirstlane(tid_ >> 6);
  const int r = lane & 31, hf = lane >> 5;
  STAMP(0, 1);
  const int xl = blockIdx.x & 7;
  const int b = xl * 31 + (xl < 2 ? xl : 2) + (blockIdx.x >> 3);
  const int n0 = b * BN;

  unsigned cw[2][2];

  int pa = 0, np = NP;
  bool staged = false;
  for (;;) {
    const u16 *h = karg<const u16>(0), *pos = karg<const u16>(1), *we = karg<const u16>(2), *wx = karg<const u16>(3);
    const char* ws = karg<const char>(5);
    const char *recs = ws + WS_RECS, *ents = ws + WS_ENT + (size_t)b * 16;
    int tid = tid_;
    asm volatile("" : "+v"(tid));
    if (!staged) dma_piece((const char*)we + w * 1024, lane * 16, LB + L_RING + w * 1024);
    unsigned e3[4];
#pragma unroll
    for (int k = 0; k < 4; ++k) e3[k] = *(const unsigned*)(ents + (size_t)(64 * k + (tid >> 3)) * 4096 + 12);
    u32x4 ent = {0, 0, 0, 0};
    if (w < 4) ent = *(const u32x4*)(ents + (size_t)tid * 4096);

    if (!staged) {
      for (int o = tid * 16; o < 8 * S_WAVE; o += 512 * 16) lds_st<u32x4>(L, L_S + o, u32x4{0, 0, 0, 0});
      for (int o = tid * 16; o < BN * ACC_NODE; o += 512 * 16) lds_st<u32x4>(L, L_ACC + o, u32x4{0, 0, 0, 0});
      if (tid < 8) lds_st<unsigned>(L, L_LOCK + 4 * tid, 0u);
    }
    SB();
    STAMP(1, 1);

    const unsigned j2 = 2 * (tid & 7);
    u32x4 ra[4], rb[4];
    unsigned cnt[4];
#pragma unroll
    for (int k = 0; k < 4; ++k) {
      const int p = 64 * k + (tid >> 3);
      cnt[k] = ((unsigned)(p - pa) < (unsigned)np) ? (e3[k] >> 16) : 0;
      ra[k] = u32x4{0, 0, 0, 0};
      if (j2 < cnt[k]) ra[k] = *(const u32x4*)(recs + ((size_t)p * CH + (e3[k] & 0xffffu) + j2) * 8);
    }
#pragma unroll
    for (int k = 0; k < 4; ++k) {
      const int p = 64 * k + (tid >> 3);
      rb[k] = u32x4{0, 0, 0, 0};
      if (j2 + 16 < cnt[k]) rb[k] = *(const u32x4*)(recs + ((size_t)p * CH + (e3[k] & 0xffffu) + j2 + 16) * 8);
    }
    SB();
    bf16x8 ha[4];
    unsigned sraw[3] = {0, 0, 0}, craw[4];
    if (!staged) {
      if (w >= 4) {
        const int u = (w - 4) >> 1, t = w & 1;
        const int nl = 32 * u + r, nlc = nl < BN ? nl : BN - 1;
#pragma unroll
        for (int s = 0; s < 4; ++s) ha[s] = *(const bf16x8*)(h + (size_t)(n0 + nlc) * 64 + 16 * s + 8 * hf);
      } else if (w == 0) {
        const u16* pp = pos + (n0 + (lane < BN ? lane : 0)) * 3;
        sraw[0] = pp[0]; sraw[1] = pp[1]; sraw[2] = pp[2];
      } else if (w == 1) {
        sraw[0] = wx[lane];
      }
#pragma unroll
      for (int t = 0; t < 2; ++t) { craw[2 * t] = we[128 * 64 + 32 * t + r]; craw[2 * t + 1] = we[129 * 64 + 32 * t + r]; }
    }
    SB();
    STAMP(2, 1);

    unsigned cA = 0, cB = 0, cC = 0, cD = 0, iA = 0, iB = 0, iC = 0, iD = 0;
    if (w < 4) {
      if ((unsigned)(tid - pa) < (unsigned)np) {
        const unsigned s0 = ent[0] & 0xffffu, s1 = ent[0] >> 16, s2 = ent[1] & 0xffffu, s3 = ent[1] >> 16;
        const unsigned s4 = ent[2] & 0xffffu, s5 = ent[2] >> 16;
        cA = (s1 - s0) | ((s2 - s1) << 16);
        cB = (s3 - s2) | ((s4 - s3) << 16);
        cC = s5 - s4;
        cD = s5 - s0;
      }
      iA = wave_incl_scan(cA); iB = wave_incl_scan(cB); iC = wave_incl_scan(cC); iD = wave_incl_scan(cD);
      if (lane == 63) {
        lds_st<unsigned>(L, L_WT + 4 * w, iA); lds_st<unsigned>(L, L_WT + 16 + 4 * w, iB);
        lds_st<unsigned>(L, L_WT + 32 + 4 * w, iC); lds_st<unsigned>(L, L_WT + 48 + 4 * w, iD);
      }
    }
    __syncthreads();
    STAMP(3, 1);
    const u32x4 wA = lds_ld<u32x4>(L, L_WT), wB = lds_ld<u32x4>(L, L_WT + 16), wC = lds_ld<u32x4>(L, L_WT + 32), wD = lds_ld<u32x4>(L, L_WT + 48);
    const unsigned tA = wA[0] + wA[1] + wA[2] + wA[3], tB = wB[0] + wB[1] + wB[2] + wB[3];
    const unsigned tC = wC[0] + wC[1] + wC[2] + wC[3], tD = wD[0] + wD[1] + wD[2] + wD[3];
    unsigned tot[5] = {tA & 0xffffu, tA >> 16, tB & 0xffffu, tB >> 16, tC};
    unsigned base[6];
    base[0] = 0;
#pragma unroll
    for (int g = 0; g < 5; ++g) base[g + 1] = base[g] + ((tot[g] + 31) & ~31u);
    const bool fits = __builtin_amdgcn_readfirstlane((int)(tD <= (unsigned)CAP && base[5] <= (unsigned)CAP)) != 0;
    if (!fits) {
      np >>= 1;
      __syncthreads();
      continue;
    }
    if (w < 4) {
      const unsigned pA = iA - cA + (w > 0 ? wA[0] : 0) + (w > 1 ? wA[1] : 0) + (w > 2 ? wA[2] : 0);
      const unsigned pB = iB - cB + (w > 0 ? wB[0] : 0) + (w > 1 ? wB[1] : 0) + (w > 2 ? wB[2] : 0);
      const unsigned pC = iC - cC + (w > 0 ? wC[0] : 0) + (w > 1 ? wC[1] : 0) + (w > 2 ? wC[2] : 0);
      const unsigned s0 = ent[0] & 0xffffu, s1 = ent[0] >> 16, s2 = ent[1] & 0xffffu, s3 = ent[1] >> 16, s4 = ent[2] & 0xffffu;
      const unsigned v0 = L_RAW + 8 * (base[0] + (pA & 0xffffu));
      const unsigned v1 = L_RAW + 8 * (base[1] + (pA >> 16) - (s1 - s0));
      const unsigned v2 = L_RAW + 8 * (base[2] + (pB & 0xffffu) - (s2 - s0));
      const unsigned v3 = L_RAW + 8 * (base[3] + (pB >> 16) - (s3 - s0));
      const unsigned v4 = L_RAW + 8 * (base[4] + pC - (s4 - s0));
      lds_st<u32x4>(L, L_TBL + 16 * tid, u32x4{(v0 & 0xffffu) | (v1 << 16), (v2 & 0xffffu) | (v3 << 16), v4 & 0xffffu, 0});
    } else {
      const int t = tid - 256;
      const u32x2 pad = {(unsigned)NN | ((unsigned)(L_PD + 16 * BN) << 16), 0};
      if (t < 160) {
        const int g = t >> 5, k = t & 31;
        const unsigned bg = g == 0 ? base[0] : g == 1 ? base[1] : g == 2 ? base[2] : g == 3 ? base[3] : base[4];
        const unsigned bn = g == 0 ? base[1] : g == 1 ? base[2] : g == 2 ? base[3] : g == 3 ? base[4] : base[5];
        const unsigned tg = g == 0 ? tot[0] : g == 1 ? tot[1] : g == 2 ? tot[2] : g == 3 ? tot[3] : tot[4];
        const unsigned slot = bg + tg + k;
        if (slot < bn) lds_st<u32x2>(L, L_RAW + 8 * slot, pad);
      } else {
        lds_st<u32x2>(L, L_RAW + 8 * (base[5] + t - 160), pad);
      }
    }
    if (!staged) {
      staged = true;
      if (w >= 4) {
        const int u = (w - 4) >> 1, t = w & 1;
        f32x16 pacc;
#pragma unroll
        for (int i = 0; i < 16; ++i) pacc[i] = 0.f;
#pragma unroll
        for (int s = 0; s < 4; ++s) {
          const unsigned o = L_RING + (16 * s + 8 * (lane >> 5) + ((lane & 15) >> 2)) * 128 + (32 * t + 16 * ((lane >> 4) & 1) + 4 * (lane & 3)) * 2;
          const u32x2 b0 = lds_tr(L, o), b1 = lds_tr(L, o + 512);
          pacc = mfma(ha[s], as_frag(u32x4{b0[0], b0[1], b1[0], b1[1]}), pacc);
        }
#pragma unroll
        for (int q = 0; q < 4; ++q)
          if (4 * u + q < 5)
            lds_st<u32x2>(L, L_PFR + ((4 * u + q) * 2 + t) * 512 + lane * 8,
                          u32x2{cvt_pk(pacc[4 * q] * NLOG2E, pacc[4 * q + 1] * NLOG2E), cvt_pk(pacc[4 * q + 2] * NLOG2E, pacc[4 * q + 3] * NLOG2E)});
      } else if (w == 0) {
        if (lane <= BN)
          lds_st<f32x4>(L, L_PD + 16 * lane, lane < BN ? f32x4{bf_lo(sraw[0]), bf_lo(sraw[1]), bf_lo(sraw[2]), 1.0f} : f32x4{0.f, 0.f, 0.f, 0.f});
      } else if (w == 1) {
        lds_st<float>(L, L_WX + 4 * lane, bf_lo(sraw[0]));
      }
#pragma unroll
      for (int t = 0; t < 2; ++t) {
        const float d = bf_lo(craw[2 * t]) * NLOG2E, a = bf_lo(craw[2 * t + 1]) * NLOG2E;
        const unsigned hi = cvt_pk(d, a);
        cw[t][0] = hi;
        cw[t][1] = cvt_pk(d - bf_lo(hi), a - bf_hi(hi));
      }
    }
    __syncthreads();
    STAMP(4, 1);

    {
      unsigned a0[4], a1[4];
#pragma unroll
      for (int k = 0; k < 4; ++k) {
        const unsigned tb = L_TBL + 16 * (64 * k + (tid >> 3));
        a0[k] = lds_ld<u16>(L, tb + 2 * ((ra[k][0] >> 23) & 7)) + 8 * j2;
        a1[k] = lds_ld<u16>(L, tb + 2 * ((ra[k][2] >> 23) & 7)) + 8 * j2 + 8;
      }
#pragma unroll
      for (int k = 0; k < 4; ++k) {
        if (j2 < cnt[k]) lds_st<u32x2>(L, a0[k], u32x2{ra[k][0], ra[k][1]});
        if (j2 + 1 < cnt[k]) lds_st<u32x2>(L, a1[k], u32x2{ra[k][2], ra[k][3]});
      }
#pragma unroll
      for (int k = 0; k < 4; ++k) {
        if (j2 + 16 < cnt[k]) {
          const unsigned tb = L_TBL + 16 * (64 * k + (tid >> 3));
          const unsigned b0 = lds_ld<u16>(L, tb + 2 * ((rb[k][0] >> 23) & 7)) + 8 * (j2 + 16);
          const unsigned b1 = lds_ld<u16>(L, tb + 2 * ((rb[k][2] >> 23) & 7)) + 8 * (j2 + 17);
          lds_st<u32x2>(L, b0, u32x2{rb[k][0], rb[k][1]});
          if (j2 + 17 < cnt[k]) lds_st<u32x2>(L, b1, u32x2{rb[k][2], rb[k][3]});
        }
      }
#pragma unroll
      for (int k = 0; k < 4; ++k) {
        for (unsigned jj = (tid & 7) + 32; jj < cnt[k]; jj += 8) {
          const int p = 64 * k + (tid >> 3);
          const u32x2 rec = *(const u32x2*)(recs + ((size_t)p * CH + (e3[k] & 0xffffu) + jj) * 8);
          const unsigned a = lds_ld<u16>(L, L_TBL + 16 * p + 2 * ((rec[0] >> 23) & 7));
          lds_st<u32x2>(L, a + 8 * jj, rec);
        }
      }
    }
    __syncthreads();
    STAMP(5, 1);

    const int T = __builtin_amdgcn_readfirstlane((int)(base[5] >> 5));
    const int tq8 = T >> 3, trem = T & 7;
    const int ta = w * tq8 + (w < trem ? w : trem);
    const int tb = ta + tq8 + (w < trem ? 1 : 0);
    const int tg1 = __builtin_amdgcn_readfirstlane((int)(base[1] >> 5)), tg2 = __builtin_amdgcn_readfirstlane((int)(base[2] >> 5));
    const int tg3 = __builtin_amdgcn_readfirstlane((int)(base[3] >> 5)), tg4 = __builtin_amdgcn_readfirstlane((int)(base[4] >> 5));

    if (ta < tb) {
      const char *qtab = ws + WS_QTAB, *pos4 = ws + WS_POS4;
      const unsigned ringb = L_RING + w * 4096;
      unsigned aq[4];
#pragma unroll
      for (int s = 0; s < 4; ++s) aq[s] = ringb + r * 128 + (((2 * s + hf) ^ ((r >> 1) & 7)) << 4);
      const unsigned dc0 = ((lane & 7) ^ ((lane >> 4) & 7)) << 4, dc1 = ((lane & 7) ^ ((4 + (lane >> 4)) & 7)) << 4;
      const unsigned Sb = L_S + w * S_WAVE;
      const unsigned pi_e = (r & 0x13) | ((r & 4) << 1) | ((r & 8) >> 1);
      const unsigned sa_c = Sb + 2 * pi_e;
      const unsigned s_rd = Sb + r * 80 + 16 * hf;
      const int g16 = lane >> 4, tq = (lane & 15) >> 2, tp = lane & 3, tu = g16 & 1, th = g16 >> 1;
      const unsigned s_tr = Sb + (4 * (4 * th + tq) + 3) * 80 + 2 * (16 * tu + 8 * (tp & 1) + 4 * (tp >> 1));
      const unsigned acc_l = L_ACC + hf * ACC_NODE + 4 * r;
      bf16x8 bid[2];
#pragma unroll
      for (int u = 0; u < 2; ++u)
#pragma unroll
        for (int j = 0; j < 8; ++j) bid[u][j] = (r == (j & 3) + 8 * (2 * hf + (j >> 2)) + 4 * u) ? (short)0x3f80 : (short)0;

      auto group_of = [&](int t) { return __builtin_amdgcn_readfirstlane((t >= tg1) + (t >= tg2) + (t >= tg3) + (t >= tg4)); };
      auto group_end = [&](int g) { return __builtin_amdgcn_readfirstlane(g == 0 ? tg1 : g == 1 ? tg2 : g == 2 ? tg3 : g == 3 ? tg4 : T); };
      int gend = group_end(group_of(ta));
      int tflush = -1;

      bf16x8 bxA0, bxA1, bxB0, bxB1;
      auto load_bx = [&](int gg) {
        const u32x2 p0 = lds_ld<u32x2>(L, L_PFR + gg * 1024 + lane * 8), p1 = lds_ld<u32x2>(L, L_PFR + gg * 1024 + 512 + lane * 8);
        bxA0 = as_frag(u32x4{p0[0], p0[1], hf ? 0u : cw[0][0], hf ? 0u : cw[0][1]});
        bxB0 = as_frag(u32x4{p0[0], p0[1], hf ? cw[0][0] : 0u, hf ? cw[0][1] : 0u});
        bxA1 = as_frag(u32x4{p1[0], p1[1], hf ? 0u : cw[1][0], hf ? 0u : cw[1][1]});
        bxB1 = as_frag(u32x4{p1[0], p1[1], hf ? cw[1][0] : 0u, hf ? cw[1][1] : 0u});
        asm volatile("" : "+v"(bxA0), "+v"(bxB0), "+v"(bxA1), "+v"(bxB1));
      };
      auto flush = [&](int gg) {
        const unsigned a = LB + acc_l + gg * (8 * ACC_NODE);
        if (lds_lock(LB + L_LOCK + 4 * gg) != 0) acc_gather(a, a + 2 * ACC_NODE, a + 4 * ACC_NODE, a + 6 * ACC_NODE);
        acc_store(a, a + 2 * ACC_NODE, a + 4 * ACC_NODE, a + 6 * ACC_NODE);
        lds_st<unsigned>(L, L_LOCK + 4 * gg, 2u);
        asm volatile("" ::: "memory");
      };
      struct Idx { unsigned i0, i1, i2, i3; };
      unsigned a_idx = L_RAW + ta * 256 + (lane >> 3) * 8, a_raw = L_RAW + ta * 256 + lane * 8;
      auto idx_load = [&](int dt) {
        const unsigned a = a_idx + dt * 256;
        return Idx{lds_ld<u16>(L, a), lds_ld<u16>(L, a + 64), lds_ld<u16>(L, a + 128), lds_ld<u16>(L, a + 192)};
      };
      auto issue_rows = [&](const Idx& x) {
        dma_tile(qtab, x.i0 * 128 + dc0, x.i1 * 128 + dc1, x.i2 * 128 + dc0, x.i3 * 128 + dc1);
      };
      auto raw_load = [&](int dt) {
        const unsigned a = a_raw + dt * 256;
        Raw x;
        unsigned s = lds_ld<u16>(L, a);
        x.pda = lds_ld<u16>(L, a + 2);
        x.w1 = lds_ld<unsigned>(L, a + 4);
        x.n320 = lds_ld<u16>(L, a + 4);
        asm volatile("global_load_dwordx2 %0, %1, %2" : "=v"(x.ps) : "v"(s * 8u), "s"(pos4) : "memory");
        return x;
      };
      auto prep = [&](const Raw& x) {
        const f32x4 pd = lds_ld<f32x4>(L, x.pda);
        const float dx = pd[0] - bf_lo(x.ps[0]), dy = pd[1] - bf_hi(x.ps[0]), dz = pd[2] - as_f(x.ps[1]);
        unsigned wa = cvt_pk(dx, dy), wb = cvt_pk(dz, pd[3]);
        const float dzr = bf_lo(wb);
        const bf16x2v wav = __builtin_bit_cast(bf16x2v, wa);
        const float d2 = __builtin_amdgcn_fdot2_f32_bf16(wav, wav, dzr * dzr, false);
        Prep o;
        o.de = cvt_pk(d2, as_f(x.w1));
        asm volatile("" : "+v"(o.de), "+v"(wa), "+v"(wb));
        const unsigned P = x.n320 + sa_c, Q = P + 160;
        const auto sa = __builtin_amdgcn_permlane32_swap(P, Q, false, false);
        const auto sd = __builtin_amdgcn_permlane32_swap(wa, wb, false, false);
        o.saA = sa[0]; o.saB = sa[1]; o.sdA = sd[0]; o.sdB = sd[1];
        return o;
      };
      auto wait_ps = [&](Raw& x) { asm volatile("s_waitcnt vmcnt(0)" : "+v"(x.ps) :: "memory"); };

      struct Ops { bf16x8 q0, q1, q2, q3, as0, as1, ax; };
      auto read_ops = [&](unsigned sa, unsigned sd, unsigned de) {
        Ops o;
        o.q0 = lds_ld<bf16x8>(L, aq[0]); o.q1 = lds_ld<bf16x8>(L, aq[1]);
        o.q2 = lds_ld<bf16x8>(L, aq[2]); o.q3 = lds_ld<bf16x8>(L, aq[3]);
        lds_st<u16>(L, sa, (u16)sd);
        lds_st<u16>(L, sa + 80, (u16)(sd >> 16));
        const u32x2 oh = lds_tr(L, s_tr);
        o.as0 = lds_ld<bf16x8>(L, s_rd);
        o.as1 = lds_ld<bf16x8>(L, s_rd + 32);
        lds_st<u16>(L, sa, (u16)0);
        lds_st<u16>(L, sa + 80, (u16)0);
        unsigned de2;
        asm volatile("v_mov_b32 %0, %1" : "=v"(de2) : "v"(de));
        o.ax = as_frag(u32x4{oh[0], oh[1], de, de2});
        return o;
      };
      auto alpha = [&](int t, const Ops& c, const bf16x8& asl, const bf16x8& bx1, auto&& mid) {
        alpha1(asl);
        if (__builtin_expect(t == tflush, 0)) flush(group_of(t - 1));
        alpha2(c.ax, bx1, c.q2, bid[0]);
        SB(); mid(); SB();
        alpha3(c.q3, bid[1], c.as0);
      };
      auto beta = [&](const Ops& c, const Ops& n, const bf16x8& bx0n, auto&& mid) {
        beta1(c.as1);
        beta2(n.ax, bx0n, n.q0, bid[0]);
        SB(); mid(); SB();
        beta3(n.q1, bid[1], c.as0);
      };
      auto next_group = [&](int t) {
        if (__builtin_expect(t == gend, 0)) { const int g = group_of(t); gend = group_end(g); tflush = t; load_bx(g); }
      };

      set_m0(LB + ringb);
      load_bx(group_of(ta));
      issue_rows(idx_load(0));
      Raw rw = raw_load(0);
      wait_ps(rw);
      Prep pr = prep(rw);
      Ops A = read_ops(pr.saA, pr.sdA, pr.de), B;
      B.as1 = A.as1;
      const Idx ix0 = idx_load(1);
      tiles_init();
      chain_y0(A.ax, bxA0, A.q0, A.q1, bid[0], bid[1]);
      asm volatile("s_waitcnt lgkmcnt(0)" ::: "memory");
      issue_rows(ix0);
      STAMP(6, 1);
      CSTAMP(10, 1);
      for (int t = ta;; t += 2) {
        alpha(t, A, B.as1, bxA1, [&]() { rw = raw_load(2); });
        wait_ps(rw);
        next_group(t + 1);
        const Idx ixa = idx_load(2);
        B = read_ops(pr.saB, pr.sdB, pr.de);
        SB();
        beta(A, B, bxB0, [&]() { issue_rows(ixa); });
        if (__builtin_expect(t + 1 >= tb, 0)) { last_block(A.as1); flush(group_of(t)); break; }
        alpha(t + 1, B, A.as1, bxB1, [&]() { pr = prep(rw); });
        wait_vm0();
        next_group(t + 2);
        const Idx ixb = idx_load(3);
        A = read_ops(pr.saA, pr.sdA, pr.de);
        SB();
        beta(B, A, bxA0, [&]() { issue_rows(ixb); });
        if (__builtin_expect(t + 2 >= tb, 0)) { last_block(B.as1); flush(group_of(t + 1)); break; }
        a_idx += 512;
        a_raw += 512;
      }
      wait_vm0();
      const char* wh = karg<const char>(4);
      dma_piece(wh + w * 2048, lane * 16, LB + ringb);
      dma_piece(wh + w * 2048 + 1024, lane * 16, LB + ringb + 1024);
      STAMP(7, 1);
      CSTAMP(11, 1);
    } else {
      const char* wh = karg<const char>(4);
      dma_piece(wh + w * 2048, lane * 16, LB + L_RING + w * 4096);
      dma_piece(wh + w * 2048 + 1024, lane * 16, LB + L_RING + w * 4096 + 1024);
    }
    pa += np;
    if (pa >= NP) break;
    wait_vm0();
    __syncthreads();
  }
  wait_vm0();
  __syncthreads();
  STAMP(8, 1);

  u16* out = karg<u16>(6);
  if (w < 4) {
    const int g16 = lane >> 4, tq = (lane & 15) >> 2, tp = lane & 3, tu = g16 & 1, th = g16 >> 1;
    const int u = w >> 1, t = w & 1;
    const int nl = 32 * u + r, nlc = nl < BN ? nl : BN - 1;
    const u16* hrow = karg<const u16>(0) + (size_t)(n0 + nlc) * 64;
    f32x16 d;
#pragma unroll
    for (int i = 0; i < 16; ++i) d[i] = 0.f;
    const unsigned w_tr = L_RING + (8 * th + tq) * 128 + (32 * t + 16 * tu + 4 * tp) * 2;
#pragma unroll
    for (int s = 0; s < 8; ++s) {
      const u32x2 a0 = lds_tr(L, w_tr + s * 4096), a1 = lds_tr(L, w_tr + s * 4096 + 512);
      const bf16x8 a = as_frag(u32x4{a0[0], a0[1], a1[0], a1[1]});
      bf16x8 bb;
      if (s < 4) {
        bb = *(const bf16x8*)(hrow + 16 * s + 8 * hf);
      } else {
        const unsigned o = L_ACC + nlc * ACC_NODE + 3 * 256 + (16 * (s - 4) + 8 * hf) * 4;
        const f32x4 m0 = lds_ld<f32x4>(L, o) * NLN2, m1 = lds_ld<f32x4>(L, o + 16) * NLN2;
        bb = as_frag(u32x4{cvt_pk(m0[0], m0[1]), cvt_pk(m0[2], m0[3]), cvt_pk(m1[0], m1[1]), cvt_pk(m1[2], m1[3])});
      }
      d = mfma(a, bb, d);
    }
    if (nl < BN) {
#pragma unroll
      for (int q = 0; q < 4; ++q) {
        const int f = 32 * t + 8 * q + 4 * hf;
        const u32x2 hv = *(const u32x2*)(hrow + f);
        float o4[4];
#pragma unroll
        for (int k = 0; k < 4; ++k) {
          const float act = bf_round(silu_f(bf_round(d[4 * q + k])));
          const float hres = (k & 1) ? bf_hi(hv[k >> 1]) : bf_lo(hv[k >> 1]);
          o4[k] = hres + act;
        }
        *(u32x2*)(out + (size_t)(n0 + nl) * 64 + f) = u32x2{cvt_pk(o4[0], o4[1]), cvt_pk(o4[2], o4[3])};
      }
    }
  } else {
    const int idx = tid_ - 256, nc = idx >> 1, half = idx & 1;
    const int ncc = nc < 3 * BN ? nc : 3 * BN - 1;
    const int n = ncc / 3, c = ncc - 3 * n;
    const unsigned o = L_ACC + n * ACC_NODE + c * 256 + half * 128;
    float s = 0.f;
#pragma unroll
    for (int k = 0; k < 8; ++k) {
      const f32x4 v = lds_ld<f32x4>(L, o + 16 * k), x = lds_ld<f32x4>(L, L_WX + half * 128 + 16 * k);
      s += v[0] * x[0] + v[1] * x[1] + v[2] * x[2] + v[3] * x[3];
    }
    s += __builtin_bit_cast(float, __builtin_amdgcn_update_dpp(0, __builtin_bit_cast(int, s), 0xb1, 0xf, 0xf, true));
    if (nc < 3 * BN && half == 0) {
      const float p0 = lds_ld<float>(L, L_PD + 16 * n + 4 * c);
      out[(size_t)NN * 64 + (size_t)n0 * 3 + nc] = (u16)cvt_pk(p0 + bf_round(s * NLN2), 0.f);
    }
  }
  STAMP(9, 1);
}

}

extern "C" void kernel_launch(void* const* d_in, const int* in_sizes, int n_in, void* d_out, int out_size, void* d_ws, size_t ws_size,
                              hipStream_t stream) {
  const u16* h = (const u16*)d_in[0];
  const u16* pos = (const u16*)d_in[1];
  const u16* ea = (const u16*)d_in[2];
  const int* src = (const int*)d_in[3];
  const int* dst = (const int*)d_in[4];
  const u16* we = (const u16*)d_in[5];
  const u16* wx = (const u16*)d_in[6];
  const u16* wh = (const u16*)d_in[7];
  sort_kernel<<<NP + K1_TABLE_BLOCKS, K1_THREADS, 0, stream>>>(src, dst, ea, h, pos, we, (char*)d_ws);
  edge_kernel<<<NBK, 512, 0, stream>>>(h, pos, we, wx, wh, (const char*)d_ws, (u16*)d_out);
}
```

```cpp
#include <hip/hip_runtime.h>
#include <hip/hip_bf16.h>
#include <stdint.h>

#define DI __device__ __forceinline__
#define AS3 __attribute__((address_space(3)))
#define SB() __builtin_amdgcn_sched_barrier(0)

typedef short bf16x8 __attribute__((ext_vector_type(8)));
typedef short s16x4 __attribute__((ext_vector_type(4)));
typedef float f32x16 __attribute__((ext_vector_type(16)));
typedef float f32x4 __attribute__((ext_vector_type(4)));
typedef float f32x2 __attribute__((ext_vector_type(2)));
typedef unsigned u32x4 __attribute__((ext_vector_type(4)));
typedef unsigned u32x3 __attribute__((ext_vector_type(3)));
typedef unsigned u32x2 __attribute__((ext_vector_type(2)));
typedef int i32x4 __attribute__((ext_vector_type(4)));
typedef __bf16 bf16x2v __attribute__((ext_vector_type(2)));
typedef unsigned short u16;

namespace {

constexpr int NN = 10000;
constexpr int NE = 640000;
constexpr int NP = 256;
constexpr int CH = NE / NP;
constexpr int BN = 40;
constexpr int NBK = NN / BN;
constexpr int K1_THREADS = 640;
constexpr float NLOG2E = -1.4426950408889634f;
constexpr float NLN2 = -0.6931471805599453f;

constexpr size_t WS_RECS = 0;
constexpr size_t WS_ENT = WS_RECS + (size_t)NP * CH * 8;
constexpr size_t WS_POS4 = WS_ENT + (size_t)NP * 256 * 16;

constexpr int CAP = 2880;
constexpr int PADS = 160;
constexpr int L_S = 0;
constexpr int S_WAVE = 2560;
constexpr int L_PD = 20480;
constexpr int L_WT = 21248;
constexpr int L_LOCK = 21312;
constexpr int L_WX = 21504;
constexpr int L_TBL = 21760;
constexpr int L_RAW = L_TBL + 4096;
constexpr int L_PFR = L_RAW + (CAP + PADS) * 8;
constexpr int L_RING = L_PFR + 5120;
constexpr int L_ACC = L_RING + 8 * 4096;
constexpr int ACC_NODE = 1040;
constexpr int L_W = L_ACC + BN * ACC_NODE;
constexpr int L_H = L_W + 16384;
constexpr int L_END = L_H + BN * 128;
static_assert(L_PD % 1024 == 0, "group id is read from bits 7..9 of the pos address");
static_assert(L_RAW + (CAP + PADS) * 8 < 65536 && L_RAW >= 8 * CH, "run table holds 16-bit LDS addresses");
static_assert(L_PFR % 16 == 0 && L_RING % 16 == 0 && L_ACC % 16 == 0 && L_W % 16 == 0 && L_END <= 163840, "LDS map");

DI unsigned cvt_pk(float a, float b) { f32x2 v = {a, b}; return __builtin_bit_cast(unsigned, __builtin_convertvector(v, bf16x2v)); }
DI float as_f(unsigned u) { return __builtin_bit_cast(float, u); }
DI float bf_lo(unsigned u) { return __builtin_bit_cast(float, u << 16); }
DI float bf_hi(unsigned u) { return __builtin_bit_cast(float, u & 0xffff0000u); }
DI float bf_round(float x) { return bf_lo(cvt_pk(x, x)); }
DI float bf_ld(const u16* p) { return __builtin_bit_cast(float, (unsigned)*p << 16); }
DI f32x16 mfma(bf16x8 a, bf16x8 b, f32x16 c) { return __builtin_amdgcn_mfma_f32_32x32x16_bf16(a, b, c, 0, 0, 0); }
DI bf16x8 as_frag(u32x4 v) { return __builtin_bit_cast(bf16x8, v); }

DI unsigned wave_incl_scan(unsigned x) {
  x += __builtin_amdgcn_update_dpp(0, (int)x, 0x111, 0xf, 0xf, true);
  x += __builtin_amdgcn_update_dpp(0, (int)x, 0x112, 0xf, 0xf, true);
  x += __builtin_amdgcn_update_dpp(0, (int)x, 0x114, 0xf, 0xf, true);
  x += __builtin_amdgcn_update_dpp(0, (int)x, 0x118, 0xf, 0xf, true);
  x += __builtin_amdgcn_update_dpp(0, (int)x, 0x142, 0xa, 0xf, false);
  x += __builtin_amdgcn_update_dpp(0, (int)x, 0x143, 0xc, 0xf, false);
  return x;
}

template <typename T> DI T lds_ld(AS3 char* L, unsigned off) { return *(AS3 T*)(L + off); }
template <typename T> DI void lds_st(AS3 char* L, unsigned off, T v) { *(AS3 T*)(L + off) = v; }
DI u32x2 lds_tr(AS3 char* L, unsigned off) {
  return __builtin_bit_cast(u32x2, __builtin_amdgcn_ds_read_tr16_b64_v4i16((AS3 s16x4*)(L + off)));
}

DI const char* uni_ptr(const char* p) {
  const unsigned long long v = (unsigned long long)p;
  const unsigned lo = __builtin_amdgcn_readfirstlane((int)(unsigned)v), hi = __builtin_amdgcn_readfirstlane((int)(unsigned)(v >> 32));
  return (const char*)(__attribute__((address_space(1))) const char*)(((unsigned long long)hi << 32) | lo);
}
DI void dma_piece(const char* base_, unsigned voff, unsigned ldsaddr_) {
  const char* base = uni_ptr(base_);
  const unsigned ldsaddr = __builtin_amdgcn_readfirstlane((int)ldsaddr_);
  unsigned keep;
  asm volatile("s_mov_b32 %0, m0\n\ts_mov_b32 m0, %3\n\ts_nop 0\n\tglobal_load_lds_dwordx4 %1, %2\n\ts_mov_b32 m0, %0"
               : "=&s"(keep) : "v"(voff), "s"(base), "s"(ldsaddr) : "memory");
}
DI void set_m0(unsigned ldsaddr) { asm volatile("s_mov_b32 m0, %0\n\ts_nop 0" :: "s"(ldsaddr) : "memory"); }
DI void dma_tile(const char* base, unsigned v0, unsigned v1, unsigned v2, unsigned v3) {
  asm volatile("global_load_lds_dwordx4 %0, %4\n\t"
               "global_load_lds_dwordx4 %1, %5 offset:1024\n\t"
               "global_load_lds_dwordx4 %2, %6 offset:2048\n\t"
               "global_load_lds_dwordx4 %3, %7 offset:3072"
               :: "v"(v0), "v"(v1), "v"(v2), "v"(v3), "s"(base), "s"(base - 1024), "s"(base - 2048), "s"(base - 3072)
               : "memory");
}
#define STAMP(k, kern)
#define CSTAMP(k, kern)
DI void st16_wt(void* p, u32x4 v) { __builtin_nontemporal_store(v, (u32x4*)p); }
DI void wait_vm0() { asm volatile("s_waitcnt vmcnt(0)" ::: "memory"); }

__global__ __launch_bounds__(K1_THREADS) void sort_kernel(const int* __restrict__ src, const int* __restrict__ dst,
                                                          const u16* __restrict__ ea, const u16* __restrict__ pos, char* ws) {
  __shared__ __attribute__((aligned(16))) char lds[4096 + 64 + CH * 8];
  AS3 char* L = (AS3 char*)lds;
  constexpr int C_CNT = 0, C_WT = 4096, C_SORT = 4160;
  const int tid = threadIdx.x, lane = tid & 63;
  const int w = __builtin_amdgcn_readfirstlane(tid >> 6);
  const int p = blockIdx.x;
  STAMP(0, 0);
  const bool act = tid < CH / 4;
  i32x4 d4 = {0, 0, 0, 0}, s4 = {0, 0, 0, 0};
  u32x2 a2 = {0, 0};
  if (act) {
    const int e0 = p * CH + 4 * tid;
    d4 = *(const i32x4*)(dst + e0);
    s4 = *(const i32x4*)(src + e0);
    a2 = *(const u32x2*)(ea + e0);
  }
  const int pn = p * BN + tid - (K1_THREADS - 64);
  const bool do_pos = tid >= K1_THREADS - 64 && tid < K1_THREADS - 64 + BN && p < NBK;
  unsigned px = 0, py = 0, pz = 0;
  if (do_pos) { px = pos[pn * 3]; py = pos[pn * 3 + 1]; pz = pos[pn * 3 + 2]; }
  if (tid < 256) lds_st<u32x4>(L, C_CNT + 16 * tid, u32x4{0, 0, 0, 0});
  __syncthreads();
  STAMP(1, 0);
  unsigned idx[4], rank[4], bk[4];
#pragma unroll
  for (int i = 0; i < 4; ++i) {
    const unsigned G = (unsigned)d4[i] >> 3;
    bk[i] = (G * 52429u) >> 18;
    idx[i] = G + 3 * bk[i];
    rank[i] = 0;
    if (act) {
      const unsigned sh = (idx[i] & 1) << 4;
      const unsigned old = __hip_atomic_fetch_add((AS3 unsigned*)(L + C_CNT + 4 * (idx[i] >> 1)), 1u << sh, __ATOMIC_RELAXED,
                                                  __HIP_MEMORY_SCOPE_WORKGROUP);
      rank[i] = (old >> sh) & 0xffffu;
    }
  }
  STAMP(2, 0);
  __syncthreads();
  STAMP(3, 0);
  u32x4 c = {0, 0, 0, 0};
  unsigned tot = 0, incl = 0;
  if (tid < 256) {
    c = lds_ld<u32x4>(L, C_CNT + 16 * tid);
    tot = (c[0] & 0xffffu) + (c[0] >> 16) + (c[1] & 0xffffu) + (c[1] >> 16) + (c[2] & 0xffffu);
    incl = wave_incl_scan(tot);
    if (lane == 63) lds_st<unsigned>(L, C_WT + 4 * w, incl);
  }
  __syncthreads();
  if (tid < 256) {
    const u32x4 wt = lds_ld<u32x4>(L, C_WT);
    const unsigned s0 = incl - tot + (w > 0 ? wt[0] : 0) + (w > 1 ? wt[1] : 0) + (w > 2 ? wt[2] : 0);
    const unsigned s1 = s0 + (c[0] & 0xffffu), s2 = s1 + (c[0] >> 16), s3 = s2 + (c[1] & 0xffffu), s4_ = s3 + (c[1] >> 16);
    const unsigned s5 = s0 + tot;
    const u32x4 e = {s0 | (s1 << 16), s2 | (s3 << 16), s4_ | (s5 << 16), s0 | (tot << 16)};
    lds_st<u32x4>(L, C_CNT + 16 * tid, e);
    st16_wt(ws + WS_ENT + ((size_t)p * 256 + tid) * 16, e);
  }
  __syncthreads();
  STAMP(4, 0);
  if (act) {
    unsigned ps[4];
#pragma unroll
    for (int i = 0; i < 4; ++i) ps[i] = lds_ld<u16>(L, C_CNT + 2 * idx[i]) + rank[i];
#pragma unroll
    for (int i = 0; i < 4; ++i) {
      const unsigned d = (unsigned)d4[i], dl = d - BN * bk[i];
      const unsigned eav = (i & 1) ? (a2[i >> 1] & 0xffff0000u) : (a2[i >> 1] << 16);
      const u32x2 rec = {(unsigned)s4[i] | ((L_PD + 16 * dl) << 16), ((d & 7) * 320) | eav};
      lds_st<u32x2>(L, C_SORT + 8 * ps[i], rec);
    }
  }
  __syncthreads();
  STAMP(5, 0);
  if (do_pos) __builtin_nontemporal_store(u32x2{px | (py << 16), pz << 16}, (u32x2*)(ws + WS_POS4 + (size_t)pn * 8));
  char* out = ws + WS_RECS + (size_t)p * (CH * 8);
#pragma unroll
  for (int i = 0; i < 2; ++i) {
    const int k = tid + i * K1_THREADS;
    if (k < CH / 2) st16_wt(out + 16 * k, lds_ld<u32x4>(L, C_SORT + 16 * k));
  }
  STAMP(6, 0);
  STAMP(7, 0);
}

struct Prep {
  unsigned sdA, sdB;
  unsigned saA, saB;
  unsigned de;
};
struct Raw { unsigned src, pda, w1, n320; };

DI float silu_f(float x) { return x * __builtin_amdgcn_rcpf(1.0f + __builtin_amdgcn_exp2f(x * NLOG2E)); }

#pragma clang diagnostic ignored "-Winline-asm"
#define T4(op, d, s0, s1, s2, s3, d1, d2, d3) op " v" #d ", v" #s0 "\n\t" op " v" #d1 ", v" #s1 "\n\t" op " v" #d2 ", v" #s2 "\n\t" op " v" #d3 ", v" #s3 "\n\t"
#define EXP_LO(a, b, c, d) T4("v_exp_f32", 184, a, b, c, d, 185, 186, 187)
#define EXP_HI(a, b, c, d) T4("v_exp_f32", 188, a, b, c, d, 189, 190, 191)
#define RCP_LO T4("v_rcp_f32", 184, 184, 185, 186, 187, 185, 186, 187)
#define RCP_HI T4("v_rcp_f32", 188, 188, 189, 190, 191, 189, 190, 191)
#define PKA(a, b) "v_pk_add_f32 v[" #a ":" #b "], v[" #a ":" #b "], 1.0 op_sel_hi:[1,0]\n\t"
#define ADD1 PKA(184, 185) PKA(186, 187) PKA(188, 189) PKA(190, 191)
#define PKM(a, b, c, d) "v_pk_mul_f32 v[" #a ":" #b "], v[" #a ":" #b "], v[" #c ":" #d "]\n\t"
#define MULY(a0, a1, a2, a3, a4, a5, a6, a7) PKM(184, 185, a0, a1) PKM(186, 187, a2, a3) PKM(188, 189, a4, a5) PKM(190, 191, a6, a7)
#define CVT(d, a, b) "v_cvt_pk_bf16_f32 v" #d ", v" #a ", v" #b "\n\t"
#define PACK(d0, d1, d2, d3) CVT(d0, 184, 185) CVT(d1, 186, 187) CVT(d2, 188, 189) CVT(d3, 190, 191)
#define MF(acc, a, b) "v_mfma_f32_32x32x16_bf16 " acc ", " a ", " b ", " acc "\n\t"
#define MF0(acc, a, b) "v_mfma_f32_32x32x16_bf16 " acc ", " a ", " b ", 0\n\t"
#define Y0 "v[192:207]"
#define Y1 "v[208:223]"
#define ACC0 "v[224:239]"
#define ACC1 "v[240:255]"
#define X0 "v[176:179]"
#define X1 "v[180:183]"

DI void tiles_init() {
  asm volatile(
               "v_mov_b64 v[224:225], 0\n\t"
               "v_mov_b64 v[226:227], 0\n\t"
               "v_mov_b64 v[228:229], 0\n\t"
               "v_mov_b64 v[230:231], 0\n\t"
               "v_mov_b64 v[232:233], 0\n\t"
               "v_mov_b64 v[234:235], 0\n\t"
               "v_mov_b64 v[236:237], 0\n\t"
               "v_mov_b64 v[238:239], 0\n\t"
               "v_mov_b64 v[240:241], 0\n\t"
               "v_mov_b64 v[242:243], 0\n\t"
               "v_mov_b64 v[244:245], 0\n\t"
               "v_mov_b64 v[246:247], 0\n\t"
               "v_mov_b64 v[248:249], 0\n\t"
               "v_mov_b64 v[250:251], 0\n\t"
               "v_mov_b64 v[252:253], 0\n\t"
               "v_mov_b64 v[254:255], 0\n\t"
               "v_mov_b64 v[180:181], 0\n\t"
               "v_mov_b64 v[182:183], 0\n\t"
               "s_nop 1" ::: "v176", "v255");
}
DI void alpha1(const bf16x8& asl) {
  asm volatile(EXP_LO(192, 193, 194, 195) MF(ACC1, "%[asl]", X1) EXP_HI(196, 197, 198, 199) :: [asl] "v"(asl));
}
DI void alpha2(const bf16x8& ax, const bf16x8& bx, const bf16x8& q0, const bf16x8& w0) {
  asm volatile(ADD1 MF0(Y1, "%[ax]", "%[bx]") RCP_LO MF(Y1, "%[q0]", "%[w0]") RCP_HI
               MULY(192, 193, 194, 195, 196, 197, 198, 199) PACK(176, 177, 178, 179)
               :: [ax] "v"(ax), [bx] "v"(bx), [q0] "v"(q0), [w0] "v"(w0));
}
DI void alpha3(const bf16x8& q1, const bf16x8& w1, const bf16x8& q2, const bf16x8& w2, const bf16x8& q3, const bf16x8& w3, const bf16x8& as0) {
  asm volatile(MF(Y1, "%[q1]", "%[w1]") EXP_LO(200, 201, 202, 203) MF(Y1, "%[q2]", "%[w2]") EXP_HI(204, 205, 206, 207) ADD1
               MF(Y1, "%[q3]", "%[w3]") RCP_LO MF(ACC0, "%[as]", X0) RCP_HI
               MULY(200, 201, 202, 203, 204, 205, 206, 207) PACK(180, 181, 182, 183)
               :: [q1] "v"(q1), [w1] "v"(w1), [q2] "v"(q2), [w2] "v"(w2), [q3] "v"(q3), [w3] "v"(w3), [as] "v"(as0));
}
DI void beta1(const bf16x8& as1) {
  asm volatile(EXP_LO(208, 209, 210, 211) MF(ACC0, "%[as]", X1) EXP_HI(212, 213, 214, 215) ADD1 :: [as] "v"(as1));
}
DI void beta2(const bf16x8& ax, const bf16x8& bx, const bf16x8& q0, const bf16x8& w0) {
  asm volatile(MF0(Y0, "%[ax]", "%[bx]") RCP_LO MF(Y0, "%[q0]", "%[w0]") RCP_HI
               MULY(208, 209, 210, 211, 212, 213, 214, 215) PACK(176, 177, 178, 179)
               :: [ax] "v"(ax), [bx] "v"(bx), [q0] "v"(q0), [w0] "v"(w0));
}
DI void beta3(const bf16x8& q1, const bf16x8& w1, const bf16x8& q2, const bf16x8& w2, const bf16x8& q3, const bf16x8& w3, const bf16x8& as0) {
  asm volatile(MF(Y0, "%[q1]", "%[w1]") EXP_LO(216, 217, 218, 219) MF(Y0, "%[q2]", "%[w2]") EXP_HI(220, 221, 222, 223) ADD1
               MF(Y0, "%[q3]", "%[w3]") RCP_LO MF(ACC1, "%[as]", X0) RCP_HI
               MULY(216, 217, 218, 219, 220, 221, 222, 223) PACK(180, 181, 182, 183)
               :: [q1] "v"(q1), [w1] "v"(w1), [q2] "v"(q2), [w2] "v"(w2), [q3] "v"(q3), [w3] "v"(w3), [as] "v"(as0));
}
DI void chain_y0(const bf16x8& ax, const bf16x8& bx, const bf16x8& q0, const bf16x8& w0, const bf16x8& q1, const bf16x8& w1,
                 const bf16x8& q2, const bf16x8& w2, const bf16x8& q3, const bf16x8& w3) {
  asm volatile("s_nop 1\n\t" MF0(Y0, "%[ax]", "%[bx]") MF(Y0, "%[q0]", "%[w0]") MF(Y0, "%[q1]", "%[w1]") MF(Y0, "%[q2]", "%[w2]")
               MF(Y0, "%[q3]", "%[w3]") "s_nop 15"
               :: [ax] "v"(ax), [bx] "v"(bx), [q0] "v"(q0), [w0] "v"(w0), [q1] "v"(q1), [w1] "v"(w1), [q2] "v"(q2), [w2] "v"(w2),
                  [q3] "v"(q3), [w3] "v"(w3));
}
DI void last_block(const bf16x8& as1) { asm volatile("s_nop 1\n\t" MF(ACC1, "%[as]", X1) :: [as] "v"(as1)); }
DI void acc_gather(unsigned a0, unsigned a1, unsigned a2, unsigned a3) {
  f32x2 t0, t1, t2, t3, t4, t5, t6, t7;
  asm volatile(
               "ds_read2_b32 %[t0], %[a0] offset0:0 offset1:64\n\t"
               "ds_read2_b32 %[t1], %[a0] offset0:128 offset1:192\n\t"
               "ds_read2_b32 %[t2], %[a0] offset0:32 offset1:96\n\t"
               "ds_read2_b32 %[t3], %[a0] offset0:160 offset1:224\n\t"
               "ds_read2_b32 %[t4], %[a1] offset0:0 offset1:64\n\t"
               "ds_read2_b32 %[t5], %[a1] offset0:128 offset1:192\n\t"
               "ds_read2_b32 %[t6], %[a1] offset0:32 offset1:96\n\t"
               "ds_read2_b32 %[t7], %[a1] offset0:160 offset1:224\n\t"
               "s_waitcnt lgkmcnt(4)\n\t"
               "v_pk_add_f32 v[224:225], v[224:225], %[t0]\n\t"
               "v_pk_add_f32 v[226:227], v[226:227], %[t1]\n\t"
               "v_pk_add_f32 v[240:241], v[240:241], %[t2]\n\t"
               "v_pk_add_f32 v[242:243], v[242:243], %[t3]\n\t"
               "s_waitcnt lgkmcnt(0)\n\t"
               "v_pk_add_f32 v[228:229], v[228:229], %[t4]\n\t"
               "v_pk_add_f32 v[230:231], v[230:231], %[t5]\n\t"
               "v_pk_add_f32 v[244:245], v[244:245], %[t6]\n\t"
               "v_pk_add_f32 v[246:247], v[246:247], %[t7]\n\t"
               "ds_read2_b32 %[t0], %[a2] offset0:0 offset1:64\n\t"
               "ds_read2_b32 %[t1], %[a2] offset0:128 offset1:192\n\t"
               "ds_read2_b32 %[t2], %[a2] offset0:32 offset1:96\n\t"
               "ds_read2_b32 %[t3], %[a2] offset0:160 offset1:224\n\t"
               "ds_read2_b32 %[t4], %[a3] offset0:0 offset1:64\n\t"
               "ds_read2_b32 %[t5], %[a3] offset0:128 offset1:192\n\t"
               "ds_read2_b32 %[t6], %[a3] offset0:32 offset1:96\n\t"
               "ds_read2_b32 %[t7], %[a3] offset0:160 offset1:224\n\t"
               "s_waitcnt lgkmcnt(4)\n\t"
               "v_pk_add_f32 v[232:233], v[232:233], %[t0]\n\t"
               "v_pk_add_f32 v[234:235], v[234:235], %[t1]\n\t"
               "v_pk_add_f32 v[248:249], v[248:249], %[t2]\n\t"
               "v_pk_add_f32 v[250:251], v[250:251], %[t3]\n\t"
               "s_waitcnt lgkmcnt(0)\n\t"
               "v_pk_add_f32 v[236:237], v[236:237], %[t4]\n\t"
               "v_pk_add_f32 v[238:239], v[238:239], %[t5]\n\t"
               "v_pk_add_f32 v[252:253], v[252:253], %[t6]\n\t"
               "v_pk_add_f32 v[254:255], v[254:255], %[t7]\n\t"
               "s_nop 0"
               : [t0] "=&v"(t0), [t1] "=&v"(t1), [t2] "=&v"(t2), [t3] "=&v"(t3), [t4] "=&v"(t4), [t5] "=&v"(t5), [t6] "=&v"(t6), [t7] "=&v"(t7)
               : [a0] "v"(a0), [a1] "v"(a1), [a2] "v"(a2), [a3] "v"(a3) : "memory");
}
DI void acc_store(unsigned a0, unsigned a1, unsigned a2, unsigned a3) {
  asm volatile(
               "ds_write2_b32 %[a0], v224, v225 offset0:0 offset1:64\n\t"
               "ds_write2_b32 %[a0], v226, v227 offset0:128 offset1:192\n\t"
               "ds_write2_b32 %[a0], v240, v241 offset0:32 offset1:96\n\t"
               "ds_write2_b32 %[a0], v242, v243 offset0:160 offset1:224\n\t"
               "ds_write2_b32 %[a1], v228, v229 offset0:0 offset1:64\n\t"
               "ds_write2_b32 %[a1], v230, v231 offset0:128 offset1:192\n\t"
               "ds_write2_b32 %[a1], v244, v245 offset0:32 offset1:96\n\t"
               "ds_write2_b32 %[a1], v246, v247 offset0:160 offset1:224\n\t"
               "ds_write2_b32 %[a2], v232, v233 offset0:0 offset1:64\n\t"
               "ds_write2_b32 %[a2], v234, v235 offset0:128 offset1:192\n\t"
               "ds_write2_b32 %[a2], v248, v249 offset0:32 offset1:96\n\t"
               "ds_write2_b32 %[a2], v250, v251 offset0:160 offset1:224\n\t"
               "ds_write2_b32 %[a3], v236, v237 offset0:0 offset1:64\n\t"
               "ds_write2_b32 %[a3], v238, v239 offset0:128 offset1:192\n\t"
               "ds_write2_b32 %[a3], v252, v253 offset0:32 offset1:96\n\t"
               "ds_write2_b32 %[a3], v254, v255 offset0:160 offset1:224\n\t"
               "s_waitcnt lgkmcnt(0)\n\t"
               "v_mov_b64 v[224:225], 0\n\t"
               "v_mov_b64 v[226:227], 0\n\t"
               "v_mov_b64 v[228:229], 0\n\t"
               "v_mov_b64 v[230:231], 0\n\t"
               "v_mov_b64 v[232:233], 0\n\t"
               "v_mov_b64 v[234:235], 0\n\t"
               "v_mov_b64 v[236:237], 0\n\t"
               "v_mov_b64 v[238:239], 0\n\t"
               "v_mov_b64 v[240:241], 0\n\t"
               "v_mov_b64 v[242:243], 0\n\t"
               "v_mov_b64 v[244:245], 0\n\t"
               "v_mov_b64 v[246:247], 0\n\t"
               "v_mov_b64 v[248:249], 0\n\t"
               "v_mov_b64 v[250:251], 0\n\t"
               "v_mov_b64 v[252:253], 0\n\t"
               "v_mov_b64 v[254:255], 0\n\t"
               "s_nop 7"
               :: [a0] "v"(a0), [a1] "v"(a1), [a2] "v"(a2), [a3] "v"(a3) : "memory");
}
DI void acc_gather_last(unsigned a0, unsigned a1, unsigned a2, unsigned a3) {
  asm volatile(
               "ds_read2_b32 v[176:177], %[a0] offset0:0 offset1:64\n\t"
               "ds_read2_b32 v[178:179], %[a0] offset0:128 offset1:192\n\t"
               "ds_read2_b32 v[180:181], %[a0] offset0:32 offset1:96\n\t"
               "ds_read2_b32 v[182:183], %[a0] offset0:160 offset1:224\n\t"
               "ds_read2_b32 v[184:185], %[a1] offset0:0 offset1:64\n\t"
               "ds_read2_b32 v[186:187], %[a1] offset0:128 offset1:192\n\t"
               "ds_read2_b32 v[188:189], %[a1] offset0:32 offset1:96\n\t"
               "ds_read2_b32 v[190:191], %[a1] offset0:160 offset1:224\n\t"
               "ds_read2_b32 v[192:193], %[a2] offset0:0 offset1:64\n\t"
               "ds_read2_b32 v[194:195], %[a2] offset0:128 offset1:192\n\t"
               "ds_read2_b32 v[196:197], %[a2] offset0:32 offset1:96\n\t"
               "ds_read2_b32 v[198:199], %[a2] offset0:160 offset1:224\n\t"
               "ds_read2_b32 v[200:201], %[a3] offset0:0 offset1:64\n\t"
               "ds_read2_b32 v[202:203], %[a3] offset0:128 offset1:192\n\t"
               "ds_read2_b32 v[204:205], %[a3] offset0:32 offset1:96\n\t"
               "ds_read2_b32 v[206:207], %[a3] offset0:160 offset1:224\n\t"
               "s_waitcnt lgkmcnt(12)\n\t"
               "v_pk_add_f32 v[224:225], v[224:225], v[176:177]\n\t"
               "v_pk_add_f32 v[226:227], v[226:227], v[178:179]\n\t"
               "v_pk_add_f32 v[240:241], v[240:241], v[180:181]\n\t"
               "v_pk_add_f32 v[242:243], v[242:243], v[182:183]\n\t"
               "s_waitcnt lgkmcnt(8)\n\t"
               "v_pk_add_f32 v[228:229], v[228:229], v[184:185]\n\t"
               "v_pk_add_f32 v[230:231], v[230:231], v[186:187]\n\t"
               "v_pk_add_f32 v[244:245], v[244:245], v[188:189]\n\t"
               "v_pk_add_f32 v[246:247], v[246:247], v[190:191]\n\t"
               "s_waitcnt lgkmcnt(4)\n\t"
               "v_pk_add_f32 v[232:233], v[232:233], v[192:193]\n\t"
               "v_pk_add_f32 v[234:235], v[234:235], v[194:195]\n\t"
               "v_pk_add_f32 v[248:249], v[248:249], v[196:197]\n\t"
               "v_pk_add_f32 v[250:251], v[250:251], v[198:199]\n\t"
               "s_waitcnt lgkmcnt(0)\n\t"
               "v_pk_add_f32 v[236:237], v[236:237], v[200:201]\n\t"
               "v_pk_add_f32 v[238:239], v[238:239], v[202:203]\n\t"
               "v_pk_add_f32 v[252:253], v[252:253], v[204:205]\n\t"
               "v_pk_add_f32 v[254:255], v[254:255], v[206:207]\n\t"
               "s_nop 0"
               :: [a0] "v"(a0), [a1] "v"(a1), [a2] "v"(a2), [a3] "v"(a3) : "memory");
}
DI void acc_store_last(unsigned a0, unsigned a1, unsigned a2, unsigned a3) {
  asm volatile(
               "ds_write2_b32 %[a0], v224, v225 offset0:0 offset1:64\n\t"
               "ds_write2_b32 %[a0], v226, v227 offset0:128 offset1:192\n\t"
               "ds_write2_b32 %[a0], v240, v241 offset0:32 offset1:96\n\t"
               "ds_write2_b32 %[a0], v242, v243 offset0:160 offset1:224\n\t"
               "ds_write2_b32 %[a1], v228, v229 offset0:0 offset1:64\n\t"
               "ds_write2_b32 %[a1], v230, v231 offset0:128 offset1:192\n\t"
               "ds_write2_b32 %[a1], v244, v245 offset0:32 offset1:96\n\t"
               "ds_write2_b32 %[a1], v246, v247 offset0:160 offset1:224\n\t"
               "ds_write2_b32 %[a2], v232, v233 offset0:0 offset1:64\n\t"
               "ds_write2_b32 %[a2], v234, v235 offset0:128 offset1:192\n\t"
               "ds_write2_b32 %[a2], v248, v249 offset0:32 offset1:96\n\t"
               "ds_write2_b32 %[a2], v250, v251 offset0:160 offset1:224\n\t"
               "ds_write2_b32 %[a3], v236, v237 offset0:0 offset1:64\n\t"
               "ds_write2_b32 %[a3], v238, v239 offset0:128 offset1:192\n\t"
               "ds_write2_b32 %[a3], v252, v253 offset0:32 offset1:96\n\t"
               "ds_write2_b32 %[a3], v254, v255 offset0:160 offset1:224\n\t"
               "s_nop 0"
               :: [a0] "v"(a0), [a1] "v"(a1), [a2] "v"(a2), [a3] "v"(a3) : "memory");
}
DI unsigned lds_lock(unsigned addr) {
  unsigned t, old;
  unsigned long long sv;
  asm volatile("s_mov_b64 %2, exec\n\ts_mov_b64 exec, 1\n"
               "1:\n\tds_wrxchg_rtn_b32 %0, %3, %4\n\ts_waitcnt lgkmcnt(0)\n\tv_cmp_eq_u32 vcc, 1, %0\n\ts_cbranch_vccz 2f\n\t"
               "s_sleep 1\n\ts_branch 1b\n"
               "2:\n\tv_readfirstlane_b32 %1, %0\n\ts_mov_b64 exec, %2\n\ts_nop 15"
               : "=&v"(t), "=&s"(old), "=&s"(sv) : "v"(addr), "v"(1u) : "vcc", "memory");
  return old;
}

template <typename T> DI T* karg(int k) {
  typedef __attribute__((address_space(4))) const unsigned long long* kp_t;
  kp_t kp = (kp_t)__builtin_amdgcn_kernarg_segment_ptr();
  asm volatile("" : "+s"(kp));
  return (T*)(__attribute__((address_space(1))) T*)kp[k];
}

__global__ __launch_bounds__(512) __attribute__((amdgpu_num_vgpr(88))) void edge_kernel(const u16* __restrict__ h_, const u16* __restrict__ pos_, const u16* __restrict__ we_,
                                                   const u16* __restrict__ wx_, const u16* __restrict__ wh_,
                                                   const char* __restrict__ ws_, u16* __restrict__ out_) {
  __shared__ __attribute__((aligned(1024))) char lds[L_END];
  AS3 char* L = (AS3 char*)lds;
  const unsigned LB = (unsigned)(size_t)L;
  const int tid_ = threadIdx.x;
  const int w = __builtin_amdgcn_readfirstlane(tid_ >> 6);
  STAMP(0, 1);
  const int xl = blockIdx.x & 7;
  const int b = xl * 31 + (xl < 2 ? xl : 2) + (blockIdx.x >> 3);
  const int n0 = b * BN;

  unsigned cw[2][2];

  int pa = 0, np = NP;
  bool staged = false;
  for (;;) {
    const u16 *h = karg<const u16>(0), *pos = karg<const u16>(1), *we = karg<const u16>(2), *wx = karg<const u16>(3);
    const char* ws = karg<const char>(5);
    const char *recs = ws + WS_RECS, *ents = ws + WS_ENT + (size_t)b * 16;
    int tid = tid_;
    asm volatile("" : "+v"(tid));
    const int lane = tid & 63, r = lane & 31, hf = lane >> 5;
    unsigned e3[4];
#pragma unroll
    for (int k = 0; k < 4; ++k) e3[k] = *(const unsigned*)(ents + (size_t)(64 * k + (tid >> 3)) * 4096 + 12);
    u32x3 ent = {0, 0, 0};
    if (w < 4) ent = *(const u32x3*)(ents + (size_t)tid * 4096);
    SB();
    if (!staged) {
      dma_piece((const char*)we + w * 1024, lane * 16, LB + L_W + w * 1024);
      if (w < 5) dma_piece((const char*)h + (size_t)n0 * 128 + w * 1024, lane * 16, LB + L_H + w * 1024);
    }
    const u32x4 w2raw = *(const u32x4*)(we + 64 * 64 + tid * 8);
    unsigned sraw[3], craw[4];
#pragma unroll
    for (int t = 0; t < 2; ++t) { craw[2 * t] = we[128 * 64 + 32 * t + r]; craw[2 * t + 1] = we[129 * 64 + 32 * t + r]; }
    {
      const u16* pp = w == 1 ? wx + lane : pos + (lane < BN ? n0 + lane : 0) * 3;
      const int st = w == 1 ? 0 : 1;
      sraw[0] = pp[0]; sraw[1] = pp[st]; sraw[2] = pp[2 * st];
    }
    SB();

    if (!staged) {
      const u32x4 z4 = {0, 0, 0, 0};
      static_assert(8 * S_WAVE == 2 * 8192 + 256 * 16 && BN * ACC_NODE == 5 * 8192 + 40 * 16, "clearing pattern");
      lds_st<u32x4>(L, L_S + tid * 16, z4);
      lds_st<u32x4>(L, L_S + 8192 + tid * 16, z4);
#pragma unroll
      for (int k = 0; k < 5; ++k) lds_st<u32x4>(L, L_ACC + k * 8192 + tid * 16, z4);
      if (tid < 256) lds_st<u32x4>(L, L_S + 16384 + tid * 16, z4);
      if (tid < 40) lds_st<u32x4>(L, L_ACC + 5 * 8192 + tid * 16, z4);
      if (tid < 8) lds_st<unsigned>(L, L_LOCK + 4 * tid, 0u);
    }
    wait_vm0();
    SB();
    STAMP(1, 1);

    unsigned cA = 0, cB = 0, cC = 0, cD = 0, iA = 0, iB = 0, iC = 0, iD = 0;
    if (w < 4) {
      if ((unsigned)(tid - pa) < (unsigned)np) {
        const unsigned s0 = ent[0] & 0xffffu, s1 = ent[0] >> 16, s2 = ent[1] & 0xffffu, s3 = ent[1] >> 16;
        const unsigned s4 = ent[2] & 0xffffu, s5 = ent[2] >> 16;
        cA = (s1 - s0) | ((s2 - s1) << 16);
        cB = (s3 - s2) | ((s4 - s3) << 16);
        cC = s5 - s4;
        cD = s5 - s0;
      }
      iA = wave_incl_scan(cA); iB = wave_incl_scan(cB); iC = wave_incl_scan(cC); iD = wave_incl_scan(cD);
      if (lane == 63) {
        lds_st<unsigned>(L, L_WT + 4 * w, iA); lds_st<unsigned>(L, L_WT + 16 + 4 * w, iB);
        lds_st<unsigned>(L, L_WT + 32 + 4 * w, iC); lds_st<unsigned>(L, L_WT + 48 + 4 * w, iD);
      }
    }
    SB();
    const unsigned j2 = 2 * (tid & 7);
    u32x4 ra[4], rb[4];
    unsigned cnt[4];
#pragma unroll
    for (int k = 0; k < 4; ++k) {
      const int p = 64 * k + (tid >> 3);
      cnt[k] = ((unsigned)(p - pa) < (unsigned)np) ? (e3[k] >> 16) : 0;
      ra[k] = u32x4{0, 0, 0, 0};
      if (j2 < cnt[k]) ra[k] = *(const u32x4*)(recs + ((size_t)p * CH + (e3[k] & 0xffffu) + j2) * 8);
    }
#pragma unroll
    for (int k = 0; k < 4; ++k) {
      const int p = 64 * k + (tid >> 3);
      rb[k] = u32x4{0, 0, 0, 0};
      if (j2 + 16 < cnt[k]) rb[k] = *(const u32x4*)(recs + ((size_t)p * CH + (e3[k] & 0xffffu) + j2 + 16) * 8);
    }
    SB();
    if (!staged) {
      u32x4 o;
#pragma unroll
      for (int k = 0; k < 4; ++k) o[k] = cvt_pk(bf_lo(w2raw[k]) * NLOG2E, bf_hi(w2raw[k]) * NLOG2E);
      lds_st<u32x4>(L, L_W + 8192 + tid * 16, o);
    }
    SB();
    STAMP(2, 1);

    __syncthreads();
    STAMP(3, 1);
    const u32x4 wA = lds_ld<u32x4>(L, L_WT), wB = lds_ld<u32x4>(L, L_WT + 16), wC = lds_ld<u32x4>(L, L_WT + 32), wD = lds_ld<u32x4>(L, L_WT + 48);
    const unsigned tA = wA[0] + wA[1] + wA[2] + wA[3], tB = wB[0] + wB[1] + wB[2] + wB[3];
    const unsigned tC = wC[0] + wC[1] + wC[2] + wC[3], tD = wD[0] + wD[1] + wD[2] + wD[3];
    unsigned tot[5] = {tA & 0xffffu, tA >> 16, tB & 0xffffu, tB >> 16, tC};
    unsigned base[6];
    base[0] = 0;
#pragma unroll
    for (int g = 0; g < 5; ++g) base[g + 1] = base[g] + ((tot[g] + 31) & ~31u);
    const bool fits = __builtin_amdgcn_readfirstlane((int)(tD <= (unsigned)CAP && base[5] <= (unsigned)CAP)) != 0;
    if (!fits) {
      np >>= 1;
      __syncthreads();
      continue;
    }
    if (w < 4) {
      const unsigned pA = iA - cA + (w > 0 ? wA[0] : 0) + (w > 1 ? wA[1] : 0) + (w > 2 ? wA[2] : 0);
      const unsigned pB = iB - cB + (w > 0 ? wB[0] : 0) + (w > 1 ? wB[1] : 0) + (w > 2 ? wB[2] : 0);
      const unsigned pC = iC - cC + (w > 0 ? wC[0] : 0) + (w > 1 ? wC[1] : 0) + (w > 2 ? wC[2] : 0);
      const unsigned s0 = ent[0] & 0xffffu, s1 = ent[0] >> 16, s2 = ent[1] & 0xffffu, s3 = ent[1] >> 16, s4 = ent[2] & 0xffffu;
      const unsigned v0 = L_RAW + 8 * (base[0] + (pA & 0xffffu));
      const unsigned v1 = L_RAW + 8 * (base[1] + (pA >> 16) - (s1 - s0));
      const unsigned v2 = L_RAW + 8 * (base[2] + (pB & 0xffffu) - (s2 - s0));
      const unsigned v3 = L_RAW + 8 * (base[3] + (pB >> 16) - (s3 - s0));
      const unsigned v4 = L_RAW + 8 * (base[4] + pC - (s4 - s0));
      lds_st<u32x4>(L, L_TBL + 16 * tid, u32x4{(v0 & 0xffffu) | (v1 << 16), (v2 & 0xffffu) | (v3 << 16), v4 & 0xffffu, 0});
    } else {
      const int t = tid - 256;
      const u32x2 pad = {(unsigned)(L_PD + 16 * BN) << 16, 0};
      if (t < 160) {
        const int g = t >> 5, k = t & 31;
        const unsigned bg = g == 0 ? base[0] : g == 1 ? base[1] : g == 2 ? base[2] : g == 3 ? base[3] : base[4];
        const unsigned bn = g == 0 ? base[1] : g == 1 ? base[2] : g == 2 ? base[3] : g == 3 ? base[4] : base[5];
        const unsigned tg = g == 0 ? tot[0] : g == 1 ? tot[1] : g == 2 ? tot[2] : g == 3 ? tot[3] : tot[4];
        const unsigned slot = bg + tg + k;
        if (slot < bn) lds_st<u32x2>(L, L_RAW + 8 * slot, pad);
      }
      if (t < PADS) lds_st<u32x2>(L, L_RAW + 8 * (base[5] + t), pad);
    }
    if (!staged) {
      staged = true;
      if (w >= 4) {
        const int u = (w - 4) >> 1, t = w & 1;
        f32x16 pacc;
#pragma unroll
        for (int i = 0; i < 16; ++i) pacc[i] = 0.f;
#pragma unroll
        for (int s = 0; s < 4; ++s) {
          const int nl = 32 * u + r, nlc = nl < BN ? nl : BN - 1;
          const bf16x8 ha = lds_ld<bf16x8>(L, L_H + nlc * 128 + 32 * s + 16 * hf);
          const unsigned o = L_W + (16 * s + 8 * (lane >> 5) + ((lane & 15) >> 2)) * 128 + (32 * t + 16 * ((lane >> 4) & 1) + 4 * (lane & 3)) * 2;
          const u32x2 b0 = lds_tr(L, o), b1 = lds_tr(L, o + 512);
          pacc = mfma(ha, as_frag(u32x4{b0[0], b0[1], b1[0], b1[1]}), pacc);
        }
#pragma unroll
        for (int q = 0; q < 4; ++q)
          if (4 * u + q < 5)
            lds_st<u32x2>(L, L_PFR + ((4 * u + q) * 2 + t) * 512 + lane * 8,
                          u32x2{cvt_pk(pacc[4 * q] * NLOG2E, pacc[4 * q + 1] * NLOG2E), cvt_pk(pacc[4 * q + 2] * NLOG2E, pacc[4 * q + 3] * NLOG2E)});
      } else if (w == 0) {
        if (lane <= BN)
          lds_st<f32x4>(L, L_PD + 16 * lane, f32x4{bf_lo(sraw[0]), bf_lo(sraw[1]), bf_lo(sraw[2]), lane < BN ? 1.0f : 0.0f});
      } else if (w == 1) {
        lds_st<float>(L, L_WX + 4 * lane, bf_lo(sraw[0]));
      }
#pragma unroll
      for (int t = 0; t < 2; ++t) {
        const float d = bf_lo(craw[2 * t]) * NLOG2E, a = bf_lo(craw[2 * t + 1]) * NLOG2E;
        const unsigned hi = cvt_pk(d, a);
        cw[t][0] = hi;
        cw[t][1] = cvt_pk(d - bf_lo(hi), a - bf_hi(hi));
      }
    }
    __syncthreads();
    STAMP(4, 1);

    {
      unsigned a0[4], a1[4];
#pragma unroll
      for (int k = 0; k < 4; ++k) {
        const unsigned tb = L_TBL + 16 * (64 * k + (tid >> 3));
        a0[k] = lds_ld<u16>(L, tb + 2 * ((ra[k][0] >> 23) & 7)) + 8 * j2;
        a1[k] = lds_ld<u16>(L, tb + 2 * ((ra[k][2] >> 23) & 7)) + 8 * j2 + 8;
      }
#pragma unroll
      for (int k = 0; k < 4; ++k) {
        if (j2 < cnt[k]) lds_st<u32x2>(L, a0[k], u32x2{ra[k][0], ra[k][1]});
        if (j2 + 1 < cnt[k]) lds_st<u32x2>(L, a1[k], u32x2{ra[k][2], ra[k][3]});
      }
#pragma unroll
      for (int k = 0; k < 4; ++k) {
        if (j2 + 16 < cnt[k]) {
          const unsigned tb = L_TBL + 16 * (64 * k + (tid >> 3));
          const unsigned b0 = lds_ld<u16>(L, tb + 2 * ((rb[k][0] >> 23) & 7)) + 8 * (j2 + 16);
          const unsigned b1 = lds_ld<u16>(L, tb + 2 * ((rb[k][2] >> 23) & 7)) + 8 * (j2 + 17);
          lds_st<u32x2>(L, b0, u32x2{rb[k][0], rb[k][1]});
          if (j2 + 17 < cnt[k]) lds_st<u32x2>(L, b1, u32x2{rb[k][2], rb[k][3]});
        }
      }
#pragma unroll
      for (int k = 0; k < 4; ++k) {
        for (unsigned jj = (tid & 7) + 32; jj < cnt[k]; jj += 8) {
          const int p = 64 * k + (tid >> 3);
          const u32x2 rec = *(const u32x2*)(recs + ((size_t)p * CH + (e3[k] & 0xffffu) + jj) * 8);
          const unsigned a = lds_ld<u16>(L, L_TBL + 16 * p + 2 * ((rec[0] >> 23) & 7));
          lds_st<u32x2>(L, a + 8 * jj, rec);
        }
      }
    }
    __syncthreads();
    STAMP(5, 1);

    const int T = __builtin_amdgcn_readfirstlane((int)(base[5] >> 5));
    const int tq8 = T >> 3, trem = T & 7;
    const int ta = w * tq8 + (w < trem ? w : trem);
    const int tb = ta + tq8 + (w < trem ? 1 : 0);
    const int tg1 = __builtin_amdgcn_readfirstlane((int)(base[1] >> 5)), tg2 = __builtin_amdgcn_readfirstlane((int)(base[2] >> 5));
    const int tg3 = __builtin_amdgcn_readfirstlane((int)(base[3] >> 5)), tg4 = __builtin_amdgcn_readfirstlane((int)(base[4] >> 5));

    if (ta < tb) {
      const char *qtab = uni_ptr((const char*)h), *pos4 = ws + WS_POS4;
      const unsigned ringb = L_RING + w * 4096;
      unsigned aq[4];
#pragma unroll
      for (int s = 0; s < 4; ++s) aq[s] = ringb + r * 128 + (((2 * s + hf) ^ ((r >> 1) & 7)) << 4);
      const unsigned dc0 = ((lane & 7) ^ ((lane >> 4) & 7)) << 4, dc1 = ((lane & 7) ^ ((4 + (lane >> 4)) & 7)) << 4;
      const unsigned Sb = L_S + w * S_WAVE;
      const unsigned pi_e = (r & 0x13) | ((r & 4) << 1) | ((r & 8) >> 1);
      const unsigned sa_c = Sb + 2 * pi_e;
      const unsigned s_rd = Sb + r * 80 + 16 * hf;
      const int g16 = lane >> 4, tq = (lane & 15) >> 2, tp = lane & 3, tu = g16 & 1, th = g16 >> 1;
      const unsigned s_tr = Sb + (4 * (4 * th + tq) + 3) * 80 + 2 * (16 * tu + 8 * (tp & 1) + 4 * (tp >> 1));
      const unsigned acc_l = L_ACC + hf * ACC_NODE + 4 * r;
      bf16x8 wq[2][4];
      auto group_of = [&](int t) { return __builtin_amdgcn_readfirstlane((t >= tg1) + (t >= tg2) + (t >= tg3) + (t >= tg4)); };
      auto group_end = [&](int g) { return __builtin_amdgcn_readfirstlane(g == 0 ? tg1 : g == 1 ? tg2 : g == 2 ? tg3 : g == 3 ? tg4 : T); };
      int gend = group_end(group_of(ta));
      int tflush = -1;

      bf16x8 bxA0, bxA1, bxB0, bxB1;
      auto load_bx = [&](int gg) {
        const u32x2 p0 = lds_ld<u32x2>(L, L_PFR + gg * 1024 + lane * 8), p1 = lds_ld<u32x2>(L, L_PFR + gg * 1024 + 512 + lane * 8);
        bxA0 = as_frag(u32x4{p0[0], p0[1], hf ? 0u : cw[0][0], hf ? 0u : cw[0][1]});
        bxB0 = as_frag(u32x4{p0[0], p0[1], hf ? cw[0][0] : 0u, hf ? cw[0][1] : 0u});
        bxA1 = as_frag(u32x4{p1[0], p1[1], hf ? 0u : cw[1][0], hf ? 0u : cw[1][1]});
        bxB1 = as_frag(u32x4{p1[0], p1[1], hf ? cw[1][0] : 0u, hf ? cw[1][1] : 0u});
        asm volatile("" : "+v"(bxA0), "+v"(bxB0), "+v"(bxA1), "+v"(bxB1));
      };
      auto flush = [&](int gg) {
        const unsigned a = LB + acc_l + gg * (8 * ACC_NODE);
        if (lds_lock(LB + L_LOCK + 4 * gg) != 0) acc_gather(a, a + 2 * ACC_NODE, a + 4 * ACC_NODE, a + 6 * ACC_NODE);
        acc_store(a, a + 2 * ACC_NODE, a + 4 * ACC_NODE, a + 6 * ACC_NODE);
        lds_st<unsigned>(L, L_LOCK + 4 * gg, 2u);
        asm volatile("" ::: "memory");
      };
      auto flush_last = [&](int gg) {
        const unsigned a = LB + acc_l + gg * (8 * ACC_NODE);
        if (lds_lock(LB + L_LOCK + 4 * gg) != 0) acc_gather_last(a, a + 2 * ACC_NODE, a + 4 * ACC_NODE, a + 6 * ACC_NODE);
        acc_store_last(a, a + 2 * ACC_NODE, a + 4 * ACC_NODE, a + 6 * ACC_NODE);
        lds_st<unsigned>(L, L_LOCK + 4 * gg, 2u);
        asm volatile("" ::: "memory");
      };
      struct Idx { unsigned i0, i1, i2, i3; };
      unsigned a_idx = L_RAW + ta * 256 + (lane >> 3) * 8, a_raw = L_RAW + ta * 256 + lane * 8;
      auto idx_load = [&](int dt) {
        const unsigned a = a_idx + dt * 256;
        return Idx{lds_ld<u16>(L, a), lds_ld<u16>(L, a + 64), lds_ld<u16>(L, a + 128), lds_ld<u16>(L, a + 192)};
      };
      auto issue_rows = [&](const Idx& x) {
        dma_tile(qtab, x.i0 * 128 + dc0, x.i1 * 128 + dc1, x.i2 * 128 + dc0, x.i3 * 128 + dc1);
      };
      auto raw_load = [&](int dt) {
        const unsigned a = a_raw + dt * 256;
        return Raw{lds_ld<u16>(L, a), lds_ld<u16>(L, a + 2), lds_ld<unsigned>(L, a + 4), lds_ld<u16>(L, a + 4)};
      };
      auto pos_load = [&](const Raw& x) {
        u32x2 ps;
        asm volatile("global_load_dwordx2 %0, %1, %2" : "=v"(ps) : "v"(x.src * 8u), "s"(pos4) : "memory");
        return ps;
      };
      auto wait_ps = [&](u32x2& ps) { asm volatile("s_waitcnt vmcnt(0)" : "+v"(ps) :: "memory"); };
      auto pd_load = [&](const Raw& x) { return lds_ld<f32x4>(L, x.pda); };
      auto prep = [&](const Raw& x, const u32x2& ps, const f32x4& pd) {
        const float dx = pd[0] - bf_lo(ps[0]), dy = pd[1] - bf_hi(ps[0]), dz = pd[2] - as_f(ps[1]);
        unsigned wa = cvt_pk(dx, dy), wb = cvt_pk(dz, pd[3]);
        const float dzr = bf_lo(wb);
        const bf16x2v wav = __builtin_bit_cast(bf16x2v, wa);
        const float d2 = __builtin_amdgcn_fdot2_f32_bf16(wav, wav, dzr * dzr, false);
        Prep o;
        o.de = cvt_pk(d2, as_f(x.w1));
        asm volatile("" : "+v"(o.de), "+v"(wa), "+v"(wb));
        const unsigned P = x.n320 + sa_c, Q = P + 160;
        const auto sa = __builtin_amdgcn_permlane32_swap(P, Q, false, false);
        const auto sd = __builtin_amdgcn_permlane32_swap(wa, wb, false, false);
        o.saA = sa[0]; o.saB = sa[1]; o.sdA = sd[0]; o.sdB = sd[1];
        return o;
      };

      struct Ops { bf16x8 q0, q1, q2, q3, as0, as1, ax; };
      auto read_ops = [&](unsigned sa, unsigned sd, unsigned de) {
        Ops o;
        o.q0 = lds_ld<bf16x8>(L, aq[0]); o.q1 = lds_ld<bf16x8>(L, aq[1]);
        o.q2 = lds_ld<bf16x8>(L, aq[2]); o.q3 = lds_ld<bf16x8>(L, aq[3]);
        lds_st<u16>(L, sa, (u16)sd);
        lds_st<u16>(L, sa + 80, (u16)(sd >> 16));
        const u32x2 oh = lds_tr(L, s_tr);
        o.as0 = lds_ld<bf16x8>(L, s_rd);
        o.as1 = lds_ld<bf16x8>(L, s_rd + 32);
        lds_st<u16>(L, sa, (u16)0);
        lds_st<u16>(L, sa + 80, (u16)0);
        unsigned de2;
        asm volatile("v_mov_b32 %0, %1" : "=v"(de2) : "v"(de));
        o.ax = as_frag(u32x4{oh[0], oh[1], de, de2});
        return o;
      };
      auto alpha = [&](int t, const Ops& c, const bf16x8& asl, const bf16x8& bx1, auto&& mid) {
        alpha1(asl);
        if (__builtin_expect(t == tflush, 0)) flush(group_of(t - 1));
        alpha2(c.ax, bx1, c.q0, wq[1][0]);
        SB(); mid(); SB();
        alpha3(c.q1, wq[1][1], c.q2, wq[1][2], c.q3, wq[1][3], c.as0);
      };
      auto beta = [&](const Ops& c, const Ops& n, const bf16x8& bx0n, auto&& mid) {
        beta1(c.as1);
        beta2(n.ax, bx0n, n.q0, wq[0][0]);
        SB(); mid(); SB();
        beta3(n.q1, wq[0][1], n.q2, wq[0][2], n.q3, wq[0][3], c.as0);
      };
      auto next_group = [&](int t) {
        if (__builtin_expect(t == gend, 0)) { const int g = group_of(t); gend = group_end(g); tflush = t; load_bx(g); }
      };

      set_m0(LB + ringb);
      issue_rows(idx_load(0));
      Raw rw = raw_load(0);
      u32x2 ps = pos_load(rw);
      f32x4 pd = pd_load(rw);
      SB();
#pragma unroll
      for (int t = 0; t < 2; ++t)
#pragma unroll
        for (int s = 0; s < 4; ++s) {
          const unsigned o = L_W + 8192 + (16 * s + 8 * th + tq) * 128 + (32 * t + 16 * tu + 4 * tp) * 2;
          const u32x2 b0 = lds_tr(L, o), b1 = lds_tr(L, o + 512);
          wq[t][s] = as_frag(u32x4{b0[0], b0[1], b1[0], b1[1]});
        }
      load_bx(group_of(ta));
      tiles_init();
      SB();
      wait_ps(ps);
      Prep pr = prep(rw, ps, pd);
      rw = raw_load(2);
      Ops A = read_ops(pr.saA, pr.sdA, pr.de), B;
      B.as1 = A.as1;
      const Idx ix0 = idx_load(1);
      chain_y0(A.ax, bxA0, A.q0, wq[0][0], A.q1, wq[0][1], A.q2, wq[0][2], A.q3, wq[0][3]);
      asm volatile("s_waitcnt lgkmcnt(0)" ::: "memory");
      issue_rows(ix0);
      STAMP(6, 1);
      CSTAMP(10, 1);
      for (int t = ta;; t += 2) {
        alpha(t, A, B.as1, bxA1, [&]() { ps = pos_load(rw); });
        wait_ps(ps);
        next_group(t + 1);
        const Idx ixa = idx_load(2);
        B = read_ops(pr.saB, pr.sdB, pr.de);
        pd = pd_load(rw);
        SB();
        beta(A, B, bxB0, [&]() { issue_rows(ixa); });
        if (__builtin_expect(t + 1 >= tb, 0)) { last_block(A.as1); flush_last(group_of(t)); break; }
        alpha(t + 1, B, A.as1, bxB1, [&]() { pr = prep(rw, ps, pd); });
        wait_vm0();
        next_group(t + 2);
        const Idx ixb = idx_load(3);
        A = read_ops(pr.saA, pr.sdA, pr.de);
        rw = raw_load(4);
        SB();
        beta(B, A, bxA0, [&]() { issue_rows(ixb); });
        if (__builtin_expect(t + 2 >= tb, 0)) { last_block(B.as1); flush_last(group_of(t + 1)); break; }
        a_idx += 512;
        a_raw += 512;
      }
      wait_vm0();
      const char* wh = karg<const char>(4);
      dma_piece(wh + w * 2048, lane * 16, LB + ringb);
      dma_piece(wh + w * 2048 + 1024, lane * 16, LB + ringb + 1024);
      STAMP(7, 1);
      CSTAMP(11, 1);
    } else {
      const char* wh = karg<const char>(4);
      dma_piece(wh + w * 2048, lane * 16, LB + L_RING + w * 4096);
      dma_piece(wh + w * 2048 + 1024, lane * 16, LB + L_RING + w * 4096 + 1024);
    }
    pa += np;
    if (pa >= NP) break;
    wait_vm0();
    __syncthreads();
  }
  wait_vm0();
  __syncthreads();
  STAMP(8, 1);

  u16* out = karg<u16>(6);
  int tid = tid_;
  asm volatile("" : "+v"(tid));
  const int lane = tid & 63, r = lane & 31, hf = lane >> 5;
  if (w < 4) {
    const int g16 = lane >> 4, tq = (lane & 15) >> 2, tp = lane & 3, tu = g16 & 1, th = g16 >> 1;
    const int u = w >> 1, t = w & 1;
    const int nl = 32 * u + r, nlc = nl < BN ? nl : BN - 1;
    const unsigned hrow = L_H + nlc * 128;
    f32x16 d;
#pragma unroll
    for (int i = 0; i < 16; ++i) d[i] = 0.f;
    const unsigned w_tr = L_RING + (8 * th + tq) * 128 + (32 * t + 16 * tu + 4 * tp) * 2;
#pragma unroll
    for (int s = 0; s < 8; ++s) {
      const u32x2 a0 = lds_tr(L, w_tr + s * 4096), a1 = lds_tr(L, w_tr + s * 4096 + 512);
      const bf16x8 a = as_frag(u32x4{a0[0], a0[1], a1[0], a1[1]});
      bf16x8 bb;
      if (s < 4) {
        bb = lds_ld<bf16x8>(L, hrow + 32 * s + 16 * hf);
      } else {
        const unsigned o = L_ACC + nlc * ACC_NODE + 3 * 256 + (16 * (s - 4) + 8 * hf) * 4;
        const f32x4 m0 = lds_ld<f32x4>(L, o) * NLN2, m1 = lds_ld<f32x4>(L, o + 16) * NLN2;
        bb = as_frag(u32x4{cvt_pk(m0[0], m0[1]), cvt_pk(m0[2], m0[3]), cvt_pk(m1[0], m1[1]), cvt_pk(m1[2], m1[3])});
      }
      d = mfma(a, bb, d);
    }
    if (nl < BN) {
#pragma unroll
      for (int q = 0; q < 4; ++q) {
        const int f = 32 * t + 8 * q + 4 * hf;
        const u32x2 hv = lds_ld<u32x2>(L, hrow + 2 * f);
        float o4[4];
#pragma unroll
        for (int k = 0; k < 4; ++k) {
          const float act = bf_round(silu_f(bf_round(d[4 * q + k])));
          const float hres = (k & 1) ? bf_hi(hv[k >> 1]) : bf_lo(hv[k >> 1]);
          o4[k] = hres + act;
        }
        *(u32x2*)(out + (size_t)(n0 + nl) * 64 + f) = u32x2{cvt_pk(o4[0], o4[1]), cvt_pk(o4[2], o4[3])};
      }
    }
  } else {
    const int idx = tid - 256, nc = idx >> 1, half = idx & 1;
    const int ncc = nc < 3 * BN ? nc : 3 * BN - 1;
    const int n = ncc / 3, c = ncc - 3 * n;
    const unsigned o = L_ACC + n * ACC_NODE + c * 256 + half * 128;
    float s = 0.f;
#pragma unroll
    for (int k = 0; k < 8; ++k) {
      const f32x4 v = lds_ld<f32x4>(L, o + 16 * k), x = lds_ld<f32x4>(L, L_WX + half * 128 + 16 * k);
      s += v[0] * x[0] + v[1] * x[1] + v[2] * x[2] + v[3] * x[3];
    }
    s += __builtin_bit_cast(float, __builtin_amdgcn_update_dpp(0, __builtin_bit_cast(int, s), 0xb1, 0xf, 0xf, true));
    if (nc < 3 * BN && half == 0) {
      const float p0 = lds_ld<float>(L, L_PD + 16 * n + 4 * c);
      out[(size_t)NN * 64 + (size_t)n0 * 3 + nc] = (u16)cvt_pk(p0 + bf_round(s * NLN2), 0.f);
    }
  }
  STAMP(9, 1);
}

}

extern "C" void kernel_launch(void* const* d_in, const int* in_sizes, int n_in, void* d_out, int out_size, void* d_ws, size_t ws_size,
                              hipStream_t stream) {
  const u16* h = (const u16*)d_in[0];
  const u16* pos = (const u16*)d_in[1];
  const u16* ea = (const u16*)d_in[2];
  const int* src = (const int*)d_in[3];
  const int* dst = (const int*)d_in[4];
  const u16* we = (const u16*)d_in[5];
  const u16* wx = (const u16*)d_in[6];
  const u16* wh = (const u16*)d_in[7];
  sort_kernel<<<NP, K1_THREADS, 0, stream>>>(src, dst, ea, pos, (char*)d_ws);
  edge_kernel<<<NBK, 512, 0, stream>>>(h, pos, we, wx, wh, (const char*)d_ws, (u16*)d_out);
}
```

```cpp
#include <hip/hip_runtime.h>
#include <hip/hip_bf16.h>
#include <stdint.h>

#define DI __device__ __forceinline__
#define AS3 __attribute__((address_space(3)))
#define SB() __builtin_amdgcn_sched_barrier(0)

typedef short bf16x8 __attribute__((ext_vector_type(8)));
typedef short s16x4 __attribute__((ext_vector_type(4)));
typedef float f32x16 __attribute__((ext_vector_type(16)));
typedef float f32x4 __attribute__((ext_vector_type(4)));
typedef float f32x2 __attribute__((ext_vector_type(2)));
typedef unsigned u32x4 __attribute__((ext_vector_type(4)));
typedef unsigned u32x3 __attribute__((ext_vector_type(3)));
typedef unsigned u32x2 __attribute__((ext_vector_type(2)));
typedef int i32x4 __attribute__((ext_vector_type(4)));
typedef __bf16 bf16x2v __attribute__((ext_vector_type(2)));
typedef unsigned short u16;

namespace {

constexpr int NN = 10000;
constexpr int NE = 640000;
constexpr int BN = 40;
constexpr int NBK = NN / BN;
constexpr int NP = NBK;
constexpr int CH = NE / NP;
static_assert(CH == 5 * 512 && CH < 4096, "five edges per thread, 12-bit run starts");
constexpr float NLOG2E = -1.4426950408889634f;
constexpr float NLN2 = -0.6931471805599453f;
constexpr unsigned SPIN_LIMIT = 1u << 20;
#define POLL_SLEEP 1

constexpr unsigned WS_CTL = 0;
constexpr unsigned WS_ENT = 256;
constexpr unsigned WS_RECS = WS_ENT + NBK * 4096;
constexpr unsigned WS_END = WS_RECS + NP * CH * 8;
static_assert(WS_RECS % 128 == 0 && (CH * 8) % 128 == 0, "no cache line holds records of two producers");

constexpr int CAP = 2880;
constexpr int PADS = 160;
constexpr int L_S = 0;
constexpr int S_WAVE = 2560;
constexpr int L_PD = 20480;
constexpr int L_WT = 21248;
constexpr int L_LOCK = 21312;
constexpr int L_TAG = 21344;
constexpr int L_WX = 21504;
constexpr int L_TBL = 21760;
constexpr int L_RAW = L_TBL + 4096;
constexpr int L_PFR = L_RAW + (CAP + PADS) * 8;
constexpr int L_RING = L_PFR + 5120;
constexpr int L_ACC = L_RING + 8 * 4096;
constexpr int ACC_NODE = 1040;
constexpr int L_W = L_ACC + BN * ACC_NODE;
constexpr int L_H = L_W + 16384;
constexpr int L_SEG = L_H + BN * 128;
constexpr int L_END = L_SEG + 1024;
static_assert(L_PD % 1024 == 0, "group id is read from bits 7..9 of the pos address");
static_assert(L_RAW + (CAP + PADS) * 8 < 65536 && L_RAW >= 8 * CH && CAP + PADS >= CH, "run table holds 16-bit LDS addresses");
static_assert(L_PFR % 16 == 0 && L_RING % 16 == 0 && L_ACC % 16 == 0 && L_W % 16 == 0 && L_END <= 163840, "LDS map");

DI unsigned cvt_pk(float a, float b) { f32x2 v = {a, b}; return __builtin_bit_cast(unsigned, __builtin_convertvector(v, bf16x2v)); }
DI float as_f(unsigned u) { return __builtin_bit_cast(float, u); }
DI float bf_lo(unsigned u) { return __builtin_bit_cast(float, u << 16); }
DI float bf_hi(unsigned u) { return __builtin_bit_cast(float, u & 0xffff0000u); }
DI float bf_round(float x) { return bf_lo(cvt_pk(x, x)); }
DI float bf_ld(const u16* p) { return __builtin_bit_cast(float, (unsigned)*p << 16); }
DI f32x16 mfma(bf16x8 a, bf16x8 b, f32x16 c) { return __builtin_amdgcn_mfma_f32_32x32x16_bf16(a, b, c, 0, 0, 0); }
DI bf16x8 as_frag(u32x4 v) { return __builtin_bit_cast(bf16x8, v); }

DI unsigned wave_incl_scan(unsigned x) {
  x += __builtin_amdgcn_update_dpp(0, (int)x, 0x111, 0xf, 0xf, true);
  x += __builtin_amdgcn_update_dpp(0, (int)x, 0x112, 0xf, 0xf, true);
  x += __builtin_amdgcn_update_dpp(0, (int)x, 0x114, 0xf, 0xf, true);
  x += __builtin_amdgcn_update_dpp(0, (int)x, 0x118, 0xf, 0xf, true);
  x += __builtin_amdgcn_update_dpp(0, (int)x, 0x142, 0xa, 0xf, false);
  x += __builtin_amdgcn_update_dpp(0, (int)x, 0x143, 0xc, 0xf, false);
  return x;
}

template <typename T> DI T lds_ld(AS3 char* L, unsigned off) { return *(AS3 T*)(L + off); }
template <typename T> DI void lds_st(AS3 char* L, unsigned off, T v) { *(AS3 T*)(L + off) = v; }
DI u32x2 lds_tr(AS3 char* L, unsigned off) {
  return __builtin_bit_cast(u32x2, __builtin_amdgcn_ds_read_tr16_b64_v4i16((AS3 s16x4*)(L + off)));
}

DI const char* uni_ptr(const char* p) {
  const unsigned long long v = (unsigned long long)p;
  const unsigned lo = __builtin_amdgcn_readfirstlane((int)(unsigned)v), hi = __builtin_amdgcn_readfirstlane((int)(unsigned)(v >> 32));
  return (const char*)(__attribute__((address_space(1))) const char*)(((unsigned long long)hi << 32) | lo);
}
DI void dma_piece(const char* base_, unsigned voff, unsigned ldsaddr_) {
  const char* base = uni_ptr(base_);
  const unsigned ldsaddr = __builtin_amdgcn_readfirstlane((int)ldsaddr_);
  unsigned keep;
  asm volatile("s_mov_b32 %0, m0\n\ts_mov_b32 m0, %3\n\ts_nop 0\n\tglobal_load_lds_dwordx4 %1, %2\n\ts_mov_b32 m0, %0"
               : "=&s"(keep) : "v"(voff), "s"(base), "s"(ldsaddr) : "memory");
}
DI void set_m0(unsigned ldsaddr) { asm volatile("s_mov_b32 m0, %0\n\ts_nop 0" :: "s"(ldsaddr) : "memory"); }
DI void dma_tile(const char* base, unsigned v0, unsigned v1, unsigned v2, unsigned v3) {
  asm volatile("global_load_lds_dwordx4 %0, %4\n\t"
               "global_load_lds_dwordx4 %1, %5 offset:1024\n\t"
               "global_load_lds_dwordx4 %2, %6 offset:2048\n\t"
               "global_load_lds_dwordx4 %3, %7 offset:3072"
               :: "v"(v0), "v"(v1), "v"(v2), "v"(v3), "s"(base), "s"(base - 1024), "s"(base - 2048), "s"(base - 3072)
               : "memory");
}
#define STAMP(k, kern)
#define CSTAMP(k, kern)
typedef __amdgpu_buffer_rsrc_t rsrc_t;
DI rsrc_t ws_rsrc(const char* ws) { return __builtin_amdgcn_make_buffer_rsrc((void*)ws, 0, WS_END, 0x00020000); }
#define LD_AUX 16
#define ST_AUX 16
DI u32x4 ld16_dev(rsrc_t r, unsigned off) { return __builtin_amdgcn_raw_buffer_load_b128(r, off, 0, 16); }
DI u32x4 ld16_rec(rsrc_t r, unsigned off) { return __builtin_amdgcn_raw_buffer_load_b128(r, off, 0, LD_AUX); }
DI u32x2 ld8_dev(rsrc_t r, unsigned off) { return __builtin_amdgcn_raw_buffer_load_b64(r, off, 0, 16); }
DI void st16_dev(rsrc_t r, unsigned off, u32x4 v) { __builtin_amdgcn_raw_buffer_store_b128(v, r, off, 0, 16); }
DI void st16_rec(rsrc_t r, unsigned off, u32x4 v) { __builtin_amdgcn_raw_buffer_store_b128(v, r, off, 0, ST_AUX); }
DI void wait_vm0() { asm volatile("s_waitcnt vmcnt(0)" ::: "memory"); }

struct Prep {
  unsigned sdA, sdB;
  unsigned saA, saB;
  unsigned de;
};
struct Raw { unsigned src, pda, w1, n320; };

DI float silu_f(float x) { return x * __builtin_amdgcn_rcpf(1.0f + __builtin_amdgcn_exp2f(x * NLOG2E)); }

#pragma clang diagnostic ignored "-Winline-asm"
#define T4(op, d, s0, s1, s2, s3, d1, d2, d3) op " v" #d ", v" #s0 "\n\t" op " v" #d1 ", v" #s1 "\n\t" op " v" #d2 ", v" #s2 "\n\t" op " v" #d3 ", v" #s3 "\n\t"
#define EXP_LO(a, b, c, d) T4("v_exp_f32", 184, a, b, c, d, 185, 186, 187)
#define EXP_HI(a, b, c, d) T4("v_exp_f32", 188, a, b, c, d, 189, 190, 191)
#define RCP_LO T4("v_rcp_f32", 184, 184, 185, 186, 187, 185, 186, 187)
#define RCP_HI T4("v_rcp_f32", 188, 188, 189, 190, 191, 189, 190, 191)
#define PKA(a, b) "v_pk_add_f32 v[" #a ":" #b "], v[" #a ":" #b "], 1.0 op_sel_hi:[1,0]\n\t"
#define ADD1 PKA(184, 185) PKA(186, 187) PKA(188, 189) PKA(190, 191)
#define PKM(a, b, c, d) "v_pk_mul_f32 v[" #a ":" #b "], v[" #a ":" #b "], v[" #c ":" #d "]\n\t"
#define MULY(a0, a1, a2, a3, a4, a5, a6, a7) PKM(184, 185, a0, a1) PKM(186, 187, a2, a3) PKM(188, 189, a4, a5) PKM(190, 191, a6, a7)
#define CVT(d, a, b) "v_cvt_pk_bf16_f32 v" #d ", v" #a ", v" #b "\n\t"
#define PACK(d0, d1, d2, d3) CVT(d0, 184, 185) CVT(d1, 186, 187) CVT(d2, 188, 189) CVT(d3, 190, 191)
#define MF(acc, a, b) "v_mfma_f32_32x32x16_bf16 " acc ", " a ", " b ", " acc "\n\t"
#define MF0(acc, a, b) "v_mfma_f32_32x32x16_bf16 " acc ", " a ", " b ", 0\n\t"
#define Y0 "v[192:207]"
#define Y1 "v[208:223]"
#define ACC0 "v[224:239]"
#define ACC1 "v[240:255]"
#define X0 "v[176:179]"
#define X1 "v[180:183]"

DI void tiles_init() {
  asm volatile(
               "v_mov_b64 v[224:225], 0\n\t"
               "v_mov_b64 v[226:227], 0\n\t"
               "v_mov_b64 v[228:229], 0\n\t"
               "v_mov_b64 v[230:231], 0\n\t"
               "v_mov_b64 v[232:233], 0\n\t"
               "v_mov_b64 v[234:235], 0\n\t"
               "v_mov_b64 v[236:237], 0\n\t"
               "v_mov_b64 v[238:239], 0\n\t"
               "v_mov_b64 v[240:241], 0\n\t"
               "v_mov_b64 v[242:243], 0\n\t"
               "v_mov_b64 v[244:245], 0\n\t"
               "v_mov_b64 v[246:247], 0\n\t"
               "v_mov_b64 v[248:249], 0\n\t"
               "v_mov_b64 v[250:251], 0\n\t"
               "v_mov_b64 v[252:253], 0\n\t"
               "v_mov_b64 v[254:255], 0\n\t"
               "v_mov_b64 v[180:181], 0\n\t"
               "v_mov_b64 v[182:183], 0\n\t"
               "s_nop 1" ::: "v176", "v255");
}
DI void alpha1(const bf16x8& asl) {
  asm volatile(EXP_LO(192, 193, 194, 195) MF(ACC1, "%[asl]", X1) EXP_HI(196, 197, 198, 199) :: [asl] "v"(asl));
}
DI void alpha2(const bf16x8& ax, const bf16x8& bx, const bf16x8& q0, const bf16x8& w0) {
  asm volatile(ADD1 MF0(Y1, "%[ax]", "%[bx]") RCP_LO MF(Y1, "%[q0]", "%[w0]") RCP_HI
               MULY(192, 193, 194, 195, 196, 197, 198, 199) PACK(176, 177, 178, 179)
               :: [ax] "v"(ax), [bx] "v"(bx), [q0] "v"(q0), [w0] "v"(w0));
}
DI void alpha3(const bf16x8& q1, const bf16x8& w1, const bf16x8& q2, const bf16x8& w2, const bf16x8& q3, const bf16x8& w3, const bf16x8& as0) {
  asm volatile(MF(Y1, "%[q1]", "%[w1]") EXP_LO(200, 201, 202, 203) MF(Y1, "%[q2]", "%[w2]") EXP_HI(204, 205, 206, 207) ADD1
               MF(Y1, "%[q3]", "%[w3]") RCP_LO MF(ACC0, "%[as]", X0) RCP_HI
               MULY(200, 201, 202, 203, 204, 205, 206, 207) PACK(180, 181, 182, 183)
               :: [q1] "v"(q1), [w1] "v"(w1), [q2] "v"(q2), [w2] "v"(w2), [q3] "v"(q3), [w3] "v"(w3), [as] "v"(as0));
}
DI void beta1(const bf16x8& as1) {
  asm volatile(EXP_LO(208, 209, 210, 211) MF(ACC0, "%[as]", X1) EXP_HI(212, 213, 214, 215) ADD1 :: [as] "v"(as1));
}
DI void beta2(const bf16x8& ax, const bf16x8& bx, const bf16x8& q0, const bf16x8& w0) {
  asm volatile(MF0(Y0, "%[ax]", "%[bx]") RCP_LO MF(Y0, "%[q0]", "%[w0]") RCP_HI
               MULY(208, 209, 210, 211, 212, 213, 214, 215) PACK(176, 177, 178, 179)
               :: [ax] "v"(ax), [bx] "v"(bx), [q0] "v"(q0), [w0] "v"(w0));
}
DI void beta3(const bf16x8& q1, const bf16x8& w1, const bf16x8& q2, const bf16x8& w2, const bf16x8& q3, const bf16x8& w3, const bf16x8& as0) {
  asm volatile(MF(Y0, "%[q1]", "%[w1]") EXP_LO(216, 217, 218, 219) MF(Y0, "%[q2]", "%[w2]") EXP_HI(220, 221, 222, 223) ADD1
               MF(Y0, "%[q3]", "%[w3]") RCP_LO MF(ACC1, "%[as]", X0) RCP_HI
               MULY(216, 217, 218, 219, 220, 221, 222, 223) PACK(180, 181, 182, 183)
               :: [q1] "v"(q1), [w1] "v"(w1), [q2] "v"(q2), [w2] "v"(w2), [q3] "v"(q3), [w3] "v"(w3), [as] "v"(as0));
}
DI void chain_y0(const bf16x8& ax, const bf16x8& bx, const bf16x8& q0, const bf16x8& w0, const bf16x8& q1, const bf16x8& w1,
                 const bf16x8& q2, const bf16x8& w2, const bf16x8& q3, const bf16x8& w3) {
  asm volatile("s_nop 1\n\t" MF0(Y0, "%[ax]", "%[bx]") MF(Y0, "%[q0]", "%[w0]") MF(Y0, "%[q1]", "%[w1]") MF(Y0, "%[q2]", "%[w2]")
               MF(Y0, "%[q3]", "%[w3]") "s_nop 15"
               :: [ax] "v"(ax), [bx] "v"(bx), [q0] "v"(q0), [w0] "v"(w0), [q1] "v"(q1), [w1] "v"(w1), [q2] "v"(q2), [w2] "v"(w2),
                  [q3] "v"(q3), [w3] "v"(w3));
}
DI void last_block(const bf16x8& as1) { asm volatile("s_nop 1\n\t" MF(ACC1, "%[as]", X1) :: [as] "v"(as1)); }
DI void acc_gather(unsigned a0, unsigned a1, unsigned a2, unsigned a3) {
  f32x2 t0, t1, t2, t3, t4, t5, t6, t7;
  asm volatile(
               "ds_read2_b32 %[t0], %[a0] offset0:0 offset1:64\n\t"
               "ds_read2_b32 %[t1], %[a0] offset0:128 offset1:192\n\t"
               "ds_read2_b32 %[t2], %[a0] offset0:32 offset1:96\n\t"
               "ds_read2_b32 %[t3], %[a0] offset0:160 offset1:224\n\t"
               "ds_read2_b32 %[t4], %[a1] offset0:0 offset1:64\n\t"
               "ds_read2_b32 %[t5], %[a1] offset0:128 offset1:192\n\t"
               "ds_read2_b32 %[t6], %[a1] offset0:32 offset1:96\n\t"
               "ds_read2_b32 %[t7], %[a1] offset0:160 offset1:224\n\t"
               "s_waitcnt lgkmcnt(4)\n\t"
               "v_pk_add_f32 v[224:225], v[224:225], %[t0]\n\t"
               "v_pk_add_f32 v[226:227], v[226:227], %[t1]\n\t"
               "v_pk_add_f32 v[240:241], v[240:241], %[t2]\n\t"
               "v_pk_add_f32 v[242:243], v[242:243], %[t3]\n\t"
               "s_waitcnt lgkmcnt(0)\n\t"
               "v_pk_add_f32 v[228:229], v[228:229], %[t4]\n\t"
               "v_pk_add_f32 v[230:231], v[230:231], %[t5]\n\t"
               "v_pk_add_f32 v[244:245], v[244:245], %[t6]\n\t"
               "v_pk_add_f32 v[246:247], v[246:247], %[t7]\n\t"
               "ds_read2_b32 %[t0], %[a2] offset0:0 offset1:64\n\t"
               "ds_read2_b32 %[t1], %[a2] offset0:128 offset1:192\n\t"
               "ds_read2_b32 %[t2], %[a2] offset0:32 offset1:96\n\t"
               "ds_read2_b32 %[t3], %[a2] offset0:160 offset1:224\n\t"
               "ds_read2_b32 %[t4], %[a3] offset0:0 offset1:64\n\t"
               "ds_read2_b32 %[t5], %[a3] offset0:128 offset1:192\n\t"
               "ds_read2_b32 %[t6], %[a3] offset0:32 offset1:96\n\t"
               "ds_read2_b32 %[t7], %[a3] offset0:160 offset1:224\n\t"
               "s_waitcnt lgkmcnt(4)\n\t"
               "v_pk_add_f32 v[232:233], v[232:233], %[t0]\n\t"
               "v_pk_add_f32 v[234:235], v[234:235], %[t1]\n\t"
               "v_pk_add_f32 v[248:249], v[248:249], %[t2]\n\t"
               "v_pk_add_f32 v[250:251], v[250:251], %[t3]\n\t"
               "s_waitcnt lgkmcnt(0)\n\t"
               "v_pk_add_f32 v[236:237], v[236:237], %[t4]\n\t"
               "v_pk_add_f32 v[238:239], v[238:239], %[t5]\n\t"
               "v_pk_add_f32 v[252:253], v[252:253], %[t6]\n\t"
               "v_pk_add_f32 v[254:255], v[254:255], %[t7]\n\t"
               "s_nop 0"
               : [t0] "=&v"(t0), [t1] "=&v"(t1), [t2] "=&v"(t2), [t3] "=&v"(t3), [t4] "=&v"(t4), [t5] "=&v"(t5), [t6] "=&v"(t6), [t7] "=&v"(t7)
               : [a0] "v"(a0), [a1] "v"(a1), [a2] "v"(a2), [a3] "v"(a3) : "memory");
}
DI void acc_store(unsigned a0, unsigned a1, unsigned a2, unsigned a3) {
  asm volatile(
               "ds_write2_b32 %[a0], v224, v225 offset0:0 offset1:64\n\t"
               "ds_write2_b32 %[a0], v226, v227 offset0:128 offset1:192\n\t"
               "ds_write2_b32 %[a0], v240, v241 offset0:32 offset1:96\n\t"
               "ds_write2_b32 %[a0], v242, v243 offset0:160 offset1:224\n\t"
               "ds_write2_b32 %[a1], v228, v229 offset0:0 offset1:64\n\t"
               "ds_write2_b32 %[a1], v230, v231 offset0:128 offset1:192\n\t"
               "ds_write2_b32 %[a1], v244, v245 offset0:32 offset1:96\n\t"
               "ds_write2_b32 %[a1], v246, v247 offset0:160 offset1:224\n\t"
               "ds_write2_b32 %[a2], v232, v233 offset0:0 offset1:64\n\t"
               "ds_write2_b32 %[a2], v234, v235 offset0:128 offset1:192\n\t"
               "ds_write2_b32 %[a2], v248, v249 offset0:32 offset1:96\n\t"
               "ds_write2_b32 %[a2], v250, v251 offset0:160 offset1:224\n\t"
               "ds_write2_b32 %[a3], v236, v237 offset0:0 offset1:64\n\t"
               "ds_write2_b32 %[a3], v238, v239 offset0:128 offset1:192\n\t"
               "ds_write2_b32 %[a3], v252, v253 offset0:32 offset1:96\n\t"
               "ds_write2_b32 %[a3], v254, v255 offset0:160 offset1:224\n\t"
               "s_waitcnt lgkmcnt(0)\n\t"
               "v_mov_b64 v[224:225], 0\n\t"
               "v_mov_b64 v[226:227], 0\n\t"
               "v_mov_b64 v[228:229], 0\n\t"
               "v_mov_b64 v[230:231], 0\n\t"
               "v_mov_b64 v[232:233], 0\n\t"
               "v_mov_b64 v[234:235], 0\n\t"
               "v_mov_b64 v[236:237], 0\n\t"
               "v_mov_b64 v[238:239], 0\n\t"
               "v_mov_b64 v[240:241], 0\n\t"
               "v_mov_b64 v[242:243], 0\n\t"
               "v_mov_b64 v[244:245], 0\n\t"
               "v_mov_b64 v[246:247], 0\n\t"
               "v_mov_b64 v[248:249], 0\n\t"
               "v_mov_b64 v[250:251], 0\n\t"
               "v_mov_b64 v[252:253], 0\n\t"
               "v_mov_b64 v[254:255], 0\n\t"
               "s_nop 7"
               :: [a0] "v"(a0), [a1] "v"(a1), [a2] "v"(a2), [a3] "v"(a3) : "memory");
}
DI void acc_gather_last(unsigned a0, unsigned a1, unsigned a2, unsigned a3) {
  asm volatile(
               "ds_read2_b32 v[176:177], %[a0] offset0:0 offset1:64\n\t"
               "ds_read2_b32 v[178:179], %[a0] offset0:128 offset1:192\n\t"
               "ds_read2_b32 v[180:181], %[a0] offset0:32 offset1:96\n\t"
               "ds_read2_b32 v[182:183], %[a0] offset0:160 offset1:224\n\t"
               "ds_read2_b32 v[184:185], %[a1] offset0:0 offset1:64\n\t"
               "ds_read2_b32 v[186:187], %[a1] offset0:128 offset1:192\n\t"
               "ds_read2_b32 v[188:189], %[a1] offset0:32 offset1:96\n\t"
               "ds_read2_b32 v[190:191], %[a1] offset0:160 offset1:224\n\t"
               "ds_read2_b32 v[192:193], %[a2] offset0:0 offset1:64\n\t"
               "ds_read2_b32 v[194:195], %[a2] offset0:128 offset1:192\n\t"
               "ds_read2_b32 v[196:197], %[a2] offset0:32 offset1:96\n\t"
               "ds_read2_b32 v[198:199], %[a2] offset0:160 offset1:224\n\t"
               "ds_read2_b32 v[200:201], %[a3] offset0:0 offset1:64\n\t"
               "ds_read2_b32 v[202:203], %[a3] offset0:128 offset1:192\n\t"
               "ds_read2_b32 v[204:205], %[a3] offset0:32 offset1:96\n\t"
               "ds_read2_b32 v[206:207], %[a3] offset0:160 offset1:224\n\t"
               "s_waitcnt lgkmcnt(12)\n\t"
               "v_pk_add_f32 v[224:225], v[224:225], v[176:177]\n\t"
               "v_pk_add_f32 v[226:227], v[226:227], v[178:179]\n\t"
               "v_pk_add_f32 v[240:241], v[240:241], v[180:181]\n\t"
               "v_pk_add_f32 v[242:243], v[242:243], v[182:183]\n\t"
               "s_waitcnt lgkmcnt(8)\n\t"
               "v_pk_add_f32 v[228:229], v[228:229], v[184:185]\n\t"
               "v_pk_add_f32 v[230:231], v[230:231], v[186:187]\n\t"
               "v_pk_add_f32 v[244:245], v[244:245], v[188:189]\n\t"
               "v_pk_add_f32 v[246:247], v[246:247], v[190:191]\n\t"
               "s_waitcnt lgkmcnt(4)\n\t"
               "v_pk_add_f32 v[232:233], v[232:233], v[192:193]\n\t"
               "v_pk_add_f32 v[234:235], v[234:235], v[194:195]\n\t"
               "v_pk_add_f32 v[248:249], v[248:249], v[196:197]\n\t"
               "v_pk_add_f32 v[250:251], v[250:251], v[198:199]\n\t"
               "s_waitcnt lgkmcnt(0)\n\t"
               "v_pk_add_f32 v[236:237], v[236:237], v[200:201]\n\t"
               "v_pk_add_f32 v[238:239], v[238:239], v[202:203]\n\t"
               "v_pk_add_f32 v[252:253], v[252:253], v[204:205]\n\t"
               "v_pk_add_f32 v[254:255], v[254:255], v[206:207]\n\t"
               "s_nop 0"
               :: [a0] "v"(a0), [a1] "v"(a1), [a2] "v"(a2), [a3] "v"(a3) : "memory");
}
DI void acc_store_last(unsigned a0, unsigned a1, unsigned a2, unsigned a3) {
  asm volatile(
               "ds_write2_b32 %[a0], v224, v225 offset0:0 offset1:64\n\t"
               "ds_write2_b32 %[a0], v226, v227 offset0:128 offset1:192\n\t"
               "ds_write2_b32 %[a0], v240, v241 offset0:32 offset1:96\n\t"
               "ds_write2_b32 %[a0], v242, v243 offset0:160 offset1:224\n\t"
               "ds_write2_b32 %[a1], v228, v229 offset0:0 offset1:64\n\t"
               "ds_write2_b32 %[a1], v230, v231 offset0:128 offset1:192\n\t"
               "ds_write2_b32 %[a1], v244, v245 offset0:32 offset1:96\n\t"
               "ds_write2_b32 %[a1], v246, v247 offset0:160 offset1:224\n\t"
               "ds_write2_b32 %[a2], v232, v233 offset0:0 offset1:64\n\t"
               "ds_write2_b32 %[a2], v234, v235 offset0:128 offset1:192\n\t"
               "ds_write2_b32 %[a2], v248, v249 offset0:32 offset1:96\n\t"
               "ds_write2_b32 %[a2], v250, v251 offset0:160 offset1:224\n\t"
               "ds_write2_b32 %[a3], v236, v237 offset0:0 offset1:64\n\t"
               "ds_write2_b32 %[a3], v238, v239 offset0:128 offset1:192\n\t"
               "ds_write2_b32 %[a3], v252, v253 offset0:32 offset1:96\n\t"
               "ds_write2_b32 %[a3], v254, v255 offset0:160 offset1:224\n\t"
               "s_nop 0"
               :: [a0] "v"(a0), [a1] "v"(a1), [a2] "v"(a2), [a3] "v"(a3) : "memory");
}
DI unsigned lds_lock(unsigned addr) {
  unsigned t, old;
  unsigned long long sv;
  asm volatile("s_mov_b64 %2, exec\n\ts_mov_b64 exec, 1\n"
               "1:\n\tds_wrxchg_rtn_b32 %0, %3, %4\n\ts_waitcnt lgkmcnt(0)\n\tv_cmp_eq_u32 vcc, 1, %0\n\ts_cbranch_vccz 2f\n\t"
               "s_sleep 1\n\ts_branch 1b\n"
               "2:\n\tv_readfirstlane_b32 %1, %0\n\ts_mov_b64 exec, %2\n\ts_nop 15"
               : "=&v"(t), "=&s"(old), "=&s"(sv) : "v"(addr), "v"(1u) : "vcc", "memory");
  return old;
}

template <typename T> DI T* karg(int k) {
  typedef __attribute__((address_space(4))) const unsigned long long* kp_t;
  kp_t kp = (kp_t)__builtin_amdgcn_kernarg_segment_ptr();
  asm volatile("" : "+s"(kp));
  return (T*)(__attribute__((address_space(1))) T*)kp[k];
}

__global__ __launch_bounds__(512) __attribute__((amdgpu_num_vgpr(88))) void egnn_kernel(
    const u16* __restrict__ h_, const u16* __restrict__ pos_, const u16* __restrict__ we_, const u16* __restrict__ wx_,
    const u16* __restrict__ wh_, char* __restrict__ ws_, u16* __restrict__ out_, const int* __restrict__ src_,
    const int* __restrict__ dst_, const u16* __restrict__ ea_) {
  __shared__ __attribute__((aligned(1024))) char lds[L_END];
  AS3 char* L = (AS3 char*)lds;
  const unsigned LB = (unsigned)(size_t)L;
  const int tid_ = threadIdx.x;
  const int w = __builtin_amdgcn_readfirstlane(tid_ >> 6);
  STAMP(0, 1);
  const int xl = blockIdx.x & 7;
  const int b = xl * 31 + (xl < 2 ? xl : 2) + (blockIdx.x >> 3);
  const int n0 = b * BN;
  unsigned cw[2][2];

  {
    const int tid = tid_, lane = tid & 63, r = lane & 31, hf = lane >> 5, p = blockIdx.x;
    const rsrc_t rs = ws_rsrc(ws_);
    const int e0 = p * CH + 4 * tid, e1 = p * CH + 2048 + tid;
    const i32x4 d4 = *(const i32x4*)(dst_ + e0), s4 = *(const i32x4*)(src_ + e0);
    const u32x2 a2 = *(const u32x2*)(ea_ + e0);
    unsigned dd[5] = {(unsigned)d4[0], (unsigned)d4[1], (unsigned)d4[2], (unsigned)d4[3], (unsigned)dst_[e1]};
    const unsigned s5 = src_[e1], a5 = ea_[e1];
    const u32x2 ctl = ld8_dev(rs, WS_CTL);
    SB();
    dma_piece((const char*)we_ + w * 1024, lane * 16, LB + L_W + w * 1024);
    if (w < 5) dma_piece((const char*)h_ + (size_t)n0 * 128 + w * 1024, lane * 16, LB + L_H + w * 1024);
    const u32x4 w2raw = *(const u32x4*)(we_ + 64 * 64 + tid * 8);
    unsigned sraw[3], craw[4];
#pragma unroll
    for (int t = 0; t < 2; ++t) { craw[2 * t] = we_[128 * 64 + 32 * t + r]; craw[2 * t + 1] = we_[129 * 64 + 32 * t + r]; }
    {
      const u16* pp = w == 1 ? wx_ + lane : pos_ + (lane < BN ? n0 + lane : 0) * 3;
      const int st = w == 1 ? 0 : 1;
      sraw[0] = pp[0]; sraw[1] = pp[st]; sraw[2] = pp[2 * st];
    }
    SB();
    {
      const u32x4 z4 = {0, 0, 0, 0};
      static_assert(8 * S_WAVE == 2 * 8192 + 256 * 16 && BN * ACC_NODE == 5 * 8192 + 40 * 16, "clearing pattern");
      if (tid < 256) lds_st<u32x4>(L, L_TBL + 16 * tid, z4);
      lds_st<u32x4>(L, L_S + tid * 16, z4);
      lds_st<u32x4>(L, L_S + 8192 + tid * 16, z4);
#pragma unroll
      for (int k = 0; k < 5; ++k) lds_st<u32x4>(L, L_ACC + k * 8192 + tid * 16, z4);
      if (tid < 256) lds_st<u32x4>(L, L_S + 16384 + tid * 16, z4);
      if (tid < 40) lds_st<u32x4>(L, L_ACC + 5 * 8192 + tid * 16, z4);
      if (tid < 8) lds_st<unsigned>(L, L_LOCK + 4 * tid, 0u);
    }
    wait_vm0();
    const unsigned tlo = ctl[0] + 1, thi = ctl[1] + (tlo == 0);
    const unsigned tag0 = tlo & 0xfffffffu, tag1 = ((tlo >> 28) | (thi << 4)) & 0xfffffffu;
    if (tid == 0) { lds_st<u32x4>(L, L_TAG, u32x4{tag0, tag1, 0u, 0u}); lds_st<u32x2>(L, L_TAG + 16, u32x2{tlo, thi}); }
    {
      u32x4 o;
#pragma unroll
      for (int k = 0; k < 4; ++k) o[k] = cvt_pk(bf_lo(w2raw[k]) * NLOG2E, bf_hi(w2raw[k]) * NLOG2E);
      lds_st<u32x4>(L, L_W + 8192 + tid * 16, o);
    }
    __syncthreads();
    STAMP(1, 1);
    unsigned idx[5], rank[5], bk[5];
#pragma unroll
    for (int i = 0; i < 5; ++i) {
      const unsigned G = dd[i] >> 3;
      bk[i] = (G * 52429u) >> 18;
      idx[i] = G + 3 * bk[i];
      const unsigned sh = (idx[i] & 1) << 4;
      const unsigned old = __hip_atomic_fetch_add((AS3 unsigned*)(L + L_TBL + 4 * (idx[i] >> 1)), 1u << sh, __ATOMIC_RELAXED,
                                                  __HIP_MEMORY_SCOPE_WORKGROUP);
      rank[i] = (old >> sh) & 0xffffu;
    }
    if (w >= 4) {
      const int u = (w - 4) >> 1, t = w & 1;
      f32x16 pacc;
#pragma unroll
      for (int i = 0; i < 16; ++i) pacc[i] = 0.f;
#pragma unroll
      for (int s = 0; s < 4; ++s) {
        const int nl = 32 * u + r, nlc = nl < BN ? nl : BN - 1;
        const bf16x8 ha = lds_ld<bf16x8>(L, L_H + nlc * 128 + 32 * s + 16 * hf);
        const unsigned o = L_W + (16 * s + 8 * (lane >> 5) + ((lane & 15) >> 2)) * 128 + (32 * t + 16 * ((lane >> 4) & 1) + 4 * (lane & 3)) * 2;
        const u32x2 b0 = lds_tr(L, o), b1 = lds_tr(L, o + 512);
        pacc = mfma(ha, as_frag(u32x4{b0[0], b0[1], b1[0], b1[1]}), pacc);
      }
#pragma unroll
      for (int q = 0; q < 4; ++q)
        if (4 * u + q < 5)
          lds_st<u32x2>(L, L_PFR + ((4 * u + q) * 2 + t) * 512 + lane * 8,
                        u32x2{cvt_pk(pacc[4 * q] * NLOG2E, pacc[4 * q + 1] * NLOG2E), cvt_pk(pacc[4 * q + 2] * NLOG2E, pacc[4 * q + 3] * NLOG2E)});
    } else if (w == 0) {
      if (lane <= BN)
        lds_st<f32x4>(L, L_PD + 16 * lane, f32x4{bf_lo(sraw[0]), bf_lo(sraw[1]), bf_lo(sraw[2]), lane < BN ? 1.0f : 0.0f});
    } else if (w == 1) {
      lds_st<float>(L, L_WX + 4 * lane, bf_lo(sraw[0]));
    }
#pragma unroll
    for (int t = 0; t < 2; ++t) {
      const float d = bf_lo(craw[2 * t]) * NLOG2E, a = bf_lo(craw[2 * t + 1]) * NLOG2E;
      const unsigned hi = cvt_pk(d, a);
      cw[t][0] = hi;
      cw[t][1] = cvt_pk(d - bf_lo(hi), a - bf_hi(hi));
    }
    __syncthreads();
    STAMP(2, 1);
    u32x4 c = {0, 0, 0, 0};
    unsigned tot = 0, incl = 0;
    if (w < 4) {
      c = lds_ld<u32x4>(L, L_TBL + 16 * tid);
      tot = (c[0] & 0xffffu) + (c[0] >> 16) + (c[1] & 0xffffu) + (c[1] >> 16) + (c[2] & 0xffffu);
      incl = wave_incl_scan(tot);
      if (lane == 63) lds_st<unsigned>(L, L_WT + 4 * w, incl);
    }
    __syncthreads();
    u32x4 entry = {0, 0, 0, 0};
    if (w < 4) {
      const u32x4 wt = lds_ld<u32x4>(L, L_WT);
      const unsigned s0 = incl - tot + (w > 0 ? wt[0] : 0) + (w > 1 ? wt[1] : 0) + (w > 2 ? wt[2] : 0);
      const unsigned s1 = s0 + (c[0] & 0xffffu), s2 = s1 + (c[0] >> 16), s3 = s2 + (c[1] & 0xffffu), s4_ = s3 + (c[1] >> 16);
      const unsigned s5_ = s0 + tot;
      lds_st<u32x3>(L, L_TBL + 16 * tid, u32x3{s0 | (s1 << 16), s2 | (s3 << 16), s4_});
      entry = u32x4{s0 | (s1 << 12) | (s2 << 24), (s2 >> 8) | (tag0 << 4), s3 | (s4_ << 12) | (s5_ << 24), (s5_ >> 8) | (tag1 << 4)};
    }
    __syncthreads();
    STAMP(3, 1);
    {
      unsigned ps[5];
#pragma unroll
      for (int i = 0; i < 5; ++i) ps[i] = lds_ld<u16>(L, L_TBL + 2 * idx[i]) + rank[i];
#pragma unroll
      for (int i = 0; i < 5; ++i) {
        const unsigned d = dd[i], dl = d - BN * bk[i];
        const unsigned sv = i < 4 ? (unsigned)s4[i] : s5;
        const unsigned eav = i == 4 ? a5 << 16 : (i & 1) ? (a2[i >> 1] & 0xffff0000u) : (a2[i >> 1] << 16);
        lds_st<u32x2>(L, L_RAW + 8 * ps[i], u32x2{sv | ((L_PD + 16 * dl) << 16), ((d & 7) * 320) | eav});
      }
    }
    __syncthreads();
    STAMP(4, 1);
    {
      const unsigned o = WS_RECS + p * (CH * 8) + 16 * tid;
      const u32x4 v0 = lds_ld<u32x4>(L, L_RAW + 16 * tid), v1 = lds_ld<u32x4>(L, L_RAW + 8192 + 16 * tid);
      st16_rec(rs, o, v0);
      st16_rec(rs, o + 8192, v1);
      if (tid < 256) st16_rec(rs, o + 16384, lds_ld<u32x4>(L, L_RAW + 16384 + 16 * tid));
    }
    wait_vm0();
    __syncthreads();
    if (tid < NBK) st16_dev(rs, WS_ENT + tid * 4096 + p * 16, entry);
    STAMP(5, 1);
  }

  int pa = 0, np = NP;
  for (;;) {
    const u16 *h = karg<const u16>(0), *pos = karg<const u16>(1);
    const char* ws = karg<const char>(5);
    const rsrc_t rs = ws_rsrc(ws);
    int tid = tid_;
    asm volatile("" : "+v"(tid));
    const int lane = tid & 63, r = lane & 31, hf = lane >> 5;
    u32x4 ent = {0, 0, 0, 0};
    if (w < 4) {
      const u32x2 tg = lds_ld<u32x2>(L, L_TAG);
      for (unsigned spins = 0;; ++spins) {
        ent = ld16_dev(rs, WS_ENT + b * 4096 + tid * 16);
        if (__all(tid >= NP || ((ent[1] >> 4) == tg[0] && (ent[3] >> 4) == tg[1]))) break;
        if (spins > SPIN_LIMIT) { lds_st<unsigned>(L, L_TAG + 8, 1u); break; }
        __builtin_amdgcn_s_sleep(POLL_SLEEP);
      }
    }
    SB();
    STAMP(6, 1);

    unsigned cA = 0, cB = 0, cC = 0, cD = 0, iA = 0, iB = 0, iC = 0, iD = 0;
    const unsigned s0 = ent[0] & 0xfffu, s1 = (ent[0] >> 12) & 0xfffu, s2 = (ent[0] >> 24) | ((ent[1] & 15u) << 8);
    const unsigned s3 = ent[2] & 0xfffu, s4 = (ent[2] >> 12) & 0xfffu, s5 = (ent[2] >> 24) | ((ent[3] & 15u) << 8);
    if (w < 4) {
      if ((unsigned)(tid - pa) < (unsigned)np && tid < NP) {
        cA = (s1 - s0) | ((s2 - s1) << 16);
        cB = (s3 - s2) | ((s4 - s3) << 16);
        cC = s5 - s4;
        cD = s5 - s0;
      }
      lds_st<unsigned>(L, L_SEG + 4 * tid, s0 | (cD << 16));
      iA = wave_incl_scan(cA); iB = wave_incl_scan(cB); iC = wave_incl_scan(cC); iD = wave_incl_scan(cD);
      if (lane == 63) {
        lds_st<unsigned>(L, L_WT + 4 * w, iA); lds_st<unsigned>(L, L_WT + 16 + 4 * w, iB);
        lds_st<unsigned>(L, L_WT + 32 + 4 * w, iC); lds_st<unsigned>(L, L_WT + 48 + 4 * w, iD);
      }
    }
    __syncthreads();
    STAMP(7, 1);
    if (lds_ld<unsigned>(L, L_TAG + 8) != 0) return;
    if (blockIdx.x == 0 && tid == 448 && pa == 0 && np == NP) {
      const u32x2 e = lds_ld<u32x2>(L, L_TAG + 16);
      __builtin_amdgcn_raw_buffer_store_b64(e, rs, WS_CTL, 0, 16);
    }
    const unsigned j2 = 2 * (tid & 7);
    u32x4 ra[4], rb[4];
    unsigned cnt[4], e3[4];
#pragma unroll
    for (int k = 0; k < 4; ++k) e3[k] = lds_ld<unsigned>(L, L_SEG + 4 * (64 * k + (tid >> 3)));
#pragma unroll
    for (int k = 0; k < 4; ++k) {
      const int p = 64 * k + (tid >> 3);
      cnt[k] = e3[k] >> 16;
      ra[k] = u32x4{0, 0, 0, 0};
      if (j2 < cnt[k]) ra[k] = ld16_rec(rs, WS_RECS + (p * CH + (e3[k] & 0xffffu) + j2) * 8);
    }
#pragma unroll
    for (int k = 0; k < 4; ++k) {
      const int p = 64 * k + (tid >> 3);
      rb[k] = u32x4{0, 0, 0, 0};
      if (j2 + 16 < cnt[k]) rb[k] = ld16_rec(rs, WS_RECS + (p * CH + (e3[k] & 0xffffu) + j2 + 16) * 8);
    }
    SB();
    const u32x4 wA = lds_ld<u32x4>(L, L_WT), wB = lds_ld<u32x4>(L, L_WT + 16), wC = lds_ld<u32x4>(L, L_WT + 32), wD = lds_ld<u32x4>(L, L_WT + 48);
    const unsigned tA = wA[0] + wA[1] + wA[2] + wA[3], tB = wB[0] + wB[1] + wB[2] + wB[3];
    const unsigned tC = wC[0] + wC[1] + wC[2] + wC[3], tD = wD[0] + wD[1] + wD[2] + wD[3];
    unsigned tot[5] = {tA & 0xffffu, tA >> 16, tB & 0xffffu, tB >> 16, tC};
    unsigned base[6];
    base[0] = 0;
#pragma unroll
    for (int g = 0; g < 5; ++g) base[g + 1] = base[g] + ((tot[g] + 31) & ~31u);
    const bool fits = __builtin_amdgcn_readfirstlane((int)(tD <= (unsigned)CAP && base[5] <= (unsigned)CAP)) != 0;
    if (!fits) {
      np = (np + 1) >> 1;
      __syncthreads();
      continue;
    }
    if (w < 4) {
      const unsigned pA = iA - cA + (w > 0 ? wA[0] : 0) + (w > 1 ? wA[1] : 0) + (w > 2 ? wA[2] : 0);
      const unsigned pB = iB - cB + (w > 0 ? wB[0] : 0) + (w > 1 ? wB[1] : 0) + (w > 2 ? wB[2] : 0);
      const unsigned pC = iC - cC + (w > 0 ? wC[0] : 0) + (w > 1 ? wC[1] : 0) + (w > 2 ? wC[2] : 0);
      const unsigned v0 = L_RAW + 8 * (base[0] + (pA & 0xffffu));
      const unsigned v1 = L_RAW + 8 * (base[1] + (pA >> 16) - (s1 - s0));
      const unsigned v2 = L_RAW + 8 * (base[2] + (pB & 0xffffu) - (s2 - s0));
      const unsigned v3 = L_RAW + 8 * (base[3] + (pB >> 16) - (s3 - s0));
      const unsigned v4 = L_RAW + 8 * (base[4] + pC - (s4 - s0));
      lds_st<u32x4>(L, L_TBL + 16 * tid, u32x4{(v0 & 0xffffu) | (v1 << 16), (v2 & 0xffffu) | (v3 << 16), v4 & 0xffffu, 0});
    } else {
      const int t = tid - 256;
      const u32x2 pad = {(unsigned)(L_PD + 16 * BN) << 16, 0};
      if (t < 160) {
        const int g = t >> 5, k = t & 31;
        const unsigned bg = g == 0 ? base[0] : g == 1 ? base[1] : g == 2 ? base[2] : g == 3 ? base[3] : base[4];
        const unsigned bn = g == 0 ? base[1] : g == 1 ? base[2] : g == 2 ? base[3] : g == 3 ? base[4] : base[5];
        const unsigned tg = g == 0 ? tot[0] : g == 1 ? tot[1] : g == 2 ? tot[2] : g == 3 ? tot[3] : tot[4];
        const unsigned slot = bg + tg + k;
        if (slot < bn) lds_st<u32x2>(L, L_RAW + 8 * slot, pad);
      }
      if (t < PADS) lds_st<u32x2>(L, L_RAW + 8 * (base[5] + t), pad);
    }
    __syncthreads();
    STAMP(8, 1);

    {
      unsigned a0[4], a1[4];
#pragma unroll
      for (int k = 0; k < 4; ++k) {
        const unsigned tb = L_TBL + 16 * (64 * k + (tid >> 3));
        a0[k] = lds_ld<u16>(L, tb + 2 * ((ra[k][0] >> 23) & 7)) + 8 * j2;
        a1[k] = lds_ld<u16>(L, tb + 2 * ((ra[k][2] >> 23) & 7)) + 8 * j2 + 8;
      }
#pragma unroll
      for (int k = 0; k < 4; ++k) {
        if (j2 < cnt[k]) lds_st<u32x2>(L, a0[k], u32x2{ra[k][0], ra[k][1]});
        if (j2 + 1 < cnt[k]) lds_st<u32x2>(L, a1[k], u32x2{ra[k][2], ra[k][3]});
      }
#pragma unroll
      for (int k = 0; k < 4; ++k) {
        if (j2 + 16 < cnt[k]) {
          const unsigned tb = L_TBL + 16 * (64 * k + (tid >> 3));
          const unsigned b0 = lds_ld<u16>(L, tb + 2 * ((rb[k][0] >> 23) & 7)) + 8 * (j2 + 16);
          const unsigned b1 = lds_ld<u16>(L, tb + 2 * ((rb[k][2] >> 23) & 7)) + 8 * (j2 + 17);
          lds_st<u32x2>(L, b0, u32x2{rb[k][0], rb[k][1]});
          if (j2 + 17 < cnt[k]) lds_st<u32x2>(L, b1, u32x2{rb[k][2], rb[k][3]});
        }
      }
#pragma unroll
      for (int k = 0; k < 4; ++k) {
        for (unsigned jj = (tid & 7) + 32; jj < cnt[k]; jj += 8) {
          const int p = 64 * k + (tid >> 3);
          const u32x2 rec = ld8_dev(rs, WS_RECS + (p * CH + (e3[k] & 0xffffu) + jj) * 8);
          const unsigned a = lds_ld<u16>(L, L_TBL + 16 * p + 2 * ((rec[0] >> 23) & 7));
          lds_st<u32x2>(L, a + 8 * jj, rec);
        }
      }
    }
    __syncthreads();
    STAMP(9, 1);

    const int T = __builtin_amdgcn_readfirstlane((int)(base[5] >> 5));
    const int tq8 = T >> 3, trem = T & 7;
    const int ta = w * tq8 + (w < trem ? w : trem);
    const int tb = ta + tq8 + (w < trem ? 1 : 0);
    const int tg1 = __builtin_amdgcn_readfirstlane((int)(base[1] >> 5)), tg2 = __builtin_amdgcn_readfirstlane((int)(base[2] >> 5));
    const int tg3 = __builtin_amdgcn_readfirstlane((int)(base[3] >> 5)), tg4 = __builtin_amdgcn_readfirstlane((int)(base[4] >> 5));

    if (ta < tb) {
      const char *qtab = uni_ptr((const char*)h), *pos6 = uni_ptr((const char*)pos);
      const unsigned ringb = L_RING + w * 4096;
      unsigned aq[4];
#pragma unroll
      for (int s = 0; s < 4; ++s) aq[s] = ringb + r * 128 + (((2 * s + hf) ^ ((r >> 1) & 7)) << 4);
      const unsigned dc0 = ((lane & 7) ^ ((lane >> 4) & 7)) << 4, dc1 = ((lane & 7) ^ ((4 + (lane >> 4)) & 7)) << 4;
      const unsigned Sb = L_S + w * S_WAVE;
      const unsigned pi_e = (r & 0x13) | ((r & 4) << 1) | ((r & 8) >> 1);
      const unsigned sa_c = Sb + 2 * pi_e;
      const unsigned s_rd = Sb + r * 80 + 16 * hf;
      const int g16 = lane >> 4, tq = (lane & 15) >> 2, tp = lane & 3, tu = g16 & 1, th = g16 >> 1;
      const unsigned s_tr = Sb + (4 * (4 * th + tq) + 3) * 80 + 2 * (16 * tu + 8 * (tp & 1) + 4 * (tp >> 1));
      const unsigned acc_l = L_ACC + hf * ACC_NODE + 4 * r;
      bf16x8 wq[2][4];
      auto group_of = [&](int t) { return __builtin_amdgcn_readfirstlane((t >= tg1) + (t >= tg2) + (t >= tg3) + (t >= tg4)); };
      auto group_end = [&](int g) { return __builtin_amdgcn_readfirstlane(g == 0 ? tg1 : g == 1 ? tg2 : g == 2 ? tg3 : g == 3 ? tg4 : T); };
      int gend = group_end(group_of(ta));
      int tflush = -1;

      bf16x8 bxA0, bxA1, bxB0, bxB1;
      auto load_bx = [&](int gg) {
        const u32x2 p0 = lds_ld<u32x2>(L, L_PFR + gg * 1024 + lane * 8), p1 = lds_ld<u32x2>(L, L_PFR + gg * 1024 + 512 + lane * 8);
        bxA0 = as_frag(u32x4{p0[0], p0[1], hf ? 0u : cw[0][0], hf ? 0u : cw[0][1]});
        bxB0 = as_frag(u32x4{p0[0], p0[1], hf ? cw[0][0] : 0u, hf ? cw[0][1] : 0u});
        bxA1 = as_frag(u32x4{p1[0], p1[1], hf ? 0u : cw[1][0], hf ? 0u : cw[1][1]});
        bxB1 = as_frag(u32x4{p1[0], p1[1], hf ? cw[1][0] : 0u, hf ? cw[1][1] : 0u});
        asm volatile("" : "+v"(bxA0), "+v"(bxB0), "+v"(bxA1), "+v"(bxB1));
      };
      auto flush = [&](int gg) {
        const unsigned a = LB + acc_l + gg * (8 * ACC_NODE);
        if (lds_lock(LB + L_LOCK + 4 * gg) != 0) acc_gather(a, a + 2 * ACC_NODE, a + 4 * ACC_NODE, a + 6 * ACC_NODE);
        acc_store(a, a + 2 * ACC_NODE, a + 4 * ACC_NODE, a + 6 * ACC_NODE);
        lds_st<unsigned>(L, L_LOCK + 4 * gg, 2u);
        asm volatile("" ::: "memory");
      };
      auto flush_last = [&](int gg) {
        const unsigned a = LB + acc_l + gg * (8 * ACC_NODE);
        if (lds_lock(LB + L_LOCK + 4 * gg) != 0) acc_gather_last(a, a + 2 * ACC_NODE, a + 4 * ACC_NODE, a + 6 * ACC_NODE);
        acc_store_last(a, a + 2 * ACC_NODE, a + 4 * ACC_NODE, a + 6 * ACC_NODE);
        lds_st<unsigned>(L, L_LOCK + 4 * gg, 2u);
        asm volatile("" ::: "memory");
      };
      struct Idx { unsigned i0, i1, i2, i3; };
      unsigned a_idx = L_RAW + ta * 256 + (lane >> 3) * 8, a_raw = L_RAW + ta * 256 + lane * 8;
      auto idx_load = [&](int dt) {
        const unsigned a = a_idx + dt * 256;
        return Idx{lds_ld<u16>(L, a), lds_ld<u16>(L, a + 64), lds_ld<u16>(L, a + 128), lds_ld<u16>(L, a + 192)};
      };
      auto issue_rows = [&](const Idx& x) {
        dma_tile(qtab, x.i0 * 128 + dc0, x.i1 * 128 + dc1, x.i2 * 128 + dc0, x.i3 * 128 + dc1);
      };
      auto raw_load = [&](int dt) {
        const unsigned a = a_raw + dt * 256;
        return Raw{lds_ld<u16>(L, a), lds_ld<u16>(L, a + 2), lds_ld<unsigned>(L, a + 4), lds_ld<u16>(L, a + 4)};
      };
      auto pos_load = [&](const Raw& x) {
        u32x2 ps;
        asm volatile("global_load_dwordx2 %0, %1, %2" : "=v"(ps) : "v"(x.src * 6u), "s"(pos6) : "memory");
        return ps;
      };
      auto wait_ps = [&](u32x2& ps) { asm volatile("s_waitcnt vmcnt(0)" : "+v"(ps) :: "memory"); };
      auto pd_load = [&](const Raw& x) { return lds_ld<f32x4>(L, x.pda); };
      auto prep = [&](const Raw& x, const u32x2& ps, const f32x4& pd) {
        const float dx = pd[0] - bf_lo(ps[0]), dy = pd[1] - bf_hi(ps[0]), dz = pd[2] - bf_lo(ps[1]);
        unsigned wa = cvt_pk(dx, dy), wb = cvt_pk(dz, pd[3]);
        const float dzr = bf_lo(wb);
        const bf16x2v wav = __builtin_bit_cast(bf16x2v, wa);
        const float d2 = __builtin_amdgcn_fdot2_f32_bf16(wav, wav, dzr * dzr, false);
        Prep o;
        o.de = cvt_pk(d2, as_f(x.w1));
        asm volatile("" : "+v"(o.de), "+v"(wa), "+v"(wb));
        const unsigned P = x.n320 + sa_c, Q = P + 160;
        const auto sa = __builtin_amdgcn_permlane32_swap(P, Q, false, false);
        const auto sd = __builtin_amdgcn_permlane32_swap(wa, wb, false, false);
        o.saA = sa[0]; o.saB = sa[1]; o.sdA = sd[0]; o.sdB = sd[1];
        return o;
      };

      struct Ops { bf16x8 q0, q1, q2, q3, as0, as1, ax; };
      auto read_ops = [&](unsigned sa, unsigned sd, unsigned de) {
        Ops o;
        o.q0 = lds_ld<bf16x8>(L, aq[0]); o.q1 = lds_ld<bf16x8>(L, aq[1]);
        o.q2 = lds_ld<bf16x8>(L, aq[2]); o.q3 = lds_ld<bf16x8>(L, aq[3]);
        lds_st<u16>(L, sa, (u16)sd);
        lds_st<u16>(L, sa + 80, (u16)(sd >> 16));
        const u32x2 oh = lds_tr(L, s_tr);
        o.as0 = lds_ld<bf16x8>(L, s_rd);
        o.as1 = lds_ld<bf16x8>(L, s_rd + 32);
        lds_st<u16>(L, sa, (u16)0);
        lds_st<u16>(L, sa + 80, (u16)0);
        unsigned de2;
        asm volatile("v_mov_b32 %0, %1" : "=v"(de2) : "v"(de));
        o.ax = as_frag(u32x4{oh[0], oh[1], de, de2});
        return o;
      };
      auto alpha = [&](int t, const Ops& c, const bf16x8& asl, const bf16x8& bx1, auto&& mid) {
        alpha1(asl);
        if (__builtin_expect(t == tflush, 0)) flush(group_of(t - 1));
        alpha2(c.ax, bx1, c.q0, wq[1][0]);
        SB(); mid(); SB();
        alpha3(c.q1, wq[1][1], c.q2, wq[1][2], c.q3, wq[1][3], c.as0);
      };
      auto beta = [&](const Ops& c, const Ops& n, const bf16x8& bx0n, auto&& mid) {
        beta1(c.as1);
        beta2(n.ax, bx0n, n.q0, wq[0][0]);
        SB(); mid(); SB();
        beta3(n.q1, wq[0][1], n.q2, wq[0][2], n.q3, wq[0][3], c.as0);
      };
      auto next_group = [&](int t) {
        if (__builtin_expect(t == gend, 0)) { const int g = group_of(t); gend = group_end(g); tflush = t; load_bx(g); }
      };

      set_m0(LB + ringb);
      issue_rows(idx_load(0));
      Raw rw = raw_load(0);
      u32x2 ps = pos_load(rw);
      f32x4 pd = pd_load(rw);
      SB();
#pragma unroll
      for (int t = 0; t < 2; ++t)
#pragma unroll
        for (int s = 0; s < 4; ++s) {
          const unsigned o = L_W + 8192 + (16 * s + 8 * th + tq) * 128 + (32 * t + 16 * tu + 4 * tp) * 2;
          const u32x2 b0 = lds_tr(L, o), b1 = lds_tr(L, o + 512);
          wq[t][s] = as_frag(u32x4{b0[0], b0[1], b1[0], b1[1]});
        }
      load_bx(group_of(ta));
      tiles_init();
      SB();
      wait_ps(ps);
      Prep pr = prep(rw, ps, pd);
      rw = raw_load(2);
      Ops A = read_ops(pr.saA, pr.sdA, pr.de), B;
#pragma unroll
      for (int j = 0; j < 8; ++j) B.as1[j] = 0;
      asm volatile("" : "+v"(B.as1));
      const int glast = group_of(tb - 1);
      const Idx ix0 = idx_load(1);
      chain_y0(A.ax, bxA0, A.q0, wq[0][0], A.q1, wq[0][1], A.q2, wq[0][2], A.q3, wq[0][3]);
      asm volatile("s_waitcnt lgkmcnt(0)" ::: "memory");
      issue_rows(ix0);
      STAMP(13, 1);
      CSTAMP(10, 1);
      for (int t = ta;; t += 2) {
        alpha(t, A, B.as1, bxA1, [&]() { ps = pos_load(rw); });
        wait_ps(ps);
        next_group(t + 1);
        const Idx ixa = idx_load(2);
        B = read_ops(pr.saB, pr.sdB, pr.de);
        pd = pd_load(rw);
        SB();
        beta(A, B, bxB0, [&]() { issue_rows(ixa); });
        if (__builtin_expect(t + 1 >= tb, 0)) { last_block(A.as1); break; }
        alpha(t + 1, B, A.as1, bxB1, [&]() { pr = prep(rw, ps, pd); });
        wait_vm0();
        next_group(t + 2);
        const Idx ixb = idx_load(3);
        A = read_ops(pr.saA, pr.sdA, pr.de);
        rw = raw_load(4);
        SB();
        beta(B, A, bxA0, [&]() { issue_rows(ixb); });
        if (__builtin_expect(t + 2 >= tb, 0)) { last_block(B.as1); break; }
        a_idx += 512;
        a_raw += 512;
      }
      flush_last(glast);
      wait_vm0();
      const char* wh = karg<const char>(4);
      dma_piece(wh + w * 2048, lane * 16, LB + ringb);
      dma_piece(wh + w * 2048 + 1024, lane * 16, LB + ringb + 1024);
      STAMP(14, 1);
      CSTAMP(11, 1);
    } else {
      const char* wh = karg<const char>(4);
      dma_piece(wh + w * 2048, lane * 16, LB + L_RING + w * 4096);
      dma_piece(wh + w * 2048 + 1024, lane * 16, LB + L_RING + w * 4096 + 1024);
    }
    pa += np;
    if (pa >= NP) break;
    wait_vm0();
    __syncthreads();
  }
  wait_vm0();
  __syncthreads();
  STAMP(15, 1);

  u16* out = karg<u16>(6);
  int tid = tid_;
  asm volatile("" : "+v"(tid));
  const int lane = tid & 63, r = lane & 31, hf = lane >> 5;
  if (w < 4) {
    const int g16 = lane >> 4, tq = (lane & 15) >> 2, tp = lane & 3, tu = g16 & 1, th = g16 >> 1;
    const int u = w >> 1, t = w & 1;
    const int nl = 32 * u + r, nlc = nl < BN ? nl : BN - 1;
    const unsigned hrow = L_H + nlc * 128;
    f32x16 d;
#pragma unroll
    for (int i = 0; i < 16; ++i) d[i] = 0.f;
    const unsigned w_tr = L_RING + (8 * th + tq) * 128 + (32 * t + 16 * tu + 4 * tp) * 2;
#pragma unroll
    for (int s = 0; s < 8; ++s) {
      const u32x2 a0 = lds_tr(L, w_tr + s * 4096), a1 = lds_tr(L, w_tr + s * 4096 + 512);
      const bf16x8 a = as_frag(u32x4{a0[0], a0[1], a1[0], a1[1]});
      bf16x8 bb;
      if (s < 4) {
        bb = lds_ld<bf16x8>(L, hrow + 32 * s + 16 * hf);
      } else {
        const unsigned o = L_ACC + nlc * ACC_NODE + 3 * 256 + (16 * (s - 4) + 8 * hf) * 4;
        const f32x4 m0 = lds_ld<f32x4>(L, o) * NLN2, m1 = lds_ld<f32x4>(L, o + 16) * NLN2;
        bb = as_frag(u32x4{cvt_pk(m0[0], m0[1]), cvt_pk(m0[2], m0[3]), cvt_pk(m1[0], m1[1]), cvt_pk(m1[2], m1[3])});
      }
      d = mfma(a, bb, d);
    }
    if (nl < BN) {
#pragma unroll
      for (int q = 0; q < 4; ++q) {
        const int f = 32 * t + 8 * q + 4 * hf;
        const u32x2 hv = lds_ld<u32x2>(L, hrow + 2 * f);
        float o4[4];
#pragma unroll
        for (int k = 0; k < 4; ++k) {
          const float act = bf_round(silu_f(bf_round(d[4 * q + k])));
          const float hres = (k & 1) ? bf_hi(hv[k >> 1]) : bf_lo(hv[k >> 1]);
          o4[k] = hres + act;
        }
        *(u32x2*)(out + (size_t)(n0 + nl) * 64 + f) = u32x2{cvt_pk(o4[0], o4[1]), cvt_pk(o4[2], o4[3])};
      }
    }
  } else {
    const int idx = tid - 256, nc = idx >> 1, half = idx & 1;
    const int ncc = nc < 3 * BN ? nc : 3 * BN - 1;
    const int n = ncc / 3, c = ncc - 3 * n;
    const unsigned o = L_ACC + n * ACC_NODE + c * 256 + half * 128;
    float s = 0.f;
#pragma unroll
    for (int k = 0; k < 8; ++k) {
      const f32x4 v = lds_ld<f32x4>(L, o + 16 * k), x = lds_ld<f32x4>(L, L_WX + half * 128 + 16 * k);
      s += v[0] * x[0] + v[1] * x[1] + v[2] * x[2] + v[3] * x[3];
    }
    s += __builtin_bit_cast(float, __builtin_amdgcn_update_dpp(0, __builtin_bit_cast(int, s), 0xb1, 0xf, 0xf, true));
    if (nc < 3 * BN && half == 0) {
      const float p0 = lds_ld<float>(L, L_PD + 16 * n + 4 * c);
      out[(size_t)NN * 64 + (size_t)n0 * 3 + nc] = (u16)cvt_pk(p0 + bf_round(s * NLN2), 0.f);
    }
  }
}

}

extern "C" void kernel_launch(void* const* d_in, const int* in_sizes, int n_in, void* d_out, int out_size, void* d_ws, size_t ws_size,
                              hipStream_t stream) {
  const u16* h = (const u16*)d_in[0];
  const u16* pos = (const u16*)d_in[1];
  const u16* ea = (const u16*)d_in[2];
  const int* src = (const int*)d_in[3];
  const int* dst = (const int*)d_in[4];
  const u16* we = (const u16*)d_in[5];
  const u16* wx = (const u16*)d_in[6];
  const u16* wh = (const u16*)d_in[7];
  egnn_kernel<<<NBK, 512, 0, stream>>>(h, pos, we, wx, wh, (char*)d_ws, (u16*)d_out, src, dst, ea);
}
```

```cpp
#include <hip/hip_runtime.h>
#include <hip/hip_bf16.h>
#include <stdint.h>

#define DI __device__ __forceinline__
#define AS3 __attribute__((address_space(3)))
#define SB() __builtin_amdgcn_sched_barrier(0)

typedef short bf16x8 __attribute__((ext_vector_type(8)));
typedef short s16x4 __attribute__((ext_vector_type(4)));
typedef float f32x16 __attribute__((ext_vector_type(16)));
typedef float f32x4 __attribute__((ext_vector_type(4)));
typedef float f32x2 __attribute__((ext_vector_type(2)));
typedef unsigned u32x4 __attribute__((ext_vector_type(4)));
typedef unsigned u32x3 __attribute__((ext_vector_type(3)));
typedef unsigned u32x2 __attribute__((ext_vector_type(2)));
typedef int i32x4 __attribute__((ext_vector_type(4)));
typedef __bf16 bf16x2v __attribute__((ext_vector_type(2)));
typedef unsigned short u16;

namespace {

constexpr int NN = 10000;
constexpr int NE = 640000;
constexpr int BN = 40;
constexpr int NBK = NN / BN;
constexpr int NP = NBK;
constexpr int CH = NE / NP;
static_assert(CH == 5 * 512 && CH < 4096, "five edges per thread, 12-bit run starts");
constexpr float NLOG2E = -1.4426950408889634f;
constexpr float NLN2 = -0.6931471805599453f;
constexpr unsigned SPIN_LIMIT = 1u << 20;
#define POLL_SLEEP 4

constexpr unsigned WS_CTL = 0;
constexpr unsigned WS_ENT = 256;
constexpr unsigned WS_RECS = WS_ENT + NBK * 4096;
constexpr unsigned REC_STRIDE = CH * 6;
constexpr unsigned WS_END = WS_RECS + NP * REC_STRIDE;
static_assert(WS_RECS % 128 == 0 && REC_STRIDE % 128 == 0, "no cache line holds records of two producers");

constexpr int CAP = 2880;
constexpr int PADS = 160;
constexpr int L_S = 0;
constexpr int S_WAVE = 2560;
constexpr int L_PD = 20480;
constexpr int L_WT = 21248;
constexpr int L_LOCK = 21312;
constexpr int L_TAG = 21344;
constexpr int L_WX = 21504;
constexpr int L_TBL = 21760;
constexpr int L_RAW = L_TBL + 4096;
constexpr int L_PFR = L_RAW + (CAP + PADS) * 8;
constexpr int L_RING = L_PFR + 5120;
constexpr int L_ACC = L_RING + 8 * 4096;
constexpr int ACC_NODE = 1040;
constexpr int L_W = L_ACC + BN * ACC_NODE;
constexpr int L_H = L_W + 16384;
constexpr int L_SEG = L_H + BN * 128;
constexpr int L_END = L_SEG + 1024;
static_assert(L_PD % 1024 == 0, "group id is read from bits 7..9 of the pos address");
static_assert(L_RAW + (CAP + PADS) * 8 < 65536 && L_RAW >= 8 * CH && CAP + PADS >= CH, "run table holds 16-bit LDS addresses");
static_assert(L_PFR % 16 == 0 && L_RING % 16 == 0 && L_ACC % 16 == 0 && L_W % 16 == 0 && L_END <= 163840, "LDS map");

DI unsigned cvt_pk(float a, float b) { f32x2 v = {a, b}; return __builtin_bit_cast(unsigned, __builtin_convertvector(v, bf16x2v)); }
DI float as_f(unsigned u) { return __builtin_bit_cast(float, u); }
DI float bf_lo(unsigned u) { return __builtin_bit_cast(float, u << 16); }
DI float bf_hi(unsigned u) { return __builtin_bit_cast(float, u & 0xffff0000u); }
DI float bf_round(float x) { return bf_lo(cvt_pk(x, x)); }
DI float bf_ld(const u16* p) { return __builtin_bit_cast(float, (unsigned)*p << 16); }
DI f32x16 mfma(bf16x8 a, bf16x8 b, f32x16 c) { return __builtin_amdgcn_mfma_f32_32x32x16_bf16(a, b, c, 0, 0, 0); }
DI bf16x8 as_frag(u32x4 v) { return __builtin_bit_cast(bf16x8, v); }

DI unsigned wave_incl_scan(unsigned x) {
  x += __builtin_amdgcn_update_dpp(0, (int)x, 0x111, 0xf, 0xf, true);
  x += __builtin_amdgcn_update_dpp(0, (int)x, 0x112, 0xf, 0xf, true);
  x += __builtin_amdgcn_update_dpp(0, (int)x, 0x114, 0xf, 0xf, true);
  x += __builtin_amdgcn_update_dpp(0, (int)x, 0x118, 0xf, 0xf, true);
  x += __builtin_amdgcn_update_dpp(0, (int)x, 0x142, 0xa, 0xf, false);
  x += __builtin_amdgcn_update_dpp(0, (int)x, 0x143, 0xc, 0xf, false);
  return x;
}

template <typename T> DI T lds_ld(AS3 char* L, unsigned off) { return *(AS3 T*)(L + off); }
template <typename T> DI void lds_st(AS3 char* L, unsigned off, T v) { *(AS3 T*)(L + off) = v; }
DI u32x2 lds_tr(AS3 char* L, unsigned off) {
  return __builtin_bit_cast(u32x2, __builtin_amdgcn_ds_read_tr16_b64_v4i16((AS3 s16x4*)(L + off)));
}

DI const char* uni_ptr(const char* p) {
  const unsigned long long v = (unsigned long long)p;
  const unsigned lo = __builtin_amdgcn_readfirstlane((int)(unsigned)v), hi = __builtin_amdgcn_readfirstlane((int)(unsigned)(v >> 32));
  return (const char*)(__attribute__((address_space(1))) const char*)(((unsigned long long)hi << 32) | lo);
}
DI void dma_piece(const char* base_, unsigned voff, unsigned ldsaddr_) {
  const char* base = uni_ptr(base_);
  const unsigned ldsaddr = __builtin_amdgcn_readfirstlane((int)ldsaddr_);
  unsigned keep;
  asm volatile("s_mov_b32 %0, m0\n\ts_mov_b32 m0, %3\n\ts_nop 0\n\tglobal_load_lds_dwordx4 %1, %2\n\ts_mov_b32 m0, %0"
               : "=&s"(keep) : "v"(voff), "s"(base), "s"(ldsaddr) : "memory");
}
DI void set_m0(unsigned ldsaddr) { asm volatile("s_mov_b32 m0, %0\n\ts_nop 0" :: "s"(ldsaddr) : "memory"); }
DI void dma_tile(const char* base, unsigned v0, unsigned v1, unsigned v2, unsigned v3) {
  asm volatile("global_load_lds_dwordx4 %0, %4\n\t"
               "global_load_lds_dwordx4 %1, %5 offset:1024\n\t"
               "global_load_lds_dwordx4 %2, %6 offset:2048\n\t"
               "global_load_lds_dwordx4 %3, %7 offset:3072"
               :: "v"(v0), "v"(v1), "v"(v2), "v"(v3), "s"(base), "s"(base - 1024), "s"(base - 2048), "s"(base - 3072)
               : "memory");
}
#define STAMP(k, kern)
#define CSTAMP(k, kern)
typedef __amdgpu_buffer_rsrc_t rsrc_t;
DI rsrc_t ws_rsrc(const char* ws) { return __builtin_amdgcn_make_buffer_rsrc((void*)ws, 0, WS_END, 0x00020000); }
#define LD_AUX 16
#define ST_AUX 16
DI u32x4 ld16_dev(rsrc_t r, unsigned off) { return __builtin_amdgcn_raw_buffer_load_b128(r, off, 0, 16); }
DI u32x3 ld12_rec(rsrc_t r, unsigned off) { return __builtin_amdgcn_raw_buffer_load_b96(r, off, 0, LD_AUX); }
DI unsigned ld4_rec(rsrc_t r, unsigned off) { return __builtin_amdgcn_raw_buffer_load_b32(r, off, 0, LD_AUX); }
DI unsigned ld2_rec(rsrc_t r, unsigned off) { return __builtin_amdgcn_raw_buffer_load_b16(r, off, 0, LD_AUX); }
DI u32x2 ld8_dev(rsrc_t r, unsigned off) { return __builtin_amdgcn_raw_buffer_load_b64(r, off, 0, 16); }
DI void st16_dev(rsrc_t r, unsigned off, u32x4 v) { __builtin_amdgcn_raw_buffer_store_b128(v, r, off, 0, 16); }
DI void st16_rec(rsrc_t r, unsigned off, u32x4 v) { __builtin_amdgcn_raw_buffer_store_b128(v, r, off, 0, ST_AUX); }
DI void wait_vm0() { asm volatile("s_waitcnt vmcnt(0)" ::: "memory"); }

struct Prep {
  unsigned sdA, sdB;
  unsigned saA, saB;
  unsigned de;
};
struct Raw { unsigned src, pda, w1, n320; };

DI float silu_f(float x) { return x * __builtin_amdgcn_rcpf(1.0f + __builtin_amdgcn_exp2f(x * NLOG2E)); }

#pragma clang diagnostic ignored "-Winline-asm"
#define T4(op, d, s0, s1, s2, s3, d1, d2, d3) op " v" #d ", v" #s0 "\n\t" op " v" #d1 ", v" #s1 "\n\t" op " v" #d2 ", v" #s2 "\n\t" op " v" #d3 ", v" #s3 "\n\t"
#define EXP_LO(a, b, c, d) T4("v_exp_f32", 184, a, b, c, d, 185, 186, 187)
#define EXP_HI(a, b, c, d) T4("v_exp_f32", 188, a, b, c, d, 189, 190, 191)
#define RCP_LO T4("v_rcp_f32", 184, 184, 185, 186, 187, 185, 186, 187)
#define RCP_HI T4("v_rcp_f32", 188, 188, 189, 190, 191, 189, 190, 191)
#define PKA(a, b) "v_pk_add_f32 v[" #a ":" #b "], v[" #a ":" #b "], 1.0 op_sel_hi:[1,0]\n\t"
#define ADD1 PKA(184, 185) PKA(186, 187) PKA(188, 189) PKA(190, 191)
#define PKM(a, b, c, d) "v_pk_mul_f32 v[" #a ":" #b "], v[" #a ":" #b "], v[" #c ":" #d "]\n\t"
#define MULY(a0, a1, a2, a3, a4, a5, a6, a7) PKM(184, 185, a0, a1) PKM(186, 187, a2, a3) PKM(188, 189, a4, a5) PKM(190, 191, a6, a7)
#define CVT(d, a, b) "v_cvt_pk_bf16_f32 v" #d ", v" #a ", v" #b "\n\t"
#define PACK(d0, d1, d2, d3) CVT(d0, 184, 185) CVT(d1, 186, 187) CVT(d2, 188, 189) CVT(d3, 190, 191)
#define MF(acc, a, b) "v_mfma_f32_32x32x16_bf16 " acc ", " a ", " b ", " acc "\n\t"
#define MF0(acc, a, b) "v_mfma_f32_32x32x16_bf16 " acc ", " a ", " b ", 0\n\t"
#define Y0 "v[192:207]"
#define Y1 "v[208:223]"
#define ACC0 "v[224:239]"
#define ACC1 "v[240:255]"
#define X0 "v[176:179]"
#define X1 "v[180:183]"

DI void tiles_init() {
  asm volatile(
               "v_mov_b64 v[224:225], 0\n\t"
               "v_mov_b64 v[226:227], 0\n\t"
               "v_mov_b64 v[228:229], 0\n\t"
               "v_mov_b64 v[230:231], 0\n\t"
               "v_mov_b64 v[232:233], 0\n\t"
               "v_mov_b64 v[234:235], 0\n\t"
               "v_mov_b64 v[236:237], 0\n\t"
               "v_mov_b64 v[238:239], 0\n\t"
               "v_mov_b64 v[240:241], 0\n\t"
               "v_mov_b64 v[242:243], 0\n\t"
               "v_mov_b64 v[244:245], 0\n\t"
               "v_mov_b64 v[246:247], 0\n\t"
               "v_mov_b64 v[248:249], 0\n\t"
               "v_mov_b64 v[250:251], 0\n\t"
               "v_mov_b64 v[252:253], 0\n\t"
               "v_mov_b64 v[254:255], 0\n\t"
               "v_mov_b64 v[180:181], 0\n\t"
               "v_mov_b64 v[182:183], 0\n\t"
               "s_nop 1" ::: "v176", "v255");
}
DI void alpha1(const bf16x8& asl) {
  asm volatile(EXP_LO(192, 193, 194, 195) MF(ACC1, "%[asl]", X1) EXP_HI(196, 197, 198, 199) :: [asl] "v"(asl));
}
DI void alpha2(const bf16x8& ax, const bf16x8& bx, const bf16x8& q0, const bf16x8& w0) {
  asm volatile(ADD1 MF0(Y1, "%[ax]", "%[bx]") RCP_LO MF(Y1, "%[q0]", "%[w0]") RCP_HI
               MULY(192, 193, 194, 195, 196, 197, 198, 199) PACK(176, 177, 178, 179)
               :: [ax] "v"(ax), [bx] "v"(bx), [q0] "v"(q0), [w0] "v"(w0));
}
DI void alpha3(const bf16x8& q1, const bf16x8& w1, const bf16x8& q2, const bf16x8& w2, const bf16x8& q3, const bf16x8& w3, const bf16x8& as0) {
  asm volatile(MF(Y1, "%[q1]", "%[w1]") EXP_LO(200, 201, 202, 203) MF(Y1, "%[q2]", "%[w2]") EXP_HI(204, 205, 206, 207) ADD1
               MF(Y1, "%[q3]", "%[w3]") RCP_LO MF(ACC0, "%[as]", X0) RCP_HI
               MULY(200, 201, 202, 203, 204, 205, 206, 207) PACK(180, 181, 182, 183)
               :: [q1] "v"(q1), [w1] "v"(w1), [q2] "v"(q2), [w2] "v"(w2), [q3] "v"(q3), [w3] "v"(w3), [as] "v"(as0));
}
DI void beta1(const bf16x8& as1) {
  asm volatile(EXP_LO(208, 209, 210, 211) MF(ACC0, "%[as]", X1) EXP_HI(212, 213, 214, 215) ADD1 :: [as] "v"(as1));
}
DI void beta2(const bf16x8& ax, const bf16x8& bx, const bf16x8& q0, const bf16x8& w0) {
  asm volatile(MF0(Y0, "%[ax]", "%[bx]") RCP_LO MF(Y0, "%[q0]", "%[w0]") RCP_HI
               MULY(208, 209, 210, 211, 212, 213, 214, 215) PACK(176, 177, 178, 179)
               :: [ax] "v"(ax), [bx] "v"(bx), [q0] "v"(q0), [w0] "v"(w0));
}
DI void beta3(const bf16x8& q1, const bf16x8& w1, const bf16x8& q2, const bf16x8& w2, const bf16x8& q3, const bf16x8& w3, const bf16x8& as0) {
  asm volatile(MF(Y0, "%[q1]", "%[w1]") EXP_LO(216, 217, 218, 219) MF(Y0, "%[q2]", "%[w2]") EXP_HI(220, 221, 222, 223) ADD1
               MF(Y0, "%[q3]", "%[w3]") RCP_LO MF(ACC1, "%[as]", X0) RCP_HI
               MULY(216, 217, 218, 219, 220, 221, 222, 223) PACK(180, 181, 182, 183)
               :: [q1] "v"(q1), [w1] "v"(w1), [q2] "v"(q2), [w2] "v"(w2), [q3] "v"(q3), [w3] "v"(w3), [as] "v"(as0));
}
DI void chain_y0(const bf16x8& ax, const bf16x8& bx, const bf16x8& q0, const bf16x8& w0, const bf16x8& q1, const bf16x8& w1,
                 const bf16x8& q2, const bf16x8& w2, const bf16x8& q3, const bf16x8& w3) {
  asm volatile("s_nop 1\n\t" MF0(Y0, "%[ax]", "%[bx]") MF(Y0, "%[q0]", "%[w0]") MF(Y0, "%[q1]", "%[w1]") MF(Y0, "%[q2]", "%[w2]")
               MF(Y0, "%[q3]", "%[w3]") "s_nop 15"
               :: [ax] "v"(ax), [bx] "v"(bx), [q0] "v"(q0), [w0] "v"(w0), [q1] "v"(q1), [w1] "v"(w1), [q2] "v"(q2), [w2] "v"(w2),
                  [q3] "v"(q3), [w3] "v"(w3));
}
DI void last_block(const bf16x8& as1) { asm volatile("s_nop 1\n\t" MF(ACC1, "%[as]", X1) :: [as] "v"(as1)); }
DI void acc_gather(unsigned a0, unsigned a1, unsigned a2, unsigned a3) {
  f32x2 t0, t1, t2, t3, t4, t5, t6, t7;
  asm volatile(
               "ds_read2_b32 %[t0], %[a0] offset0:0 offset1:64\n\t"
               "ds_read2_b32 %[t1], %[a0] offset0:128 offset1:192\n\t"
               "ds_read2_b32 %[t2], %[a0] offset0:32 offset1:96\n\t"
               "ds_read2_b32 %[t3], %[a0] offset0:160 offset1:224\n\t"
               "ds_read2_b32 %[t4], %[a1] offset0:0 offset1:64\n\t"
               "ds_read2_b32 %[t5], %[a1] offset0:128 offset1:192\n\t"
               "ds_read2_b32 %[t6], %[a1] offset0:32 offset1:96\n\t"
               "ds_read2_b32 %[t7], %[a1] offset0:160 offset1:224\n\t"
               "s_waitcnt lgkmcnt(4)\n\t"
               "v_pk_add_f32 v[224:225], v[224:225], %[t0]\n\t"
               "v_pk_add_f32 v[226:227], v[226:227], %[t1]\n\t"
               "v_pk_add_f32 v[240:241], v[240:241], %[t2]\n\t"
               "v_pk_add_f32 v[242:243], v[242:243], %[t3]\n\t"
               "s_waitcnt lgkmcnt(0)\n\t"
               "v_pk_add_f32 v[228:229], v[228:229], %[t4]\n\t"
               "v_pk_add_f32 v[230:231], v[230:231], %[t5]\n\t"
               "v_pk_add_f32 v[244:245], v[244:245], %[t6]\n\t"
               "v_pk_add_f32 v[246:247], v[246:247], %[t7]\n\t"
               "ds_read2_b32 %[t0], %[a2] offset0:0 offset1:64\n\t"
               "ds_read2_b32 %[t1], %[a2] offset0:128 offset1:192\n\t"
               "ds_read2_b32 %[t2], %[a2] offset0:32 offset1:96\n\t"
               "ds_read2_b32 %[t3], %[a2] offset0:160 offset1:224\n\t"
               "ds_read2_b32 %[t4], %[a3] offset0:0 offset1:64\n\t"
               "ds_read2_b32 %[t5], %[a3] offset0:128 offset1:192\n\t"
               "ds_read2_b32 %[t6], %[a3] offset0:32 offset1:96\n\t"
               "ds_read2_b32 %[t7], %[a3] offset0:160 offset1:224\n\t"
               "s_waitcnt lgkmcnt(4)\n\t"
               "v_pk_add_f32 v[232:233], v[232:233], %[t0]\n\t"
               "v_pk_add_f32 v[234:235], v[234:235], %[t1]\n\t"
               "v_pk_add_f32 v[248:249], v[248:249], %[t2]\n\t"
               "v_pk_add_f32 v[250:251], v[250:251], %[t3]\n\t"
               "s_waitcnt lgkmcnt(0)\n\t"
               "v_pk_add_f32 v[236:237], v[236:237], %[t4]\n\t"
               "v_pk_add_f32 v[238:239], v[238:239], %[t5]\n\t"
               "v_pk_add_f32 v[252:253], v[252:253], %[t6]\n\t"
               "v_pk_add_f32 v[254:255], v[254:255], %[t7]\n\t"
               "s_nop 0"
               : [t0] "=&v"(t0), [t1] "=&v"(t1), [t2] "=&v"(t2), [t3] "=&v"(t3), [t4] "=&v"(t4), [t5] "=&v"(t5), [t6] "=&v"(t6), [t7] "=&v"(t7)
               : [a0] "v"(a0), [a1] "v"(a1), [a2] "v"(a2), [a3] "v"(a3) : "memory");
}
DI void acc_store(unsigned a0, unsigned a1, unsigned a2, unsigned a3) {
  asm volatile(
               "ds_write2_b32 %[a0], v224, v225 offset0:0 offset1:64\n\t"
               "ds_write2_b32 %[a0], v226, v227 offset0:128 offset1:192\n\t"
               "ds_write2_b32 %[a0], v240, v241 offset0:32 offset1:96\n\t"
               "ds_write2_b32 %[a0], v242, v243 offset0:160 offset1:224\n\t"
               "ds_write2_b32 %[a1], v228, v229 offset0:0 offset1:64\n\t"
               "ds_write2_b32 %[a1], v230, v231 offset0:128 offset1:192\n\t"
               "ds_write2_b32 %[a1], v244, v245 offset0:32 offset1:96\n\t"
               "ds_write2_b32 %[a1], v246, v247 offset0:160 offset1:224\n\t"
               "ds_write2_b32 %[a2], v232, v233 offset0:0 offset1:64\n\t"
               "ds_write2_b32 %[a2], v234, v235 offset0:128 offset1:192\n\t"
               "ds_write2_b32 %[a2], v248, v249 offset0:32 offset1:96\n\t"
               "ds_write2_b32 %[a2], v250, v251 offset0:160 offset1:224\n\t"
               "ds_write2_b32 %[a3], v236, v237 offset0:0 offset1:64\n\t"
               "ds_write2_b32 %[a3], v238, v239 offset0:128 offset1:192\n\t"
               "ds_write2_b32 %[a3], v252, v253 offset0:32 offset1:96\n\t"
               "ds_write2_b32 %[a3], v254, v255 offset0:160 offset1:224\n\t"
               "s_waitcnt lgkmcnt(0)\n\t"
               "v_mov_b64 v[224:225], 0\n\t"
               "v_mov_b64 v[226:227], 0\n\t"
               "v_mov_b64 v[228:229], 0\n\t"
               "v_mov_b64 v[230:231], 0\n\t"
               "v_mov_b64 v[232:233], 0\n\t"
               "v_mov_b64 v[234:235], 0\n\t"
               "v_mov_b64 v[236:237], 0\n\t"
               "v_mov_b64 v[238:239], 0\n\t"
               "v_mov_b64 v[240:241], 0\n\t"
               "v_mov_b64 v[242:243], 0\n\t"
               "v_mov_b64 v[244:245], 0\n\t"
               "v_mov_b64 v[246:247], 0\n\t"
               "v_mov_b64 v[248:249], 0\n\t"
               "v_mov_b64 v[250:251], 0\n\t"
               "v_mov_b64 v[252:253], 0\n\t"
               "v_mov_b64 v[254:255], 0\n\t"
               "s_nop 7"
               :: [a0] "v"(a0), [a1] "v"(a1), [a2] "v"(a2), [a3] "v"(a3) : "memory");
}
DI void acc_gather_last(unsigned a0, unsigned a1, unsigned a2, unsigned a3) {
  asm volatile(
               "ds_read2_b32 v[176:177], %[a0] offset0:0 offset1:64\n\t"
               "ds_read2_b32 v[178:179], %[a0] offset0:128 offset1:192\n\t"
               "ds_read2_b32 v[180:181], %[a0] offset0:32 offset1:96\n\t"
               "ds_read2_b32 v[182:183], %[a0] offset0:160 offset1:224\n\t"
               "ds_read2_b32 v[184:185], %[a1] offset0:0 offset1:64\n\t"
               "ds_read2_b32 v[186:187], %[a1] offset0:128 offset1:192\n\t"
               "ds_read2_b32 v[188:189], %[a1] offset0:32 offset1:96\n\t"
               "ds_read2_b32 v[190:191], %[a1] offset0:160 offset1:224\n\t"
               "ds_read2_b32 v[192:193], %[a2] offset0:0 offset1:64\n\t"
               "ds_read2_b32 v[194:195], %[a2] offset0:128 offset1:192\n\t"
               "ds_read2_b32 v[196:197], %[a2] offset0:32 offset1:96\n\t"
               "ds_read2_b32 v[198:199], %[a2] offset0:160 offset1:224\n\t"
               "ds_read2_b32 v[200:201], %[a3] offset0:0 offset1:64\n\t"
               "ds_read2_b32 v[202:203], %[a3] offset0:128 offset1:192\n\t"
               "ds_read2_b32 v[204:205], %[a3] offset0:32 offset1:96\n\t"
               "ds_read2_b32 v[206:207], %[a3] offset0:160 offset1:224\n\t"
               "s_waitcnt lgkmcnt(12)\n\t"
               "v_pk_add_f32 v[224:225], v[224:225], v[176:177]\n\t"
               "v_pk_add_f32 v[226:227], v[226:227], v[178:179]\n\t"
               "v_pk_add_f32 v[240:241], v[240:241], v[180:181]\n\t"
               "v_pk_add_f32 v[242:243], v[242:243], v[182:183]\n\t"
               "s_waitcnt lgkmcnt(8)\n\t"
               "v_pk_add_f32 v[228:229], v[228:229], v[184:185]\n\t"
               "v_pk_add_f32 v[230:231], v[230:231], v[186:187]\n\t"
               "v_pk_add_f32 v[244:245], v[244:245], v[188:189]\n\t"
               "v_pk_add_f32 v[246:247], v[246:247], v[190:191]\n\t"
               "s_waitcnt lgkmcnt(4)\n\t"
               "v_pk_add_f32 v[232:233], v[232:233], v[192:193]\n\t"
               "v_pk_add_f32 v[234:235], v[234:235], v[194:195]\n\t"
               "v_pk_add_f32 v[248:249], v[248:249], v[196:197]\n\t"
               "v_pk_add_f32 v[250:251], v[250:251], v[198:199]\n\t"
               "s_waitcnt lgkmcnt(0)\n\t"
               "v_pk_add_f32 v[236:237], v[236:237], v[200:201]\n\t"
               "v_pk_add_f32 v[238:239], v[238:239], v[202:203]\n\t"
               "v_pk_add_f32 v[252:253], v[252:253], v[204:205]\n\t"
               "v_pk_add_f32 v[254:255], v[254:255], v[206:207]\n\t"
               "s_nop 0"
               :: [a0] "v"(a0), [a1] "v"(a1), [a2] "v"(a2), [a3] "v"(a3) : "memory");
}
DI void acc_store_last(unsigned a0, unsigned a1, unsigned a2, unsigned a3) {
  asm volatile(
               "ds_write2_b32 %[a0], v224, v225 offset0:0 offset1:64\n\t"
               "ds_write2_b32 %[a0], v226, v227 offset0:128 offset1:192\n\t"
               "ds_write2_b32 %[a0], v240, v241 offset0:32 offset1:96\n\t"
               "ds_write2_b32 %[a0], v242, v243 offset0:160 offset1:224\n\t"
               "ds_write2_b32 %[a1], v228, v229 offset0:0 offset1:64\n\t"
               "ds_write2_b32 %[a1], v230, v231 offset0:128 offset1:192\n\t"
               "ds_write2_b32 %[a1], v244, v245 offset0:32 offset1:96\n\t"
               "ds_write2_b32 %[a1], v246, v247 offset0:160 offset1:224\n\t"
               "ds_write2_b32 %[a2], v232, v233 offset0:0 offset1:64\n\t"
               "ds_write2_b32 %[a2], v234, v235 offset0:128 offset1:192\n\t"
               "ds_write2_b32 %[a2], v248, v249 offset0:32 offset1:96\n\t"
               "ds_write2_b32 %[a2], v250, v251 offset0:160 offset1:224\n\t"
               "ds_write2_b32 %[a3], v236, v237 offset0:0 offset1:64\n\t"
               "ds_write2_b32 %[a3], v238, v239 offset0:128 offset1:192\n\t"
               "ds_write2_b32 %[a3], v252, v253 offset0:32 offset1:96\n\t"
               "ds_write2_b32 %[a3], v254, v255 offset0:160 offset1:224\n\t"
               "s_nop 0"
               :: [a0] "v"(a0), [a1] "v"(a1), [a2] "v"(a2), [a3] "v"(a3) : "memory");
}
DI unsigned lds_lock(unsigned addr) {
  unsigned t, old;
  unsigned long long sv;
  asm volatile("s_mov_b64 %2, exec\n\ts_mov_b64 exec, 1\n"
               "1:\n\tds_wrxchg_rtn_b32 %0, %3, %4\n\ts_waitcnt lgkmcnt(0)\n\tv_cmp_eq_u32 vcc, 1, %0\n\ts_cbranch_vccz 2f\n\t"
               "s_sleep 1\n\ts_branch 1b\n"
               "2:\n\tv_readfirstlane_b32 %1, %0\n\ts_mov_b64 exec, %2\n\ts_nop 15"
               : "=&v"(t), "=&s"(old), "=&s"(sv) : "v"(addr), "v"(1u) : "vcc", "memory");
  return old;
}

template <typename T> DI T* karg(int k) {
  typedef __attribute__((address_space(4))) const unsigned long long* kp_t;
  kp_t kp = (kp_t)__builtin_amdgcn_kernarg_segment_ptr();
  asm volatile("" : "+s"(kp));
  return (T*)(__attribute__((address_space(1))) T*)kp[k];
}

__global__ __launch_bounds__(512) __attribute__((amdgpu_num_vgpr(88))) void egnn_kernel(
    const u16* __restrict__ h_, const u16* __restrict__ pos_, const u16* __restrict__ we_, const u16* __restrict__ wx_,
    const u16* __restrict__ wh_, char* __restrict__ ws_, u16* __restrict__ out_, const int* __restrict__ src_,
    const int* __restrict__ dst_, const u16* __restrict__ ea_) {
  __shared__ __attribute__((aligned(1024))) char lds[L_END];
  AS3 char* L = (AS3 char*)lds;
  const unsigned LB = (unsigned)(size_t)L;
  const int tid_ = threadIdx.x;
  const int w = __builtin_amdgcn_readfirstlane(tid_ >> 6);
  STAMP(0, 1);
  const int xl = blockIdx.x & 7;
  const int b = xl * 31 + (xl < 2 ? xl : 2) + (blockIdx.x >> 3);
  const int n0 = b * BN;
  unsigned cw[2][2];

  {
    const int tid = tid_, lane = tid & 63, r = lane & 31, hf = lane >> 5, p = blockIdx.x;
    const rsrc_t rs = ws_rsrc(ws_);
    const int e0 = p * CH + 4 * tid, e1 = p * CH + 2048 + tid;
    const i32x4 d4 = *(const i32x4*)(dst_ + e0), s4 = *(const i32x4*)(src_ + e0);
    const u32x2 a2 = *(const u32x2*)(ea_ + e0);
    unsigned dd[5] = {(unsigned)d4[0], (unsigned)d4[1], (unsigned)d4[2], (unsigned)d4[3], (unsigned)dst_[e1]};
    const unsigned s5 = src_[e1], a5 = ea_[e1];
    const u32x2 ctl = ld8_dev(rs, WS_CTL);
    SB();
    dma_piece((const char*)we_ + w * 1024, lane * 16, LB + L_W + w * 1024);
    if (w < 5) dma_piece((const char*)h_ + (size_t)n0 * 128 + w * 1024, lane * 16, LB + L_H + w * 1024);
    const u32x4 w2raw = *(const u32x4*)(we_ + 64 * 64 + tid * 8);
    unsigned sraw[3], craw[4];
#pragma unroll
    for (int t = 0; t < 2; ++t) { craw[2 * t] = we_[128 * 64 + 32 * t + r]; craw[2 * t + 1] = we_[129 * 64 + 32 * t + r]; }
    {
      const u16* pp = w == 1 ? wx_ + lane : pos_ + (lane < BN ? n0 + lane : 0) * 3;
      const int st = w == 1 ? 0 : 1;
      sraw[0] = pp[0]; sraw[1] = pp[st]; sraw[2] = pp[2 * st];
    }
    SB();
    {
      const u32x4 z4 = {0, 0, 0, 0};
      static_assert(8 * S_WAVE == 2 * 8192 + 256 * 16 && BN * ACC_NODE == 5 * 8192 + 40 * 16, "clearing pattern");
      if (tid < 256) lds_st<u32x4>(L, L_TBL + 16 * tid, z4);
      lds_st<u32x4>(L, L_S + tid * 16, z4);
      lds_st<u32x4>(L, L_S + 8192 + tid * 16, z4);
#pragma unroll
      for (int k = 0; k < 5; ++k) lds_st<u32x4>(L, L_ACC + k * 8192 + tid * 16, z4);
      if (tid < 256) lds_st<u32x4>(L, L_S + 16384 + tid * 16, z4);
      if (tid < 40) lds_st<u32x4>(L, L_ACC + 5 * 8192 + tid * 16, z4);
      if (tid < 8) lds_st<unsigned>(L, L_LOCK + 4 * tid, 0u);
    }
    __syncthreads();
    STAMP(1, 1);
    unsigned idx[5], rank[5], bk[5];
#pragma unroll
    for (int i = 0; i < 5; ++i) {
      const unsigned G = dd[i] >> 3;
      bk[i] = (G * 52429u) >> 18;
      idx[i] = G + 3 * bk[i];
      const unsigned sh = (idx[i] & 1) << 4;
      const unsigned old = __hip_atomic_fetch_add((AS3 unsigned*)(L + L_TBL + 4 * (idx[i] >> 1)), 1u << sh, __ATOMIC_RELAXED,
                                                  __HIP_MEMORY_SCOPE_WORKGROUP);
      rank[i] = (old >> sh) & 0xffffu;
    }
    __syncthreads();
    STAMP(2, 1);
    u32x4 c = {0, 0, 0, 0};
    unsigned tot = 0, incl = 0;
    if (w < 4) {
      c = lds_ld<u32x4>(L, L_TBL + 16 * tid);
      tot = (c[0] & 0xffffu) + (c[0] >> 16) + (c[1] & 0xffffu) + (c[1] >> 16) + (c[2] & 0xffffu);
      incl = wave_incl_scan(tot);
      if (lane == 63) lds_st<unsigned>(L, L_WT + 4 * w, incl);
    }
    __syncthreads();
    const unsigned tlo = ctl[0] + 1, thi = ctl[1] + (tlo == 0);
    const unsigned tag0 = tlo & 0xfffffffu, tag1 = ((tlo >> 28) | (thi << 4)) & 0xfffffffu;
    if (tid == 0) { lds_st<u32x4>(L, L_TAG, u32x4{tag0, tag1, 0u, 0u}); lds_st<u32x2>(L, L_TAG + 16, u32x2{tlo, thi}); }
    u32x4 entry = {0, 0, 0, 0};
    if (w < 4) {
      const u32x4 wt = lds_ld<u32x4>(L, L_WT);
      const unsigned s0 = incl - tot + (w > 0 ? wt[0] : 0) + (w > 1 ? wt[1] : 0) + (w > 2 ? wt[2] : 0);
      const unsigned s1 = s0 + (c[0] & 0xffffu), s2 = s1 + (c[0] >> 16), s3 = s2 + (c[1] & 0xffffu), s4_ = s3 + (c[1] >> 16);
      const unsigned s5_ = s0 + tot;
      lds_st<u32x3>(L, L_TBL + 16 * tid, u32x3{s0 | (s1 << 16), s2 | (s3 << 16), s4_});
      entry = u32x4{s0 | (s1 << 12) | (s2 << 24), (s2 >> 8) | (tag0 << 4), s3 | (s4_ << 12) | (s5_ << 24), (s5_ >> 8) | (tag1 << 4)};
    }
    __syncthreads();
    STAMP(3, 1);
    {
      unsigned ps[5];
#pragma unroll
      for (int i = 0; i < 5; ++i) ps[i] = lds_ld<u16>(L, L_TBL + 2 * idx[i]) + rank[i];
#pragma unroll
      for (int i = 0; i < 5; ++i) {
        const unsigned dl = dd[i] - BN * bk[i];
        const unsigned sv = i < 4 ? (unsigned)s4[i] : s5;
        const unsigned eav = i == 4 ? a5 : (i & 1) ? (a2[i >> 1] >> 16) : a2[i >> 1];
        const unsigned pb = L_RAW + 12 * (ps[i] >> 1), odd = ps[i] & 1;
        lds_st<unsigned>(L, pb + 4 * odd, sv | ((L_PD + 16 * dl) << 16));
        lds_st<u16>(L, pb + 8 + 2 * odd, (u16)eav);
      }
    }
    {
      u32x4 o;
#pragma unroll
      for (int k = 0; k < 4; ++k) o[k] = cvt_pk(bf_lo(w2raw[k]) * NLOG2E, bf_hi(w2raw[k]) * NLOG2E);
      lds_st<u32x4>(L, L_W + 8192 + tid * 16, o);
    }
    wait_vm0();
    __syncthreads();
    STAMP(4, 1);
    {
      static_assert(REC_STRIDE == (512 + 448) * 16, "copy pattern");
      const unsigned o = WS_RECS + p * REC_STRIDE + 16 * tid;
      st16_rec(rs, o, lds_ld<u32x4>(L, L_RAW + 16 * tid));
      if (tid < 448) st16_rec(rs, o + 8192, lds_ld<u32x4>(L, L_RAW + 8192 + 16 * tid));
    }
    SB();
    if (w >= 4) {
      const int u = (w - 4) >> 1, t = w & 1;
      f32x16 pacc;
#pragma unroll
      for (int i = 0; i < 16; ++i) pacc[i] = 0.f;
#pragma unroll
      for (int s = 0; s < 4; ++s) {
        const int nl = 32 * u + r, nlc = nl < BN ? nl : BN - 1;
        const bf16x8 ha = lds_ld<bf16x8>(L, L_H + nlc * 128 + 32 * s + 16 * hf);
        const unsigned o = L_W + (16 * s + 8 * (lane >> 5) + ((lane & 15) >> 2)) * 128 + (32 * t + 16 * ((lane >> 4) & 1) + 4 * (lane & 3)) * 2;
        const u32x2 b0 = lds_tr(L, o), b1 = lds_tr(L, o + 512);
        pacc = mfma(ha, as_frag(u32x4{b0[0], b0[1], b1[0], b1[1]}), pacc);
      }
#pragma unroll
      for (int q = 0; q < 4; ++q)
        if (4 * u + q < 5)
          lds_st<u32x2>(L, L_PFR + ((4 * u + q) * 2 + t) * 512 + lane * 8,
                        u32x2{cvt_pk(pacc[4 * q] * NLOG2E, pacc[4 * q + 1] * NLOG2E), cvt_pk(pacc[4 * q + 2] * NLOG2E, pacc[4 * q + 3] * NLOG2E)});
    } else if (w == 0) {
      if (lane <= BN)
        lds_st<f32x4>(L, L_PD + 16 * lane, f32x4{bf_lo(sraw[0]), bf_lo(sraw[1]), bf_lo(sraw[2]), lane < BN ? 1.0f : 0.0f});
    } else if (w == 1) {
      lds_st<float>(L, L_WX + 4 * lane, bf_lo(sraw[0]));
    }
#pragma unroll
    for (int t = 0; t < 2; ++t) {
      const float d = bf_lo(craw[2 * t]) * NLOG2E, a = bf_lo(craw[2 * t + 1]) * NLOG2E;
      const unsigned hi = cvt_pk(d, a);
      cw[t][0] = hi;
      cw[t][1] = cvt_pk(d - bf_lo(hi), a - bf_hi(hi));
    }
    wait_vm0();
    __syncthreads();
    if (tid < NBK) st16_dev(rs, WS_ENT + p * 4096 + tid * 16, entry);
    STAMP(5, 1);
  }

  int pa = 0, np = NP;
  for (;;) {
    const u16 *h = karg<const u16>(0), *pos = karg<const u16>(1);
    const char* ws = karg<const char>(5);
    const rsrc_t rs = ws_rsrc(ws);
    int tid = tid_;
    asm volatile("" : "+v"(tid));
    const int lane = tid & 63, r = lane & 31, hf = lane >> 5;
    u32x4 ent = {0, 0, 0, 0};
    if (w < 4) {
      const u32x2 tg = lds_ld<u32x2>(L, L_TAG);
      for (unsigned spins = 0;; ++spins) {
        ent = ld16_dev(rs, WS_ENT + (tid < NP ? tid : 0) * 4096 + b * 16);
        if (__all(tid >= NP || ((ent[1] >> 4) == tg[0] && (ent[3] >> 4) == tg[1]))) break;
        if (spins > SPIN_LIMIT) { lds_st<unsigned>(L, L_TAG + 8, 1u); break; }
        __builtin_amdgcn_s_sleep(POLL_SLEEP);
      }
    }
    SB();
    STAMP(6, 1);

    unsigned cA = 0, cB = 0, cC = 0, cD = 0, iA = 0, iB = 0, iC = 0, iD = 0;
    const unsigned s0 = ent[0] & 0xfffu, s1 = (ent[0] >> 12) & 0xfffu, s2 = (ent[0] >> 24) | ((ent[1] & 15u) << 8);
    const unsigned s3 = ent[2] & 0xfffu, s4 = (ent[2] >> 12) & 0xfffu, s5 = (ent[2] >> 24) | ((ent[3] & 15u) << 8);
    if (w < 4) {
      if ((unsigned)(tid - pa) < (unsigned)np && tid < NP) {
        cA = (s1 - s0) | ((s2 - s1) << 16);
        cB = (s3 - s2) | ((s4 - s3) << 16);
        cC = s5 - s4;
        cD = s5 - s0;
      }
      lds_st<unsigned>(L, L_SEG + 4 * tid, s0 | (cD << 16));
      iA = wave_incl_scan(cA); iB = wave_incl_scan(cB); iC = wave_incl_scan(cC); iD = wave_incl_scan(cD);
      if (lane == 63) {
        lds_st<unsigned>(L, L_WT + 4 * w, iA); lds_st<unsigned>(L, L_WT + 16 + 4 * w, iB);
        lds_st<unsigned>(L, L_WT + 32 + 4 * w, iC); lds_st<unsigned>(L, L_WT + 48 + 4 * w, iD);
      }
    }
    __syncthreads();
    STAMP(7, 1);
    if (lds_ld<unsigned>(L, L_TAG + 8) != 0) return;
    if (blockIdx.x == 0 && tid == 448 && pa == 0 && np == NP) {
      const u32x2 e = lds_ld<u32x2>(L, L_TAG + 16);
      __builtin_amdgcn_raw_buffer_store_b64(e, rs, WS_CTL, 0, 16);
    }
    const unsigned j2 = 2 * (tid & 7);
    u32x3 ra[4], rb[4];
    unsigned cnt[4], e3[4], rel[4];
#pragma unroll
    for (int k = 0; k < 4; ++k) e3[k] = lds_ld<unsigned>(L, L_SEG + 4 * (64 * k + (tid >> 3)));
#pragma unroll
    for (int k = 0; k < 4; ++k) {
      const unsigned o = WS_RECS + (64 * k + (tid >> 3)) * REC_STRIDE + ((e3[k] & 0xfffeu) + j2) * 6;
      cnt[k] = e3[k] >> 16;
      rel[k] = j2 - (e3[k] & 1);
      asm volatile("" : "=v"(ra[k]));
      if ((int)rel[k] < (int)cnt[k]) ra[k] = ld12_rec(rs, o);
    }
#pragma unroll
    for (int k = 0; k < 4; ++k) {
      const unsigned o = WS_RECS + (64 * k + (tid >> 3)) * REC_STRIDE + ((e3[k] & 0xfffeu) + j2 + 16) * 6;
      asm volatile("" : "=v"(rb[k]));
      if (rel[k] + 16 < cnt[k]) rb[k] = ld12_rec(rs, o);
    }
    SB();
    const u32x4 wA = lds_ld<u32x4>(L, L_WT), wB = lds_ld<u32x4>(L, L_WT + 16), wC = lds_ld<u32x4>(L, L_WT + 32), wD = lds_ld<u32x4>(L, L_WT + 48);
    const unsigned tA = wA[0] + wA[1] + wA[2] + wA[3], tB = wB[0] + wB[1] + wB[2] + wB[3];
    const unsigned tC = wC[0] + wC[1] + wC[2] + wC[3], tD = wD[0] + wD[1] + wD[2] + wD[3];
    unsigned tot[5] = {tA & 0xffffu, tA >> 16, tB & 0xffffu, tB >> 16, tC};
    unsigned base[6];
    base[0] = 0;
#pragma unroll
    for (int g = 0; g < 5; ++g) base[g + 1] = base[g] + ((tot[g] + 31) & ~31u);
    const bool fits = __builtin_amdgcn_readfirstlane((int)(tD <= (unsigned)CAP && base[5] <= (unsigned)CAP)) != 0;
    if (!fits) {
      np = (np + 1) >> 1;
      __syncthreads();
      continue;
    }
    if (w < 4) {
      const unsigned pA = iA - cA + (w > 0 ? wA[0] : 0) + (w > 1 ? wA[1] : 0) + (w > 2 ? wA[2] : 0);
      const unsigned pB = iB - cB + (w > 0 ? wB[0] : 0) + (w > 1 ? wB[1] : 0) + (w > 2 ? wB[2] : 0);
      const unsigned pC = iC - cC + (w > 0 ? wC[0] : 0) + (w > 1 ? wC[1] : 0) + (w > 2 ? wC[2] : 0);
      const unsigned v0 = L_RAW + 8 * (base[0] + (pA & 0xffffu));
      const unsigned v1 = L_RAW + 8 * (base[1] + (pA >> 16) - (s1 - s0));
      const unsigned v2 = L_RAW + 8 * (base[2] + (pB & 0xffffu) - (s2 - s0));
      const unsigned v3 = L_RAW + 8 * (base[3] + (pB >> 16) - (s3 - s0));
      const unsigned v4 = L_RAW + 8 * (base[4] + pC - (s4 - s0));
      lds_st<u32x4>(L, L_TBL + 16 * tid, u32x4{(v0 & 0xffffu) | (v1 << 16), (v2 & 0xffffu) | (v3 << 16), v4 & 0xffffu, 0});
    } else {
      const int t = tid - 256;
      const u32x2 pad = {(unsigned)(L_PD + 16 * BN) << 16, 0};
      if (t < 160) {
        const int g = t >> 5, k = t & 31;
        const unsigned bg = g == 0 ? base[0] : g == 1 ? base[1] : g == 2 ? base[2] : g == 3 ? base[3] : base[4];
        const unsigned bn = g == 0 ? base[1] : g == 1 ? base[2] : g == 2 ? base[3] : g == 3 ? base[4] : base[5];
        const unsigned tg = g == 0 ? tot[0] : g == 1 ? tot[1] : g == 2 ? tot[2] : g == 3 ? tot[3] : tot[4];
        const unsigned slot = bg + tg + k;
        if (slot < bn) lds_st<u32x2>(L, L_RAW + 8 * slot, pad);
      }
      if (t < PADS) lds_st<u32x2>(L, L_RAW + 8 * (base[5] + t), pad);
    }
    __syncthreads();
    STAMP(8, 1);

    {
      auto second = [](unsigned w0, unsigned eahi) { return (((w0 >> 20) & 7) * 320) | eahi; };
      unsigned a0[4], a1[4];
#pragma unroll
      for (int k = 0; k < 4; ++k) {
        const unsigned tb = L_TBL + 16 * (64 * k + (tid >> 3));
        a0[k] = lds_ld<u16>(L, tb + 2 * ((ra[k][0] >> 23) & 7)) + 8 * rel[k];
        a1[k] = lds_ld<u16>(L, tb + 2 * ((ra[k][1] >> 23) & 7)) + 8 * rel[k] + 8;
      }
#pragma unroll
      for (int k = 0; k < 4; ++k) {
        if (rel[k] < cnt[k]) lds_st<u32x2>(L, a0[k], u32x2{ra[k][0], second(ra[k][0], ra[k][2] << 16)});
        if (rel[k] + 1 < cnt[k]) lds_st<u32x2>(L, a1[k], u32x2{ra[k][1], second(ra[k][1], ra[k][2] & 0xffff0000u)});
      }
#pragma unroll
      for (int k = 0; k < 4; ++k) {
        if (rel[k] + 16 < cnt[k]) {
          const unsigned tb = L_TBL + 16 * (64 * k + (tid >> 3));
          const unsigned b0 = lds_ld<u16>(L, tb + 2 * ((rb[k][0] >> 23) & 7)) + 8 * (rel[k] + 16);
          const unsigned b1 = lds_ld<u16>(L, tb + 2 * ((rb[k][1] >> 23) & 7)) + 8 * (rel[k] + 17);
          lds_st<u32x2>(L, b0, u32x2{rb[k][0], second(rb[k][0], rb[k][2] << 16)});
          if (rel[k] + 17 < cnt[k]) lds_st<u32x2>(L, b1, u32x2{rb[k][1], second(rb[k][1], rb[k][2] & 0xffff0000u)});
        }
      }
#pragma unroll
      for (int k = 0; k < 4; ++k) {
        const unsigned j = tid & 7;
        for (unsigned jj = j + 32 - (e3[k] & 1); jj < cnt[k]; jj += 8) {
          const int p = 64 * k + (tid >> 3);
          const unsigned at = (e3[k] & 0xffffu) + jj, pb = WS_RECS + p * REC_STRIDE + 12 * (at >> 1);
          const unsigned w0 = ld4_rec(rs, pb + 4 * (at & 1)), eav = ld2_rec(rs, pb + 8 + 2 * (at & 1));
          const unsigned a = lds_ld<u16>(L, L_TBL + 16 * p + 2 * ((w0 >> 23) & 7));
          lds_st<u32x2>(L, a + 8 * jj, u32x2{w0, second(w0, eav << 16)});
        }
      }
    }
    __syncthreads();
    STAMP(9, 1);

    const int T = __builtin_amdgcn_readfirstlane((int)(base[5] >> 5));
#define SPLIT_OLD 45
    const int q4 = T >> 2, r4 = T & 3, ws4 = w & 3;
    const int sa4 = ws4 * q4 + (ws4 < r4 ? ws4 : r4), sn4 = q4 + (ws4 < r4 ? 1 : 0);
    const int nold = (sn4 * SPLIT_OLD + 32) >> 6;
    const int ta = w < 4 ? sa4 : sa4 + nold;
    const int tb = w < 4 ? sa4 + nold : sa4 + sn4;
    const int tg1 = __builtin_amdgcn_readfirstlane((int)(base[1] >> 5)), tg2 = __builtin_amdgcn_readfirstlane((int)(base[2] >> 5));
    const int tg3 = __builtin_amdgcn_readfirstlane((int)(base[3] >> 5)), tg4 = __builtin_amdgcn_readfirstlane((int)(base[4] >> 5));

    if (ta < tb) {
      const char *qtab = uni_ptr((const char*)h), *pos6 = uni_ptr((const char*)pos);
      const unsigned ringb = L_RING + w * 4096;
      unsigned aq[4];
#pragma unroll
      for (int s = 0; s < 4; ++s) aq[s] = ringb + r * 128 + (((2 * s + hf) ^ ((r >> 1) & 7)) << 4);
      const unsigned dc0 = ((lane & 7) ^ ((lane >> 4) & 7)) << 4, dc1 = ((lane & 7) ^ ((4 + (lane >> 4)) & 7)) << 4;
      const unsigned Sb = L_S + w * S_WAVE;
      const unsigned pi_e = (r & 0x13) | ((r & 4) << 1) | ((r & 8) >> 1);
      const unsigned sa_c = Sb + 2 * pi_e;
      const unsigned s_rd = Sb + r * 80 + 16 * hf;
      const int g16 = lane >> 4, tq = (lane & 15) >> 2, tp = lane & 3, tu = g16 & 1, th = g16 >> 1;
      const unsigned s_tr = Sb + (4 * (4 * th + tq) + 3) * 80 + 2 * (16 * tu + 8 * (tp & 1) + 4 * (tp >> 1));
      const unsigned acc_l = L_ACC + hf * ACC_NODE + 4 * r;
      bf16x8 wq[2][4];
      auto group_of = [&](int t) { return __builtin_amdgcn_readfirstlane((t >= tg1) + (t >= tg2) + (t >= tg3) + (t >= tg4)); };
      auto group_end = [&](int g) { return __builtin_amdgcn_readfirstlane(g == 0 ? tg1 : g == 1 ? tg2 : g == 2 ? tg3 : g == 3 ? tg4 : T); };
      int gend = group_end(group_of(ta));
      int tflush = -1;

      bf16x8 bxA0, bxA1, bxB0, bxB1;
      auto load_bx = [&](int gg) {
        const u32x2 p0 = lds_ld<u32x2>(L, L_PFR + gg * 1024 + lane * 8), p1 = lds_ld<u32x2>(L, L_PFR + gg * 1024 + 512 + lane * 8);
        bxA0 = as_frag(u32x4{p0[0], p0[1], hf ? 0u : cw[0][0], hf ? 0u : cw[0][1]});
        bxB0 = as_frag(u32x4{p0[0], p0[1], hf ? cw[0][0] : 0u, hf ? cw[0][1] : 0u});
        bxA1 = as_frag(u32x4{p1[0], p1[1], hf ? 0u : cw[1][0], hf ? 0u : cw[1][1]});
        bxB1 = as_frag(u32x4{p1[0], p1[1], hf ? cw[1][0] : 0u, hf ? cw[1][1] : 0u});
        asm volatile("" : "+v"(bxA0), "+v"(bxB0), "+v"(bxA1), "+v"(bxB1));
      };
      auto flush = [&](int gg) {
        const unsigned a = LB + acc_l + gg * (8 * ACC_NODE);
        if (lds_lock(LB + L_LOCK + 4 * gg) != 0) acc_gather(a, a + 2 * ACC_NODE, a + 4 * ACC_NODE, a + 6 * ACC_NODE);
        acc_store(a, a + 2 * ACC_NODE, a + 4 * ACC_NODE, a + 6 * ACC_NODE);
        lds_st<unsigned>(L, L_LOCK + 4 * gg, 2u);
        asm volatile("" ::: "memory");
      };
      auto flush_last = [&](int gg) {
        const unsigned a = LB + acc_l + gg * (8 * ACC_NODE);
        if (lds_lock(LB + L_LOCK + 4 * gg) != 0) acc_gather_last(a, a + 2 * ACC_NODE, a + 4 * ACC_NODE, a + 6 * ACC_NODE);
        acc_store_last(a, a + 2 * ACC_NODE, a + 4 * ACC_NODE, a + 6 * ACC_NODE);
        lds_st<unsigned>(L, L_LOCK + 4 * gg, 2u);
        asm volatile("" ::: "memory");
      };
      struct Idx { unsigned i0, i1, i2, i3; };
      unsigned a_idx = L_RAW + ta * 256 + (lane >> 3) * 8, a_raw = L_RAW + ta * 256 + lane * 8;
      auto idx_load = [&](int dt) {
        const unsigned a = a_idx + dt * 256;
        return Idx{lds_ld<u16>(L, a), lds_ld<u16>(L, a + 64), lds_ld<u16>(L, a + 128), lds_ld<u16>(L, a + 192)};
      };
      auto issue_rows = [&](const Idx& x) {
        dma_tile(qtab, x.i0 * 128 + dc0, x.i1 * 128 + dc1, x.i2 * 128 + dc0, x.i3 * 128 + dc1);
      };
      auto raw_load = [&](int dt) {
        const unsigned a = a_raw + dt * 256;
        return Raw{lds_ld<u16>(L, a), lds_ld<u16>(L, a + 2), lds_ld<unsigned>(L, a + 4), lds_ld<u16>(L, a + 4)};
      };
      auto pos_load = [&](const Raw& x) {
        u32x2 ps;
        asm volatile("global_load_dwordx2 %0, %1, %2" : "=v"(ps) : "v"(x.src * 6u), "s"(pos6) : "memory");
        return ps;
      };
      auto wait_ps = [&](u32x2& ps) { asm volatile("s_waitcnt vmcnt(0)" : "+v"(ps) :: "memory"); };
      auto pd_load = [&](const Raw& x) { return lds_ld<f32x4>(L, x.pda); };
      auto prep = [&](const Raw& x, const u32x2& ps, const f32x4& pd) {
        const float dx = pd[0] - bf_lo(ps[0]), dy = pd[1] - bf_hi(ps[0]), dz = pd[2] - bf_lo(ps[1]);
        unsigned wa = cvt_pk(dx, dy), wb = cvt_pk(dz, pd[3]);
        const float dzr = bf_lo(wb);
        const bf16x2v wav = __builtin_bit_cast(bf16x2v, wa);
        const float d2 = __builtin_amdgcn_fdot2_f32_bf16(wav, wav, dzr * dzr, false);
        Prep o;
        o.de = cvt_pk(d2, as_f(x.w1));
        asm volatile("" : "+v"(o.de), "+v"(wa), "+v"(wb));
        const unsigned P = x.n320 + sa_c, Q = P + 160;
        const auto sa = __builtin_amdgcn_permlane32_swap(P, Q, false, false);
        const auto sd = __builtin_amdgcn_permlane32_swap(wa, wb, false, false);
        o.saA = sa[0]; o.saB = sa[1]; o.sdA = sd[0]; o.sdB = sd[1];
        return o;
      };

      struct Ops { bf16x8 q0, q1, q2, q3, as0, as1, ax; };
      auto read_ops = [&](unsigned sa, unsigned sd, unsigned de) {
        Ops o;
        o.q0 = lds_ld<bf16x8>(L, aq[0]); o.q1 = lds_ld<bf16x8>(L, aq[1]);
        o.q2 = lds_ld<bf16x8>(L, aq[2]); o.q3 = lds_ld<bf16x8>(L, aq[3]);
        lds_st<u16>(L, sa, (u16)sd);
        lds_st<u16>(L, sa + 80, (u16)(sd >> 16));
        const u32x2 oh = lds_tr(L, s_tr);
        o.as0 = lds_ld<bf16x8>(L, s_rd);
        o.as1 = lds_ld<bf16x8>(L, s_rd + 32);
        lds_st<u16>(L, sa, (u16)0);
        lds_st<u16>(L, sa + 80, (u16)0);
        unsigned de2;
        asm volatile("v_mov_b32 %0, %1" : "=v"(de2) : "v"(de));
        o.ax = as_frag(u32x4{oh[0], oh[1], de, de2});
        return o;
      };
      auto alpha = [&](int t, const Ops& c, const bf16x8& asl, const bf16x8& bx1, auto&& mid) {
        alpha1(asl);
        if (__builtin_expect(t == tflush, 0)) flush(group_of(t - 1));
        alpha2(c.ax, bx1, c.q0, wq[1][0]);
        SB(); mid(); SB();
        alpha3(c.q1, wq[1][1], c.q2, wq[1][2], c.q3, wq[1][3], c.as0);
      };
      auto beta = [&](const Ops& c, const Ops& n, const bf16x8& bx0n, auto&& mid) {
        beta1(c.as1);
        beta2(n.ax, bx0n, n.q0, wq[0][0]);
        SB(); mid(); SB();
        beta3(n.q1, wq[0][1], n.q2, wq[0][2], n.q3, wq[0][3], c.as0);
      };
      auto next_group = [&](int t) {
        if (__builtin_expect(t == gend, 0)) { const int g = group_of(t); gend = group_end(g); tflush = t; load_bx(g); }
      };

      set_m0(LB + ringb);
      issue_rows(idx_load(0));
      Raw rw = raw_load(0);
      u32x2 ps = pos_load(rw);
      f32x4 pd = pd_load(rw);
      SB();
#pragma unroll
      for (int t = 0; t < 2; ++t)
#pragma unroll
        for (int s = 0; s < 4; ++s) {
          const unsigned o = L_W + 8192 + (16 * s + 8 * th + tq) * 128 + (32 * t + 16 * tu + 4 * tp) * 2;
          const u32x2 b0 = lds_tr(L, o), b1 = lds_tr(L, o + 512);
          wq[t][s] = as_frag(u32x4{b0[0], b0[1], b1[0], b1[1]});
        }
      load_bx(group_of(ta));
      tiles_init();
      SB();
      wait_ps(ps);
      Prep pr = prep(rw, ps, pd);
      rw = raw_load(2);
      Ops A = read_ops(pr.saA, pr.sdA, pr.de), B;
#pragma unroll
      for (int j = 0; j < 8; ++j) B.as1[j] = 0;
      asm volatile("" : "+v"(B.as1));
      const int glast = group_of(tb - 1);
      const Idx ix0 = idx_load(1);
      chain_y0(A.ax, bxA0, A.q0, wq[0][0], A.q1, wq[0][1], A.q2, wq[0][2], A.q3, wq[0][3]);
      asm volatile("s_waitcnt lgkmcnt(0)" ::: "memory");
      issue_rows(ix0);
      STAMP(13, 1);
      CSTAMP(10, 1);
      for (int t = ta;; t += 2) {
        alpha(t, A, B.as1, bxA1, [&]() { ps = pos_load(rw); });
        wait_ps(ps);
        next_group(t + 1);
        const Idx ixa = idx_load(2);
        B = read_ops(pr.saB, pr.sdB, pr.de);
        pd = pd_load(rw);
        SB();
        beta(A, B, bxB0, [&]() { issue_rows(ixa); });
        if (__builtin_expect(t + 1 >= tb, 0)) { last_block(A.as1); break; }
        alpha(t + 1, B, A.as1, bxB1, [&]() { pr = prep(rw, ps, pd); });
        wait_vm0();
        next_group(t + 2);
        const Idx ixb = idx_load(3);
        A = read_ops(pr.saA, pr.sdA, pr.de);
        rw = raw_load(4);
        SB();
        beta(B, A, bxA0, [&]() { issue_rows(ixb); });
        if (__builtin_expect(t + 2 >= tb, 0)) { last_block(B.as1); break; }
        a_idx += 512;
        a_raw += 512;
      }
      flush_last(glast);
      wait_vm0();
      const char* wh = karg<const char>(4);
      dma_piece(wh + w * 2048, lane * 16, LB + ringb);
      dma_piece(wh + w * 2048 + 1024, lane * 16, LB + ringb + 1024);
      STAMP(14, 1);
      CSTAMP(11, 1);
    } else {
      const char* wh = karg<const char>(4);
      dma_piece(wh + w * 2048, lane * 16, LB + L_RING + w * 4096);
      dma_piece(wh + w * 2048 + 1024, lane * 16, LB + L_RING + w * 4096 + 1024);
    }
    pa += np;
    if (pa >= NP) break;
    wait_vm0();
    __syncthreads();
  }
  wait_vm0();
  __syncthreads();
  STAMP(15, 1);

  u16* out = karg<u16>(6);
  int tid = tid_;
  asm volatile("" : "+v"(tid));
  const int lane = tid & 63, r = lane & 31, hf = lane >> 5;
  if (w < 4) {
    const int g16 = lane >> 4, tq = (lane & 15) >> 2, tp = lane & 3, tu = g16 & 1, th = g16 >> 1;
    const int u = w >> 1, t = w & 1;
    const int nl = 32 * u + r, nlc = nl < BN ? nl : BN - 1;
    const unsigned hrow = L_H + nlc * 128;
    f32x16 d;
#pragma unroll
    for (int i = 0; i < 16; ++i) d[i] = 0.f;
    const unsigned w_tr = L_RING + (8 * th + tq) * 128 + (32 * t + 16 * tu + 4 * tp) * 2;
#pragma unroll
    for (int s = 0; s < 8; ++s) {
      const u32x2 a0 = lds_tr(L, w_tr + s * 4096), a1 = lds_tr(L, w_tr + s * 4096 + 512);
      const bf16x8 a = as_frag(u32x4{a0[0], a0[1], a1[0], a1[1]});
      bf16x8 bb;
      if (s < 4) {
        bb = lds_ld<bf16x8>(L, hrow + 32 * s + 16 * hf);
      } else {
        const unsigned o = L_ACC + nlc * ACC_NODE + 3 * 256 + (16 * (s - 4) + 8 * hf) * 4;
        const f32x4 m0 = lds_ld<f32x4>(L, o) * NLN2, m1 = lds_ld<f32x4>(L, o + 16) * NLN2;
        bb = as_frag(u32x4{cvt_pk(m0[0], m0[1]), cvt_pk(m0[2], m0[3]), cvt_pk(m1[0], m1[1]), cvt_pk(m1[2], m1[3])});
      }
      d = mfma(a, bb, d);
    }
    if (nl < BN) {
#pragma unroll
      for (int q = 0; q < 4; ++q) {
        const int f = 32 * t + 8 * q + 4 * hf;
        const u32x2 hv = lds_ld<u32x2>(L, hrow + 2 * f);
        float o4[4];
#pragma unroll
        for (int k = 0; k < 4; ++k) {
          const float act = bf_round(silu_f(bf_round(d[4 * q + k])));
          const float hres = (k & 1) ? bf_hi(hv[k >> 1]) : bf_lo(hv[k >> 1]);
          o4[k] = hres + act;
        }
        *(u32x2*)(out + (size_t)(n0 + nl) * 64 + f) = u32x2{cvt_pk(o4[0], o4[1]), cvt_pk(o4[2], o4[3])};
      }
    }
  } else {
    const int idx = tid - 256, nc = idx >> 1, half = idx & 1;
    const int ncc = nc < 3 * BN ? nc : 3 * BN - 1;
    const int n = ncc / 3, c = ncc - 3 * n;
    const unsigned o = L_ACC + n * ACC_NODE + c * 256 + half * 128;
    float s = 0.f;
#pragma unroll
    for (int k = 0; k < 8; ++k) {
      const f32x4 v = lds_ld<f32x4>(L, o + 16 * k), x = lds_ld<f32x4>(L, L_WX + half * 128 + 16 * k);
      s += v[0] * x[0] + v[1] * x[1] + v[2] * x[2] + v[3] * x[3];
    }
    s += __builtin_bit_cast(float, __builtin_amdgcn_update_dpp(0, __builtin_bit_cast(int, s), 0xb1, 0xf, 0xf, true));
    if (nc < 3 * BN && half == 0) {
      const float p0 = lds_ld<float>(L, L_PD + 16 * n + 4 * c);
      out[(size_t)NN * 64 + (size_t)n0 * 3 + nc] = (u16)cvt_pk(p0 + bf_round(s * NLN2), 0.f);
    }
  }
}

}

extern "C" void kernel_launch(void* const* d_in, const int* in_sizes, int n_in, void* d_out, int out_size, void* d_ws, size_t ws_size,
                              hipStream_t stream) {
  const u16* h = (const u16*)d_in[0];
  const u16* pos = (const u16*)d_in[1];
  const u16* ea = (const u16*)d_in[2];
  const int* src = (const int*)d_in[3];
  const int* dst = (const int*)d_in[4];
  const u16* we = (const u16*)d_in[5];
  const u16* wx = (const u16*)d_in[6];
  const u16* wh = (const u16*)d_in[7];
  egnn_kernel<<<NBK, 512, 0, stream>>>(h, pos, we, wx, wh, (char*)d_ws, (u16*)d_out, src, dst, ea);
}
```

```cpp
#include <hip/hip_runtime.h>
#include <hip/hip_bf16.h>
#include <stdint.h>

#define DI __device__ __forceinline__
#define AS3 __attribute__((address_space(3)))
#define SB() __builtin_amdgcn_sched_barrier(0)

typedef short bf16x8 __attribute__((ext_vector_type(8)));
typedef short s16x4 __attribute__((ext_vector_type(4)));
typedef float f32x16 __attribute__((ext_vector_type(16)));
typedef float f32x4 __attribute__((ext_vector_type(4)));
typedef float f32x2 __attribute__((ext_vector_type(2)));
typedef unsigned u32x4 __attribute__((ext_vector_type(4)));
typedef unsigned u32x3 __attribute__((ext_vector_type(3)));
typedef unsigned u32x2 __attribute__((ext_vector_type(2)));
typedef int i32x4 __attribute__((ext_vector_type(4)));
typedef __bf16 bf16x2v __attribute__((ext_vector_type(2)));
typedef unsigned short u16;

namespace {

constexpr int NN = 10000;
constexpr int NE = 640000;
constexpr int BN = 40;
constexpr int NBK = NN / BN;
constexpr int NP = NBK;
constexpr int CH = NE / NP;
static_assert(CH == 5 * 512 && CH < 4096, "five edges per thread, 12-bit run starts");
constexpr float NLOG2E = -1.4426950408889634f;
constexpr float NLN2 = -0.6931471805599453f;
constexpr unsigned SPIN_LIMIT = 1u << 20;
constexpr unsigned NO_DATA = 0xffffff00u;
#define POLL_SLEEP 4

constexpr unsigned WS_CTL = 0;
constexpr unsigned WS_ENT = 256;
constexpr unsigned WS_RECS = WS_ENT + NBK * 4096;
constexpr unsigned REC_STRIDE = CH * 6;
constexpr unsigned WS_END = WS_RECS + NP * REC_STRIDE;
static_assert(WS_RECS % 128 == 0 && REC_STRIDE % 128 == 0, "no cache line holds records of two producers");

constexpr int CAP = 2880;
constexpr int PADS = 160;
constexpr int L_S = 0;
constexpr int S_WAVE = 2560;
constexpr int L_PD = 20480;
constexpr int L_WT = 21248;
constexpr int L_TOT = L_WT + 16;
constexpr int L_LOCK = 21312;
constexpr int L_TAG = 21344;
constexpr int L_WX = 21504;
constexpr int L_TBL = 21760;
constexpr int L_RAW = L_TBL + 4096;
constexpr int L_PFR = L_RAW + (CAP + PADS) * 8;
constexpr int L_RING = L_PFR + 5120;
constexpr int L_ACC = L_RING + 8 * 4096;
constexpr int ACC_NODE = 1040;
constexpr int L_W = L_ACC + BN * ACC_NODE;
constexpr int L_H = L_W + 16384;
constexpr int L_SEG = L_H + BN * 128;
constexpr int L_END = L_SEG + 1024;
static_assert(L_PD % 1024 == 0, "group id is read from bits 7..9 of the pos address");
static_assert(L_RAW + (CAP + PADS) * 8 < 65536 && L_RAW >= 8 * CH && CAP + PADS >= CH, "run table holds 16-bit LDS addresses");
static_assert(L_PFR % 16 == 0 && L_RING % 16 == 0 && L_ACC % 16 == 0 && L_W % 16 == 0 && L_END <= 163840, "LDS map");

DI unsigned cvt_pk(float a, float b) { f32x2 v = {a, b}; return __builtin_bit_cast(unsigned, __builtin_convertvector(v, bf16x2v)); }
DI float as_f(unsigned u) { return __builtin_bit_cast(float, u); }
DI float bf_lo(unsigned u) { return __builtin_bit_cast(float, u << 16); }
DI float bf_hi(unsigned u) { return __builtin_bit_cast(float, u & 0xffff0000u); }
DI float bf_round(float x) { return bf_lo(cvt_pk(x, x)); }
DI float bf_ld(const u16* p) { return __builtin_bit_cast(float, (unsigned)*p << 16); }
DI f32x16 mfma(bf16x8 a, bf16x8 b, f32x16 c) { return __builtin_amdgcn_mfma_f32_32x32x16_bf16(a, b, c, 0, 0, 0); }
DI bf16x8 as_frag(u32x4 v) { return __builtin_bit_cast(bf16x8, v); }

DI unsigned wave_incl_scan(unsigned x) {
  x += __builtin_amdgcn_update_dpp(0, (int)x, 0x111, 0xf, 0xf, true);
  x += __builtin_amdgcn_update_dpp(0, (int)x, 0x112, 0xf, 0xf, true);
  x += __builtin_amdgcn_update_dpp(0, (int)x, 0x114, 0xf, 0xf, true);
  x += __builtin_amdgcn_update_dpp(0, (int)x, 0x118, 0xf, 0xf, true);
  x += __builtin_amdgcn_update_dpp(0, (int)x, 0x142, 0xa, 0xf, false);
  x += __builtin_amdgcn_update_dpp(0, (int)x, 0x143, 0xc, 0xf, false);
  return x;
}

template <typename T> DI T lds_ld(AS3 char* L, unsigned off) { return *(AS3 T*)(L + off); }
template <typename T> DI void lds_st(AS3 char* L, unsigned off, T v) { *(AS3 T*)(L + off) = v; }
DI u32x2 lds_tr(AS3 char* L, unsigned off) {
  return __builtin_bit_cast(u32x2, __builtin_amdgcn_ds_read_tr16_b64_v4i16((AS3 s16x4*)(L + off)));
}

DI const char* uni_ptr(const char* p) {
  const unsigned long long v = (unsigned long long)p;
  const unsigned lo = __builtin_amdgcn_readfirstlane((int)(unsigned)v), hi = __builtin_amdgcn_readfirstlane((int)(unsigned)(v >> 32));
  return (const char*)(__attribute__((address_space(1))) const char*)(((unsigned long long)hi << 32) | lo);
}
DI void dma_piece(const char* base_, unsigned voff, unsigned ldsaddr_) {
  const char* base = uni_ptr(base_);
  const unsigned ldsaddr = __builtin_amdgcn_readfirstlane((int)ldsaddr_);
  unsigned keep;
  asm volatile("s_mov_b32 %0, m0\n\ts_mov_b32 m0, %3\n\ts_nop 4\n\tglobal_load_lds_dwordx4 %1, %2\n\ts_mov_b32 m0, %0"
               : "=&s"(keep) : "v"(voff), "s"(base), "s"(ldsaddr) : "memory");
}
DI void set_m0(unsigned ldsaddr) { asm volatile("s_mov_b32 m0, %0\n\ts_nop 0" :: "s"(ldsaddr) : "memory"); }
DI void dma_tile(const char* base, unsigned v0, unsigned v1, unsigned v2, unsigned v3) {
  asm volatile("global_load_lds_dwordx4 %0, %4\n\t"
               "global_load_lds_dwordx4 %1, %5 offset:1024\n\t"
               "global_load_lds_dwordx4 %2, %6 offset:2048\n\t"
               "global_load_lds_dwordx4 %3, %7 offset:3072"
               :: "v"(v0), "v"(v1), "v"(v2), "v"(v3), "s"(base), "s"(base - 1024), "s"(base - 2048), "s"(base - 3072)
               : "memory");
}
#define STAMP(k, kern)
#define CSTAMP(k, kern)
typedef __amdgpu_buffer_rsrc_t rsrc_t;
DI rsrc_t ws_rsrc(const char* ws) { return __builtin_amdgcn_make_buffer_rsrc((void*)ws, 0, WS_END, 0x00020000); }
#define LD_AUX 16
#define ST_AUX 16
DI u32x4 ld16_dev(rsrc_t r, unsigned off) { return __builtin_amdgcn_raw_buffer_load_b128(r, off, 0, 16); }
DI u32x3 ld12_rec(rsrc_t r, unsigned off) { return __builtin_amdgcn_raw_buffer_load_b96(r, off, 0, LD_AUX); }
DI unsigned ld4_rec(rsrc_t r, unsigned off) { return __builtin_amdgcn_raw_buffer_load_b32(r, off, 0, LD_AUX); }
DI unsigned ld2_rec(rsrc_t r, unsigned off) { return __builtin_amdgcn_raw_buffer_load_b16(r, off, 0, LD_AUX); }
DI u32x2 ld8_dev(rsrc_t r, unsigned off) { return __builtin_amdgcn_raw_buffer_load_b64(r, off, 0, 16); }
DI void st16_dev(rsrc_t r, unsigned off, u32x4 v) { __builtin_amdgcn_raw_buffer_store_b128(v, r, off, 0, 16); }
DI void st16_rec(rsrc_t r, unsigned off, u32x4 v) { __builtin_amdgcn_raw_buffer_store_b128(v, r, off, 0, ST_AUX); }
DI void wait_vm0() { asm volatile("s_waitcnt vmcnt(0)" ::: "memory"); }

struct Prep {
  unsigned sdA, sdB;
  unsigned saA, saB;
  unsigned de;
};
struct Raw { unsigned src, pda, w1, n320; };

#pragma clang diagnostic ignored "-Winline-asm"
#define T4(op, d, s0, s1, s2, s3, d1, d2, d3) op " v" #d ", v" #s0 "\n\t" op " v" #d1 ", v" #s1 "\n\t" op " v" #d2 ", v" #s2 "\n\t" op " v" #d3 ", v" #s3 "\n\t"
#define EXP_LO(a, b, c, d) T4("v_exp_f32", 184, a, b, c, d, 185, 186, 187)
#define EXP_HI(a, b, c, d) T4("v_exp_f32", 188, a, b, c, d, 189, 190, 191)
#define RCP_LO T4("v_rcp_f32", 184, 184, 185, 186, 187, 185, 186, 187)
#define RCP_HI T4("v_rcp_f32", 188, 188, 189, 190, 191, 189, 190, 191)
#define PKA(a, b) "v_pk_add_f32 v[" #a ":" #b "], v[" #a ":" #b "], 1.0 op_sel_hi:[1,0]\n\t"
#define ADD1 PKA(184, 185) PKA(186, 187) PKA(188, 189) PKA(190, 191)
#define PKM(a, b, c, d) "v_pk_mul_f32 v[" #a ":" #b "], v[" #a ":" #b "], v[" #c ":" #d "]\n\t"
#define MULY(a0, a1, a2, a3, a4, a5, a6, a7) PKM(184, 185, a0, a1) PKM(186, 187, a2, a3) PKM(188, 189, a4, a5) PKM(190, 191, a6, a7)
#define CVT(d, a, b) "v_cvt_pk_bf16_f32 v" #d ", v" #a ", v" #b "\n\t"
#define PACK(d0, d1, d2, d3) CVT(d0, 184, 185) CVT(d1, 186, 187) CVT(d2, 188, 189) CVT(d3, 190, 191)
#define MF(acc, a, b) "v_mfma_f32_32x32x16_bf16 " acc ", " a ", " b ", " acc "\n\t"
#define MF0(acc, a, b) "v_mfma_f32_32x32x16_bf16 " acc ", " a ", " b ", 0\n\t"
#define Y0 "v[192:207]"
#define Y1 "v[208:223]"
#define ACC0 "v[224:239]"
#define ACC1 "v[240:255]"
#define X0 "v[176:179]"
#define X1 "v[180:183]"

DI void tiles_init() {
  asm volatile(
               "v_mov_b64 v[224:225], 0\n\t"
               "v_mov_b64 v[226:227], 0\n\t"
               "v_mov_b64 v[228:229], 0\n\t"
               "v_mov_b64 v[230:231], 0\n\t"
               "v_mov_b64 v[232:233], 0\n\t"
               "v_mov_b64 v[234:235], 0\n\t"
               "v_mov_b64 v[236:237], 0\n\t"
               "v_mov_b64 v[238:239], 0\n\t"
               "v_mov_b64 v[240:241], 0\n\t"
               "v_mov_b64 v[242:243], 0\n\t"
               "v_mov_b64 v[244:245], 0\n\t"
               "v_mov_b64 v[246:247], 0\n\t"
               "v_mov_b64 v[248:249], 0\n\t"
               "v_mov_b64 v[250:251], 0\n\t"
               "v_mov_b64 v[252:253], 0\n\t"
               "v_mov_b64 v[254:255], 0\n\t"
               "v_mov_b64 v[180:181], 0\n\t"
               "v_mov_b64 v[182:183], 0\n\t"
               "s_nop 1" ::: "v174", "v255");
}
DI void alpha1(const bf16x8& asl) {
  asm volatile(EXP_LO(192, 193, 194, 195) MF(ACC1, "%[asl]", X1) EXP_HI(196, 197, 198, 199) :: [asl] "v"(asl));
}
DI void alpha2(const bf16x8& ax, const bf16x8& bx, const bf16x8& q0, const bf16x8& w0) {
  asm volatile(ADD1 MF0(Y1, "%[ax]", "%[bx]") RCP_LO MF(Y1, "%[q0]", "%[w0]") RCP_HI
               MULY(192, 193, 194, 195, 196, 197, 198, 199) PACK(176, 177, 178, 179)
               :: [ax] "v"(ax), [bx] "v"(bx), [q0] "v"(q0), [w0] "v"(w0));
}
DI void alpha3(const bf16x8& q1, const bf16x8& w1, const bf16x8& q2, const bf16x8& w2, const bf16x8& q3, const bf16x8& w3, const bf16x8& as0) {
  asm volatile(MF(Y1, "%[q1]", "%[w1]") EXP_LO(200, 201, 202, 203) MF(Y1, "%[q2]", "%[w2]") EXP_HI(204, 205, 206, 207) ADD1
               MF(Y1, "%[q3]", "%[w3]") RCP_LO MF(ACC0, "%[as]", X0) RCP_HI
               MULY(200, 201, 202, 203, 204, 205, 206, 207) PACK(180, 181, 182, 183)
               :: [q1] "v"(q1), [w1] "v"(w1), [q2] "v"(q2), [w2] "v"(w2), [q3] "v"(q3), [w3] "v"(w3), [as] "v"(as0));
}
DI void beta1(const bf16x8& as1) {
  asm volatile(EXP_LO(208, 209, 210, 211) MF(ACC0, "%[as]", X1) EXP_HI(212, 213, 214, 215) ADD1 :: [as] "v"(as1));
}
DI void beta2(const bf16x8& ax, const bf16x8& bx, const bf16x8& q0, const bf16x8& w0) {
  asm volatile(MF0(Y0, "%[ax]", "%[bx]") RCP_LO MF(Y0, "%[q0]", "%[w0]") RCP_HI
               MULY(208, 209, 210, 211, 212, 213, 214, 215) PACK(176, 177, 178, 179)
               :: [ax] "v"(ax), [bx] "v"(bx), [q0] "v"(q0), [w0] "v"(w0));
}
DI void beta3(const bf16x8& q1, const bf16x8& w1, const bf16x8& q2, const bf16x8& w2, const bf16x8& q3, const bf16x8& w3, const bf16x8& as0) {
  asm volatile(MF(Y0, "%[q1]", "%[w1]") EXP_LO(216, 217, 218, 219) MF(Y0, "%[q2]", "%[w2]") EXP_HI(220, 221, 222, 223) ADD1
               MF(Y0, "%[q3]", "%[w3]") RCP_LO MF(ACC1, "%[as]", X0) RCP_HI
               MULY(216, 217, 218, 219, 220, 221, 222, 223) PACK(180, 181, 182, 183)
               :: [q1] "v"(q1), [w1] "v"(w1), [q2] "v"(q2), [w2] "v"(w2), [q3] "v"(q3), [w3] "v"(w3), [as] "v"(as0));
}
DI void chain_y0(const bf16x8& ax, const bf16x8& bx, const bf16x8& q0, const bf16x8& w0, const bf16x8& q1, const bf16x8& w1,
                 const bf16x8& q2, const bf16x8& w2, const bf16x8& q3, const bf16x8& w3) {
  asm volatile("s_nop 1\n\t" MF0(Y0, "%[ax]", "%[bx]") MF(Y0, "%[q0]", "%[w0]") MF(Y0, "%[q1]", "%[w1]") MF(Y0, "%[q2]", "%[w2]")
               MF(Y0, "%[q3]", "%[w3]") "s_nop 15"
               :: [ax] "v"(ax), [bx] "v"(bx), [q0] "v"(q0), [w0] "v"(w0), [q1] "v"(q1), [w1] "v"(w1), [q2] "v"(q2), [w2] "v"(w2),
                  [q3] "v"(q3), [w3] "v"(w3));
}
DI void last_block(const bf16x8& as1) { asm volatile("s_nop 1\n\t" MF(ACC1, "%[as]", X1) :: [as] "v"(as1)); }
DI void acc_gather(unsigned a0, unsigned a1, unsigned a2, unsigned a3) {
  f32x2 t0, t1, t2, t3, t4, t5, t6, t7;
  asm volatile(
               "ds_read2_b32 %[t0], %[a0] offset0:0 offset1:64\n\t"
               "ds_read2_b32 %[t1], %[a0] offset0:128 offset1:192\n\t"
               "ds_read2_b32 %[t2], %[a0] offset0:32 offset1:96\n\t"
               "ds_read2_b32 %[t3], %[a0] offset0:160 offset1:224\n\t"
               "ds_read2_b32 %[t4], %[a1] offset0:0 offset1:64\n\t"
               "ds_read2_b32 %[t5], %[a1] offset0:128 offset1:192\n\t"
               "ds_read2_b32 %[t6], %[a1] offset0:32 offset1:96\n\t"
               "ds_read2_b32 %[t7], %[a1] offset0:160 offset1:224\n\t"
               "s_waitcnt lgkmcnt(4)\n\t"
               "v_pk_add_f32 v[224:225], v[224:225], %[t0]\n\t"
               "v_pk_add_f32 v[226:227], v[226:227], %[t1]\n\t"
               "v_pk_add_f32 v[240:241], v[240:241], %[t2]\n\t"
               "v_pk_add_f32 v[242:243], v[242:243], %[t3]\n\t"
               "s_waitcnt lgkmcnt(0)\n\t"
               "v_pk_add_f32 v[228:229], v[228:229], %[t4]\n\t"
               "v_pk_add_f32 v[230:231], v[230:231], %[t5]\n\t"
               "v_pk_add_f32 v[244:245], v[244:245], %[t6]\n\t"
               "v_pk_add_f32 v[246:247], v[246:247], %[t7]\n\t"
               "ds_read2_b32 %[t0], %[a2] offset0:0 offset1:64\n\t"
               "ds_read2_b32 %[t1], %[a2] offset0:128 offset1:192\n\t"
               "ds_read2_b32 %[t2], %[a2] offset0:32 offset1:96\n\t"
               "ds_read2_b32 %[t3], %[a2] offset0:160 offset1:224\n\t"
               "ds_read2_b32 %[t4], %[a3] offset0:0 offset1:64\n\t"
               "ds_read2_b32 %[t5], %[a3] offset0:128 offset1:192\n\t"
               "ds_read2_b32 %[t6], %[a3] offset0:32 offset1:96\n\t"
               "ds_read2_b32 %[t7], %[a3] offset0:160 offset1:224\n\t"
               "s_waitcnt lgkmcnt(4)\n\t"
               "v_pk_add_f32 v[232:233], v[232:233], %[t0]\n\t"
               "v_pk_add_f32 v[234:235], v[234:235], %[t1]\n\t"
               "v_pk_add_f32 v[248:249], v[248:249], %[t2]\n\t"
               "v_pk_add_f32 v[250:251], v[250:251], %[t3]\n\t"
               "s_waitcnt lgkmcnt(0)\n\t"
               "v_pk_add_f32 v[236:237], v[236:237], %[t4]\n\t"
               "v_pk_add_f32 v[238:239], v[238:239], %[t5]\n\t"
               "v_pk_add_f32 v[252:253], v[252:253], %[t6]\n\t"
               "v_pk_add_f32 v[254:255], v[254:255], %[t7]\n\t"
               "s_nop 0"
               : [t0] "=&v"(t0), [t1] "=&v"(t1), [t2] "=&v"(t2), [t3] "=&v"(t3), [t4] "=&v"(t4), [t5] "=&v"(t5), [t6] "=&v"(t6), [t7] "=&v"(t7)
               : [a0] "v"(a0), [a1] "v"(a1), [a2] "v"(a2), [a3] "v"(a3) : "memory");
}
DI void acc_store(unsigned a0, unsigned a1, unsigned a2, unsigned a3) {
  asm volatile(
               "ds_write2_b32 %[a0], v224, v225 offset0:0 offset1:64\n\t"
               "ds_write2_b32 %[a0], v226, v227 offset0:128 offset1:192\n\t"
               "ds_write2_b32 %[a0], v240, v241 offset0:32 offset1:96\n\t"
               "ds_write2_b32 %[a0], v242, v243 offset0:160 offset1:224\n\t"
               "ds_write2_b32 %[a1], v228, v229 offset0:0 offset1:64\n\t"
               "ds_write2_b32 %[a1], v230, v231 offset0:128 offset1:192\n\t"
               "ds_write2_b32 %[a1], v244, v245 offset0:32 offset1:96\n\t"
               "ds_write2_b32 %[a1], v246, v247 offset0:160 offset1:224\n\t"
               "ds_write2_b32 %[a2], v232, v233 offset0:0 offset1:64\n\t"
               "ds_write2_b32 %[a2], v234, v235 offset0:128 offset1:192\n\t"
               "ds_write2_b32 %[a2], v248, v249 offset0:32 offset1:96\n\t"
               "ds_write2_b32 %[a2], v250, v251 offset0:160 offset1:224\n\t"
               "ds_write2_b32 %[a3], v236, v237 offset0:0 offset1:64\n\t"
               "ds_write2_b32 %[a3], v238, v239 offset0:128 offset1:192\n\t"
               "ds_write2_b32 %[a3], v252, v253 offset0:32 offset1:96\n\t"
               "ds_write2_b32 %[a3], v254, v255 offset0:160 offset1:224\n\t"
               "s_waitcnt lgkmcnt(0)\n\t"
               "v_mov_b64 v[224:225], 0\n\t"
               "v_mov_b64 v[226:227], 0\n\t"
               "v_mov_b64 v[228:229], 0\n\t"
               "v_mov_b64 v[230:231], 0\n\t"
               "v_mov_b64 v[232:233], 0\n\t"
               "v_mov_b64 v[234:235], 0\n\t"
               "v_mov_b64 v[236:237], 0\n\t"
               "v_mov_b64 v[238:239], 0\n\t"
               "v_mov_b64 v[240:241], 0\n\t"
               "v_mov_b64 v[242:243], 0\n\t"
               "v_mov_b64 v[244:245], 0\n\t"
               "v_mov_b64 v[246:247], 0\n\t"
               "v_mov_b64 v[248:249], 0\n\t"
               "v_mov_b64 v[250:251], 0\n\t"
               "v_mov_b64 v[252:253], 0\n\t"
               "v_mov_b64 v[254:255], 0\n\t"
               "s_nop 7"
               :: [a0] "v"(a0), [a1] "v"(a1), [a2] "v"(a2), [a3] "v"(a3) : "memory");
}
DI void acc_gather_last(unsigned a0, unsigned a1, unsigned a2, unsigned a3) {
  asm volatile(
               "ds_read2_b32 v[176:177], %[a0] offset0:0 offset1:64\n\t"
               "ds_read2_b32 v[178:179], %[a0] offset0:128 offset1:192\n\t"
               "ds_read2_b32 v[180:181], %[a0] offset0:32 offset1:96\n\t"
               "ds_read2_b32 v[182:183], %[a0] offset0:160 offset1:224\n\t"
               "ds_read2_b32 v[184:185], %[a1] offset0:0 offset1:64\n\t"
               "ds_read2_b32 v[186:187], %[a1] offset0:128 offset1:192\n\t"
               "ds_read2_b32 v[188:189], %[a1] offset0:32 offset1:96\n\t"
               "ds_read2_b32 v[190:191], %[a1] offset0:160 offset1:224\n\t"
               "ds_read2_b32 v[192:193], %[a2] offset0:0 offset1:64\n\t"
               "ds_read2_b32 v[194:195], %[a2] offset0:128 offset1:192\n\t"
               "ds_read2_b32 v[196:197], %[a2] offset0:32 offset1:96\n\t"
               "ds_read2_b32 v[198:199], %[a2] offset0:160 offset1:224\n\t"
               "ds_read2_b32 v[200:201], %[a3] offset0:0 offset1:64\n\t"
               "ds_read2_b32 v[202:203], %[a3] offset0:128 offset1:192\n\t"
               "ds_read2_b32 v[204:205], %[a3] offset0:32 offset1:96\n\t"
               "ds_read2_b32 v[206:207], %[a3] offset0:160 offset1:224\n\t"
               "s_waitcnt lgkmcnt(12)\n\t"
               "v_pk_add_f32 v[224:225], v[224:225], v[176:177]\n\t"
               "v_pk_add_f32 v[226:227], v[226:227], v[178:179]\n\t"
               "v_pk_add_f32 v[240:241], v[240:241], v[180:181]\n\t"
               "v_pk_add_f32 v[242:243], v[242:243], v[182:183]\n\t"
               "s_waitcnt lgkmcnt(8)\n\t"
               "v_pk_add_f32 v[228:229], v[228:229], v[184:185]\n\t"
               "v_pk_add_f32 v[230:231], v[230:231], v[186:187]\n\t"
               "v_pk_add_f32 v[244:245], v[244:245], v[188:189]\n\t"
               "v_pk_add_f32 v[246:247], v[246:247], v[190:191]\n\t"
               "s_waitcnt lgkmcnt(4)\n\t"
               "v_pk_add_f32 v[232:233], v[232:233], v[192:193]\n\t"
               "v_pk_add_f32 v[234:235], v[234:235], v[194:195]\n\t"
               "v_pk_add_f32 v[248:249], v[248:249], v[196:197]\n\t"
               "v_pk_add_f32 v[250:251], v[250:251], v[198:199]\n\t"
               "s_waitcnt lgkmcnt(0)\n\t"
               "v_pk_add_f32 v[236:237], v[236:237], v[200:201]\n\t"
               "v_pk_add_f32 v[238:239], v[238:239], v[202:203]\n\t"
               "v_pk_add_f32 v[252:253], v[252:253], v[204:205]\n\t"
               "v_pk_add_f32 v[254:255], v[254:255], v[206:207]\n\t"
               "s_nop 0"
               :: [a0] "v"(a0), [a1] "v"(a1), [a2] "v"(a2), [a3] "v"(a3) : "memory");
}
DI void acc_store_last(unsigned a0, unsigned a1, unsigned a2, unsigned a3) {
  asm volatile(
               "ds_write2_b32 %[a0], v224, v225 offset0:0 offset1:64\n\t"
               "ds_write2_b32 %[a0], v226, v227 offset0:128 offset1:192\n\t"
               "ds_write2_b32 %[a0], v240, v241 offset0:32 offset1:96\n\t"
               "ds_write2_b32 %[a0], v242, v243 offset0:160 offset1:224\n\t"
               "ds_write2_b32 %[a1], v228, v229 offset0:0 offset1:64\n\t"
               "ds_write2_b32 %[a1], v230, v231 offset0:128 offset1:192\n\t"
               "ds_write2_b32 %[a1], v244, v245 offset0:32 offset1:96\n\t"
               "ds_write2_b32 %[a1], v246, v247 offset0:160 offset1:224\n\t"
               "ds_write2_b32 %[a2], v232, v233 offset0:0 offset1:64\n\t"
               "ds_write2_b32 %[a2], v234, v235 offset0:128 offset1:192\n\t"
               "ds_write2_b32 %[a2], v248, v249 offset0:32 offset1:96\n\t"
               "ds_write2_b32 %[a2], v250, v251 offset0:160 offset1:224\n\t"
               "ds_write2_b32 %[a3], v236, v237 offset0:0 offset1:64\n\t"
               "ds_write2_b32 %[a3], v238, v239 offset0:128 offset1:192\n\t"
               "ds_write2_b32 %[a3], v252, v253 offset0:32 offset1:96\n\t"
               "ds_write2_b32 %[a3], v254, v255 offset0:160 offset1:224\n\t"
               "s_nop 0"
               :: [a0] "v"(a0), [a1] "v"(a1), [a2] "v"(a2), [a3] "v"(a3) : "memory");
}
DI unsigned lds_lock(unsigned addr) {
  unsigned t, old;
  unsigned long long sv;
  asm volatile("s_mov_b64 %2, exec\n\ts_mov_b64 exec, 1\n"
               "1:\n\tds_wrxchg_rtn_b32 %0, %3, %4\n\ts_waitcnt lgkmcnt(0)\n\tv_cmp_eq_u32 vcc, 1, %0\n\ts_cbranch_vccz 2f\n\t"
               "s_sleep 1\n\ts_branch 1b\n"
               "2:\n\tv_readfirstlane_b32 %1, %0\n\ts_mov_b64 exec, %2\n\ts_nop 15"
               : "=&v"(t), "=&s"(old), "=&s"(sv) : "v"(addr), "v"(1u) : "vcc", "memory");
  return old;
}

template <typename T> DI T* karg(int k) {
  typedef __attribute__((address_space(4))) const unsigned long long* kp_t;
  kp_t kp = (kp_t)__builtin_amdgcn_kernarg_segment_ptr();
  asm volatile("" : "+s"(kp));
  return (T*)(__attribute__((address_space(1))) T*)kp[k];
}

__global__ __launch_bounds__(512) __attribute__((amdgpu_num_vgpr(87))) void egnn_kernel(
    const u16* __restrict__ h_, const u16* __restrict__ pos_, const u16* __restrict__ we_, const u16* __restrict__ wx_,
    const u16* __restrict__ wh_, char* __restrict__ ws_, u16* __restrict__ out_, const int* __restrict__ src_,
    const int* __restrict__ dst_, const u16* __restrict__ ea_) {
  __shared__ __attribute__((aligned(1024))) char lds[L_END];
  AS3 char* L = (AS3 char*)lds;
  const unsigned LB = (unsigned)(size_t)L;
  const int tid_ = threadIdx.x;
  const int w = __builtin_amdgcn_readfirstlane(tid_ >> 6);
  STAMP(0, 1);
  const int xl = blockIdx.x & 7;
  const int b = xl * 31 + (xl < 2 ? xl : 2) + (blockIdx.x >> 3);
  const int n0 = b * BN;
  unsigned cw[2][2];

  {
    const int tid = tid_, lane = tid & 63, r = lane & 31, hf = lane >> 5, p = blockIdx.x;
    const rsrc_t rs = ws_rsrc(ws_);
    const int e0 = p * CH + 4 * tid, e1 = p * CH + 2048 + tid;
    const i32x4 d4 = *(const i32x4*)(dst_ + e0), s4 = *(const i32x4*)(src_ + e0);
    const u32x2 a2 = *(const u32x2*)(ea_ + e0);
    unsigned dd[5] = {(unsigned)d4[0], (unsigned)d4[1], (unsigned)d4[2], (unsigned)d4[3], (unsigned)dst_[e1]};
    const unsigned s5 = src_[e1], a5 = ea_[e1];
    const u32x2 ctl = ld8_dev(rs, WS_CTL);
    SB();
    dma_piece((const char*)we_ + w * 1024, lane * 16, LB + L_W + w * 1024);
    if (w < 5) dma_piece((const char*)h_ + (size_t)n0 * 128 + w * 1024, lane * 16, LB + L_H + w * 1024);
    const u32x4 w2raw = *(const u32x4*)(we_ + 64 * 64 + tid * 8);
    unsigned sraw[3], craw[4];
#pragma unroll
    for (int t = 0; t < 2; ++t) { craw[2 * t] = we_[128 * 64 + 32 * t + r]; craw[2 * t + 1] = we_[129 * 64 + 32 * t + r]; }
    {
      const u16* pp = w == 1 ? wx_ + lane : pos_ + (lane < BN ? n0 + lane : 0) * 3;
      const int st = w == 1 ? 0 : 1;
      sraw[0] = pp[0]; sraw[1] = pp[st]; sraw[2] = pp[2 * st];
    }
    SB();
    {
      const u32x4 z4 = {0, 0, 0, 0};
      static_assert(8 * S_WAVE == 2 * 8192 + 256 * 16 && BN * ACC_NODE == 5 * 8192 + 40 * 16, "clearing pattern");
      if (tid < 256) lds_st<u32x4>(L, L_TBL + 16 * tid, z4);
      lds_st<u32x4>(L, L_S + tid * 16, z4);
      lds_st<u32x4>(L, L_S + 8192 + tid * 16, z4);
#pragma unroll
      for (int k = 0; k < 5; ++k) lds_st<u32x4>(L, L_ACC + k * 8192 + tid * 16, z4);
      if (tid < 256) lds_st<u32x4>(L, L_S + 16384 + tid * 16, z4);
      if (tid < 40) lds_st<u32x4>(L, L_ACC + 5 * 8192 + tid * 16, z4);
      if (tid < 8) lds_st<unsigned>(L, L_LOCK + 4 * tid, 0u);
      if (tid == 8) lds_st<unsigned>(L, L_WT, 0u);
    }
    __syncthreads();
    STAMP(1, 1);
    unsigned idx[5], rank[5], bk[5];
#pragma unroll
    for (int i = 0; i < 5; ++i) {
      const unsigned G = dd[i] >> 3;
      bk[i] = (G * 52429u) >> 18;
      idx[i] = G + 3 * bk[i];
      const unsigned sh = (idx[i] & 1) << 4;
      const unsigned old = __hip_atomic_fetch_add((AS3 unsigned*)(L + L_TBL + 4 * (idx[i] >> 1)), 1u << sh, __ATOMIC_RELAXED,
                                                  __HIP_MEMORY_SCOPE_WORKGROUP);
      rank[i] = (old >> sh) & 0xffffu;
    }
    __syncthreads();
    STAMP(2, 1);
    const unsigned tlo = ctl[0] + 1, thi = ctl[1] + (tlo == 0);
    const unsigned tag0 = tlo & 0xfffffffu, tag1 = ((tlo >> 28) | (thi << 4)) & 0xfffffffu;
    u32x4 entry = {0, 0, 0, 0};
    if (w < 4) {
      const u32x4 c = lds_ld<u32x4>(L, L_TBL + 16 * tid);
      const unsigned tot = (c[0] & 0xffffu) + (c[0] >> 16) + (c[1] & 0xffffu) + (c[1] >> 16) + (c[2] & 0xffffu);
      const unsigned incl = wave_incl_scan(tot);
      unsigned old;
      unsigned long long sv;
      asm volatile("s_mov_b64 %1, exec\n\ts_mov_b64 exec, %4\n\tds_add_rtn_u32 %0, %2, %3\n\ts_mov_b64 exec, %1\n\ts_waitcnt lgkmcnt(0)"
                   : "=&v"(old), "=&s"(sv) : "v"(LB + L_WT), "v"(incl), "s"(1ull << 63) : "memory");
      const unsigned s0 = incl - tot + __builtin_amdgcn_readlane(old, 63);
      const unsigned s1 = s0 + (c[0] & 0xffffu), s2 = s1 + (c[0] >> 16), s3 = s2 + (c[1] & 0xffffu), s4_ = s3 + (c[1] >> 16);
      const unsigned s5_ = s0 + tot;
      lds_st<u32x3>(L, L_TBL + 16 * tid, u32x3{s0 | (s1 << 16), s2 | (s3 << 16), s4_});
      entry = u32x4{s0 | (s1 << 12) | (s2 << 24), (s2 >> 8) | (tag0 << 4), s3 | (s4_ << 12) | (s5_ << 24), (s5_ >> 8) | (tag1 << 4)};
    }
    if (tid == 0) {
      lds_st<u32x4>(L, L_TAG, u32x4{tag0, tag1, tlo, thi});
      lds_st<u32x4>(L, L_TOT, u32x4{0, 0, 0, 0});
      lds_st<u32x4>(L, L_TOT + 16, u32x4{0, 0, 0, 0});
    }
    {
      u32x4 o;
#pragma unroll
      for (int k = 0; k < 4; ++k) o[k] = cvt_pk(bf_lo(w2raw[k]) * NLOG2E, bf_hi(w2raw[k]) * NLOG2E);
      lds_st<u32x4>(L, L_W + 8192 + tid * 16, o);
    }
    wait_vm0();
    __syncthreads();
    STAMP(3, 1);
    {
      unsigned ps[5];
#pragma unroll
      for (int i = 0; i < 5; ++i) ps[i] = lds_ld<u16>(L, L_TBL + 2 * idx[i]) + rank[i];
#pragma unroll
      for (int i = 0; i < 5; ++i) {
        const unsigned dl = dd[i] - BN * bk[i];
        const unsigned sv = i < 4 ? (unsigned)s4[i] : s5;
        const unsigned eav = i == 4 ? a5 : (i & 1) ? (a2[i >> 1] >> 16) : a2[i >> 1];
        const unsigned pb = L_RAW + 12 * (ps[i] >> 1), odd = ps[i] & 1;
        lds_st<unsigned>(L, pb + 4 * odd, sv | ((L_PD + 16 * dl) << 16));
        lds_st<u16>(L, pb + 8 + 2 * odd, (u16)eav);
      }
    }
    __syncthreads();
    STAMP(4, 1);
    {
      static_assert(REC_STRIDE == (512 + 448) * 16, "copy pattern");
      const unsigned o = WS_RECS + p * REC_STRIDE + 16 * tid;
      st16_rec(rs, o, lds_ld<u32x4>(L, L_RAW + 16 * tid));
      if (tid < 448) st16_rec(rs, o + 8192, lds_ld<u32x4>(L, L_RAW + 8192 + 16 * tid));
    }
    SB();
    if (w >= 4) {
      const int u = (w - 4) >> 1, t = w & 1;
      f32x16 pacc;
#pragma unroll
      for (int i = 0; i < 16; ++i) pacc[i] = 0.f;
#pragma unroll
      for (int s = 0; s < 4; ++s) {
        const int nl = 32 * u + r, nlc = nl < BN ? nl : BN - 1;
        const bf16x8 ha = lds_ld<bf16x8>(L, L_H + nlc * 128 + 32 * s + 16 * hf);
        const unsigned o = L_W + (16 * s + 8 * (lane >> 5) + ((lane & 15) >> 2)) * 128 + (32 * t + 16 * ((lane >> 4) & 1) + 4 * (lane & 3)) * 2;
        const u32x2 b0 = lds_tr(L, o), b1 = lds_tr(L, o + 512);
        pacc = mfma(ha, as_frag(u32x4{b0[0], b0[1], b1[0], b1[1]}), pacc);
      }
#pragma unroll
      for (int q = 0; q < 4; ++q)
        if (4 * u + q < 5)
          lds_st<u32x2>(L, L_PFR + ((4 * u + q) * 2 + t) * 512 + lane * 8,
                        u32x2{cvt_pk(pacc[4 * q] * NLOG2E, pacc[4 * q + 1] * NLOG2E), cvt_pk(pacc[4 * q + 2] * NLOG2E, pacc[4 * q + 3] * NLOG2E)});
    } else if (w == 0) {
      if (lane <= BN)
        lds_st<f32x4>(L, L_PD + 16 * lane, f32x4{bf_lo(sraw[0]), bf_lo(sraw[1]), bf_lo(sraw[2]), lane < BN ? 1.0f : 0.0f});
    } else if (w == 1) {
      lds_st<float>(L, L_WX + 4 * lane, bf_lo(sraw[0]));
    }
#pragma unroll
    for (int t = 0; t < 2; ++t) {
      const float d = bf_lo(craw[2 * t]) * NLOG2E, a = bf_lo(craw[2 * t + 1]) * NLOG2E;
      const unsigned hi = cvt_pk(d, a);
      cw[t][0] = hi;
      cw[t][1] = cvt_pk(d - bf_lo(hi), a - bf_hi(hi));
    }
    wait_vm0();
    __syncthreads();
    if (tid < NBK) st16_dev(rs, WS_ENT + p * 4096 + tid * 16, entry);
    STAMP(5, 1);
  }

  int pa = 0, np = NP;
  for (unsigned it = 0;; ++it) {
    const u16 *h = karg<const u16>(0), *pos = karg<const u16>(1);
    const char* ws = karg<const char>(5);
    const rsrc_t rs = ws_rsrc(ws);
    int tid = tid_;
    asm volatile("" : "+v"(tid));
    const int lane = tid & 63, r = lane & 31, hf = lane >> 5;
    const unsigned l_tot = L_TOT + 16 * (it & 1);
    unsigned pA = 0, pB = 0, pC = 0, oA = 0, oB = 0, oC = 0, d1 = 0, d2 = 0, d3 = 0, d4 = 0;
    if (w < 4) {
      const u32x2 tg = lds_ld<u32x2>(L, L_TAG);
      const unsigned eo = WS_ENT + (tid < NP ? tid : 0) * 4096 + b * 16;
      auto seen = [&](const u32x4& e) { return __all(tid >= NP || ((e[1] >> 4) == tg[0] && (e[3] >> 4) == tg[1])); };
      u32x4 ent, e0 = ld16_dev(rs, eo);
      for (unsigned spins = 0;; ++spins) {
        __builtin_amdgcn_s_sleep(POLL_SLEEP);
        const u32x4 e1 = ld16_dev(rs, eo);
        if (seen(e0)) { ent = e0; break; }
        __builtin_amdgcn_s_sleep(POLL_SLEEP);
        e0 = ld16_dev(rs, eo);
        ent = e1;
        if (seen(e1)) break;
        if (spins > SPIN_LIMIT) {
          __hip_atomic_fetch_or((AS3 unsigned*)(L + l_tot + 12), 1u, __ATOMIC_RELAXED, __HIP_MEMORY_SCOPE_WORKGROUP);
          break;
        }
      }
      STAMP(6, 1);
      const unsigned s0 = ent[0] & 0xfffu, s1 = (ent[0] >> 12) & 0xfffu, s2 = __builtin_amdgcn_alignbit(ent[1], ent[0], 24) & 0xfffu;
      const unsigned s3 = ent[2] & 0xfffu, s4 = (ent[2] >> 12) & 0xfffu, s5 = __builtin_amdgcn_alignbit(ent[3], ent[2], 24) & 0xfffu;
      const bool act = (unsigned)(tid - pa) < (unsigned)np && tid < NP;
      d1 = s1 - s0; d2 = s2 - s0; d3 = s3 - s0; d4 = s4 - s0;
      const unsigned cnt = act ? s5 - s0 : 0;
      const unsigned cA = act ? d1 | ((s2 - s1) << 16) : 0, cB = act ? (s3 - s2) | ((s4 - s3) << 16) : 0, cC = act ? s5 - s4 : 0;
      lds_st<unsigned>(L, L_SEG + 4 * tid, (tid * (CH / 2) + (s0 >> 1)) | ((cnt + (s0 & 1)) << 19) | (s0 << 31));
      const unsigned iA = wave_incl_scan(cA), iB = wave_incl_scan(cB), iC = wave_incl_scan(cC);
      {
        unsigned long long sv;
        asm volatile("s_mov_b64 %3, exec\n\ts_mov_b64 exec, %8\n\t"
                     "ds_add_rtn_u32 %0, %4, %5\n\tds_add_rtn_u32 %1, %4, %6 offset:4\n\tds_add_rtn_u32 %2, %4, %7 offset:8\n\t"
                     "s_mov_b64 exec, %3\n\ts_waitcnt lgkmcnt(0)"
                     : "=&v"(oA), "=&v"(oB), "=&v"(oC), "=&s"(sv)
                     : "v"(LB + l_tot), "v"(iA), "v"(iB), "v"(iC), "s"(1ull << 63) : "memory");
      }
      if (__any(cnt > 255))
        __hip_atomic_fetch_or((AS3 unsigned*)(L + l_tot + 12), 2u, __ATOMIC_RELAXED, __HIP_MEMORY_SCOPE_WORKGROUP);
      pA = iA - cA;
      pB = iB - cB;
      pC = iC - cC;
    }
    __syncthreads();
    STAMP(7, 1);
    const u32x4 tot4 = lds_ld<u32x4>(L, l_tot);
    unsigned e3[4];
#pragma unroll
    for (int k = 0; k < 4; ++k) e3[k] = lds_ld<unsigned>(L, L_SEG + 4 * (64 * k + (tid >> 3)));
    const unsigned flags = __builtin_amdgcn_readfirstlane(tot4[3]);
    if (flags & 1) return;
    if (tid == 511) lds_st<u32x4>(L, L_TOT + 16 * (~it & 1), u32x4{0, 0, 0, 0});
    if (blockIdx.x == 0 && tid == 448 && it == 0) __builtin_amdgcn_raw_buffer_store_b64(lds_ld<u32x2>(L, L_TAG + 8), rs, WS_CTL, 0, 16);
    const unsigned j2 = 2 * (tid & 7);
    u32x3 ra[4], rb[4];
    unsigned cntp[4], ro[4];
#pragma unroll
    for (int k = 0; k < 4; ++k) {
      cntp[k] = (e3[k] >> 19) & 0xfffu;
      ro[k] = ((e3[k] & 0x7ffffu) + (tid & 7)) * 12 + WS_RECS;
      ra[k] = ld12_rec(rs, j2 < cntp[k] ? ro[k] : NO_DATA);
    }
#pragma unroll
    for (int k = 0; k < 4; ++k) rb[k] = ld12_rec(rs, j2 + 16 < cntp[k] ? ro[k] + 96 : NO_DATA);
    SB();
    const unsigned tA = __builtin_amdgcn_readfirstlane(tot4[0]), tB = __builtin_amdgcn_readfirstlane(tot4[1]);
    const unsigned tot[5] = {tA & 0xffffu, tA >> 16, tB & 0xffffu, tB >> 16, (unsigned)__builtin_amdgcn_readfirstlane(tot4[2])};
    unsigned base[6];
    base[0] = 0;
#pragma unroll
    for (int g = 0; g < 5; ++g) base[g + 1] = base[g] + ((tot[g] + 31) & ~31u);
    if (((flags & 2) && np > 16) || base[5] > (unsigned)CAP) {
      np = (np + 1) >> 1;
      __syncthreads();
      continue;
    }
    if (w < 4) {
      constexpr unsigned R8 = L_RAW / 8;
      pA += __builtin_amdgcn_readlane(oA, 63);
      pB += __builtin_amdgcn_readlane(oB, 63);
      pC += __builtin_amdgcn_readlane(oC, 63);
      const unsigned v0 = ((pA & 0xffffu) + (base[0] + R8)) << 3, v1 = ((pA >> 16) - d1 + (base[1] + R8)) << 3;
      const unsigned v2 = ((pB & 0xffffu) - d2 + (base[2] + R8)) << 3, v3 = ((pB >> 16) - d3 + (base[3] + R8)) << 3;
      const unsigned v4 = (pC - d4 + (base[4] + R8)) << 3;
      lds_st<u32x3>(L, L_TBL + 16 * tid, u32x3{(v0 & 0xffffu) | (v1 << 16), (v2 & 0xffffu) | (v3 << 16), v4});
    } else {
      const u32x2 pad = {(unsigned)(L_PD + 16 * BN) << 16, 0};
      const unsigned lo0 = w == 4 ? base[0] + tot[0] : w == 5 ? base[2] + tot[2] : base[4] + tot[4];
      const unsigned lo1 = w == 4 ? base[1] : w == 5 ? base[3] : base[5];
      const unsigned hi0 = w == 4 ? base[1] + tot[1] : base[3] + tot[3], hi1 = w == 4 ? base[2] : base[4];
      const unsigned slot = (hf ? hi0 : lo0) + r;
      if (w < 7 && slot < (hf ? (w == 6 ? 0u : hi1) : lo1)) lds_st<u32x2>(L, L_RAW + 8 * slot, pad);
      if (tid - 256 < PADS) lds_st<u32x2>(L, L_RAW + 8 * (base[5] + tid - 256), pad);
    }

    const int T = base[5] >> 5;
#define SPLIT_OLD 45
    const int q4 = T >> 2, r4 = T & 3, ws4 = w & 3;
    const int sa4 = ws4 * q4 + (ws4 < r4 ? ws4 : r4), sn4 = q4 + (ws4 < r4 ? 1 : 0);
    const int nold = (sn4 * SPLIT_OLD + 32) >> 6;
    const int ta = w < 4 ? sa4 : sa4 + nold;
    const int tb = w < 4 ? sa4 + nold : sa4 + sn4;
    const int tg1 = base[1] >> 5, tg2 = base[2] >> 5, tg3 = base[3] >> 5, tg4 = base[4] >> 5;

    const char *qtab = (const char*)h, *pos6 = (const char*)pos;
    const unsigned ringb = L_RING + w * 4096;
    unsigned aq[4];
#pragma unroll
    for (int s = 0; s < 4; ++s) aq[s] = ringb + r * 128 + (((2 * s + hf) ^ ((r >> 1) & 7)) << 4);
    const unsigned dc0 = ((lane & 7) ^ ((lane >> 4) & 7)) << 4, dc1 = ((lane & 7) ^ ((4 + (lane >> 4)) & 7)) << 4;
    const unsigned Sb = L_S + w * S_WAVE;
    const unsigned pi_e = (r & 0x13) | ((r & 4) << 1) | ((r & 8) >> 1);
    const unsigned sa_c = Sb + 2 * pi_e;
    const unsigned s_rd = Sb + r * 80 + 16 * hf;
    const int g16 = lane >> 4, tq = (lane & 15) >> 2, tp = lane & 3, tu = g16 & 1, th = g16 >> 1;
    const unsigned s_tr = Sb + (4 * (4 * th + tq) + 3) * 80 + 2 * (16 * tu + 8 * (tp & 1) + 4 * (tp >> 1));
    const unsigned acc_l = L_ACC + hf * ACC_NODE + 4 * r;
    bf16x8 wq[2][4];
    auto group_of = [&](int t) {
      int g;
      asm("s_cmp_ge_i32 %1, %2\n\ts_cselect_b32 %0, 1, 0\n\ts_cmp_ge_i32 %1, %3\n\ts_addc_u32 %0, %0, 0\n\t"
          "s_cmp_ge_i32 %1, %4\n\ts_addc_u32 %0, %0, 0\n\ts_cmp_ge_i32 %1, %5\n\ts_addc_u32 %0, %0, 0"
          : "=&s"(g) : "s"(t), "s"(tg1), "s"(tg2), "s"(tg3), "s"(tg4) : "scc");
      return g;
    };
    auto group_end = [&](int t) {
      int e;
      asm("s_cmp_gt_i32 %5, %1\n\ts_cselect_b32 %0, %5, %6\n\ts_cmp_gt_i32 %4, %1\n\ts_cselect_b32 %0, %4, %0\n\t"
          "s_cmp_gt_i32 %3, %1\n\ts_cselect_b32 %0, %3, %0\n\ts_cmp_gt_i32 %2, %1\n\ts_cselect_b32 %0, %2, %0"
          : "=&s"(e) : "s"(t), "s"(tg1), "s"(tg2), "s"(tg3), "s"(tg4), "s"(T) : "scc");
      return e;
    };
    int gend = group_end(ta);
    int tflush = -1;

    bf16x8 bxA0, bxA1, bxB0, bxB1;
    auto load_bx = [&](int gg) {
      const u32x2 p0 = lds_ld<u32x2>(L, L_PFR + gg * 1024 + lane * 8), p1 = lds_ld<u32x2>(L, L_PFR + gg * 1024 + 512 + lane * 8);
      bxA0 = as_frag(u32x4{p0[0], p0[1], hf ? 0u : cw[0][0], hf ? 0u : cw[0][1]});
      bxB0 = as_frag(u32x4{p0[0], p0[1], hf ? cw[0][0] : 0u, hf ? cw[0][1] : 0u});
      bxA1 = as_frag(u32x4{p1[0], p1[1], hf ? 0u : cw[1][0], hf ? 0u : cw[1][1]});
      bxB1 = as_frag(u32x4{p1[0], p1[1], hf ? cw[1][0] : 0u, hf ? cw[1][1] : 0u});
      asm volatile("" : "+v"(bxA0), "+v"(bxB0), "+v"(bxA1), "+v"(bxB1));
    };
#pragma unroll
    for (int t = 0; t < 2; ++t)
#pragma unroll
      for (int s = 0; s < 4; ++s) {
        const unsigned o = L_W + 8192 + (16 * s + 8 * th + tq) * 128 + (32 * t + 16 * tu + 4 * tp) * 2;
        const u32x2 b0 = lds_tr(L, o), b1 = lds_tr(L, o + 512);
        wq[t][s] = as_frag(u32x4{b0[0], b0[1], b1[0], b1[1]});
      }
    load_bx(group_of(ta));
    tiles_init();
    set_m0(LB + ringb);
    __syncthreads();
    STAMP(8, 1);

    {
      auto second = [](unsigned w0, unsigned eahi) { return (((w0 >> 20) & 7) * 320) | eahi; };
      const unsigned tb0 = L_TBL + 16 * (tid >> 3);
      unsigned a0[4], a1[4], r8[4];
#pragma unroll
      for (int k = 0; k < 4; ++k) {
        r8[k] = 8 * j2 - ((e3[k] >> 31) << 3);
        a0[k] = lds_ld<u16>(L, tb0 + 1024 * k + ((ra[k][0] >> 22) & 14)) + r8[k];
        a1[k] = lds_ld<u16>(L, tb0 + 1024 * k + ((ra[k][1] >> 22) & 14)) + r8[k];
      }
#pragma unroll
      for (int k = 0; k < 4; ++k) {
        if (j2 < cntp[k] && (int)r8[k] >= 0) lds_st<u32x2>(L, a0[k], u32x2{ra[k][0], second(ra[k][0], ra[k][2] << 16)});
        if (j2 + 1 < cntp[k]) lds_st<u32x2>(L, a1[k] + 8, u32x2{ra[k][1], second(ra[k][1], ra[k][2] & 0xffff0000u)});
      }
#pragma unroll
      for (int k = 0; k < 4; ++k) {
        if (j2 + 16 < cntp[k]) {
          const unsigned b0 = lds_ld<u16>(L, tb0 + 1024 * k + ((rb[k][0] >> 22) & 14)) + r8[k];
          const unsigned b1 = lds_ld<u16>(L, tb0 + 1024 * k + ((rb[k][1] >> 22) & 14)) + r8[k];
          lds_st<u32x2>(L, b0 + 128, u32x2{rb[k][0], second(rb[k][0], rb[k][2] << 16)});
          if (j2 + 17 < cntp[k]) lds_st<u32x2>(L, b1 + 136, u32x2{rb[k][1], second(rb[k][1], rb[k][2] & 0xffff0000u)});
        }
      }
#pragma unroll
      for (int k = 0; k < 4; ++k) {
        const unsigned odd = e3[k] >> 31;
        for (unsigned q = (tid & 7) + 32; q < cntp[k]; q += 8) {
          const unsigned pb = ((e3[k] & 0x7ffffu) + (q >> 1)) * 12 + WS_RECS;
          const unsigned w0 = ld4_rec(rs, pb + 4 * (q & 1)), eav = ld2_rec(rs, pb + 8 + 2 * (q & 1));
          const unsigned a = lds_ld<u16>(L, tb0 + 1024 * k + ((w0 >> 22) & 14));
          lds_st<u32x2>(L, a + 8 * (q - odd), u32x2{w0, second(w0, eav << 16)});
        }
      }
    }
    __syncthreads();
    STAMP(9, 1);

    if (pa + np >= NP) {
      const char* wh = karg<const char>(4);
      dma_piece(wh + w * 2048, lane * 16, LB + L_W + w * 2048);
      dma_piece(wh + w * 2048 + 1024, lane * 16, LB + L_W + w * 2048 + 1024);
    }
    if (ta < tb) {
      auto flush = [&](int gg) {
        const unsigned a = LB + acc_l + gg * (8 * ACC_NODE);
        if (lds_lock(LB + L_LOCK + 4 * gg) != 0) acc_gather(a, a + 2 * ACC_NODE, a + 4 * ACC_NODE, a + 6 * ACC_NODE);
        acc_store(a, a + 2 * ACC_NODE, a + 4 * ACC_NODE, a + 6 * ACC_NODE);
        lds_st<unsigned>(L, L_LOCK + 4 * gg, 2u);
        asm volatile("" ::: "memory");
      };
      auto flush_last = [&](int gg) {
        const unsigned a = LB + acc_l + gg * (8 * ACC_NODE);
        if (lds_lock(LB + L_LOCK + 4 * gg) != 0) acc_gather_last(a, a + 2 * ACC_NODE, a + 4 * ACC_NODE, a + 6 * ACC_NODE);
        acc_store_last(a, a + 2 * ACC_NODE, a + 4 * ACC_NODE, a + 6 * ACC_NODE);
        lds_st<unsigned>(L, L_LOCK + 4 * gg, 2u);
        asm volatile("" ::: "memory");
      };
      struct Idx { unsigned i0, i1, i2, i3; };
      unsigned a_idx = L_RAW + ta * 256 + (lane >> 3) * 8, a_raw = L_RAW + ta * 256 + lane * 8;
      auto idx_load = [&](int dt) {
        const unsigned a = a_idx + dt * 256;
        return Idx{lds_ld<u16>(L, a), lds_ld<u16>(L, a + 64), lds_ld<u16>(L, a + 128), lds_ld<u16>(L, a + 192)};
      };
      auto issue_rows = [&](const Idx& x) {
        dma_tile(qtab, x.i0 * 128 + dc0, x.i1 * 128 + dc1, x.i2 * 128 + dc0, x.i3 * 128 + dc1);
      };
      auto raw_load = [&](int dt) {
        const unsigned a = a_raw + dt * 256;
        return Raw{lds_ld<u16>(L, a), lds_ld<u16>(L, a + 2), lds_ld<unsigned>(L, a + 4), lds_ld<u16>(L, a + 4)};
      };
      auto pos_load = [&](const Raw& x) {
        asm volatile("global_load_dwordx2 v[174:175], %0, %1" :: "v"(x.src * 6u), "s"(pos6) : "memory");
      };
      auto pd_load = [&](const Raw& x) { return lds_ld<f32x4>(L, x.pda); };
      auto prep = [&](const Raw& x, const f32x4& pd) {
        float xs, ys, zs;
        asm volatile("v_lshlrev_b32 %0, 16, v174\n\tv_and_b32 %1, 0xffff0000, v174\n\tv_lshlrev_b32 %2, 16, v175"
                     : "=v"(xs), "=v"(ys), "=v"(zs));
        const f32x2 dxy = f32x2{pd[0], pd[1]} - f32x2{xs, ys};
        unsigned wa = cvt_pk(dxy[0], dxy[1]), wb = cvt_pk(pd[2] - zs, pd[3]);
        const float dzr = bf_lo(wb);
        const bf16x2v wav = __builtin_bit_cast(bf16x2v, wa);
        const float d2 = __builtin_amdgcn_fdot2_f32_bf16(wav, wav, dzr * dzr, false);
        Prep o;
        o.de = cvt_pk(d2, as_f(x.w1));
        asm volatile("" : "+v"(o.de), "+v"(wa), "+v"(wb));
        const unsigned P = x.n320 + sa_c, Q = P + 160;
        const auto sa = __builtin_amdgcn_permlane32_swap(P, Q, false, false);
        const auto sd = __builtin_amdgcn_permlane32_swap(wa, wb, false, false);
        o.saA = sa[0]; o.saB = sa[1]; o.sdA = sd[0]; o.sdB = sd[1];
        return o;
      };

      struct Ops { bf16x8 q0, q1, q2, q3, as0, as1, ax; };
      auto read_ops = [&](unsigned sa, unsigned sd, unsigned de) {
        Ops o;
        o.q0 = lds_ld<bf16x8>(L, aq[0]); o.q1 = lds_ld<bf16x8>(L, aq[1]);
        o.q2 = lds_ld<bf16x8>(L, aq[2]); o.q3 = lds_ld<bf16x8>(L, aq[3]);
        lds_st<u16>(L, sa, (u16)sd);
        lds_st<u16>(L, sa + 80, (u16)(sd >> 16));
        const u32x2 oh = lds_tr(L, s_tr);
        o.as0 = lds_ld<bf16x8>(L, s_rd);
        o.as1 = lds_ld<bf16x8>(L, s_rd + 32);
        lds_st<u16>(L, sa, (u16)0);
        lds_st<u16>(L, sa + 80, (u16)0);
        unsigned de2;
        asm volatile("v_mov_b32 %0, %1" : "=v"(de2) : "v"(de));
        o.ax = as_frag(u32x4{oh[0], oh[1], de, de2});
        return o;
      };
      auto alpha = [&](int t, const Ops& c, const bf16x8& asl, const bf16x8& bx1, auto&& mid) {
        alpha1(asl);
        if (__builtin_expect(t == tflush, 0)) flush(group_of(t - 1));
        alpha2(c.ax, bx1, c.q0, wq[1][0]);
        SB(); mid(); SB();
        alpha3(c.q1, wq[1][1], c.q2, wq[1][2], c.q3, wq[1][3], c.as0);
      };
      auto beta = [&](const Ops& c, const Ops& n, const bf16x8& bx0n, auto&& mid) {
        beta1(c.as1);
        beta2(n.ax, bx0n, n.q0, wq[0][0]);
        SB(); mid(); SB();
        beta3(n.q1, wq[0][1], n.q2, wq[0][2], n.q3, wq[0][3], c.as0);
      };
      auto next_group = [&](int t) {
        if (__builtin_expect(t == gend, 0)) { gend = group_end(t); tflush = t; load_bx(group_of(t)); }
      };

      issue_rows(idx_load(0));
      Raw rw = raw_load(0);
      pos_load(rw);
      f32x4 pd = pd_load(rw);
      SB();
      wait_vm0();
      Prep pr = prep(rw, pd);
      rw = raw_load(2);
      Ops A = read_ops(pr.saA, pr.sdA, pr.de), B;
#pragma unroll
      for (int j = 0; j < 8; ++j) B.as1[j] = 0;
      asm volatile("" : "+v"(B.as1));
      const int glast = group_of(tb - 1);
      const Idx ix0 = idx_load(1);
      chain_y0(A.ax, bxA0, A.q0, wq[0][0], A.q1, wq[0][1], A.q2, wq[0][2], A.q3, wq[0][3]);
      asm volatile("s_waitcnt lgkmcnt(0)" ::: "memory");
      issue_rows(ix0);
      STAMP(13, 1);
      CSTAMP(10, 1);
      int t = ta;
      auto tile_a = [&]() {
        alpha(t, A, B.as1, bxA1, [&]() { pos_load(rw); });
        wait_vm0();
        next_group(t + 1);
        const Idx ixa = idx_load(2);
        B = read_ops(pr.saB, pr.sdB, pr.de);
        pd = pd_load(rw);
        SB();
        beta(A, B, bxB0, [&]() { issue_rows(ixa); });
      };
      for (; t + 2 <= tb; t += 2) {
        tile_a();
        alpha(t + 1, B, A.as1, bxB1, [&]() { pr = prep(rw, pd); });
        wait_vm0();
        next_group(t + 2);
        const Idx ixb = idx_load(3);
        A = read_ops(pr.saA, pr.sdA, pr.de);
        rw = raw_load(4);
        SB();
        beta(B, A, bxA0, [&]() { issue_rows(ixb); });
        a_idx += 512;
        a_raw += 512;
      }
      if (t < tb) {
        tile_a();
        last_block(A.as1);
      } else {
        last_block(B.as1);
      }
      flush_last(glast);
      STAMP(14, 1);
      CSTAMP(11, 1);
    } else {
      wait_vm0();
    }
    pa += np;
    if (pa >= NP) break;
    wait_vm0();
    __syncthreads();
  }
  __syncthreads();
  STAMP(15, 1);

  u16* out = karg<u16>(6);
  int tid = tid_;
  asm volatile("" : "+v"(tid));
  const int lane = tid & 63, r = lane & 31, hf = lane >> 5;
  if (w < 4) {
    const int g16 = lane >> 4, tq = (lane & 15) >> 2, tp = lane & 3, tu = g16 & 1, th = g16 >> 1;
    const int u = w >> 1, t = w & 1;
    const int nl = 32 * u + r, nlc = nl < BN ? nl : BN - 1;
    const unsigned hrow = L_H + nlc * 128;
    f32x16 d;
#pragma unroll
    for (int i = 0; i < 16; ++i) d[i] = 0.f;
    const unsigned w_tr = L_W + (8 * th + tq) * 128 + (32 * t + 16 * tu + 4 * tp) * 2;
#pragma unroll
    for (int s = 0; s < 8; ++s) {
      const u32x2 a0 = lds_tr(L, w_tr + s * 2048), a1 = lds_tr(L, w_tr + s * 2048 + 512);
      const bf16x8 a = as_frag(u32x4{a0[0], a0[1], a1[0], a1[1]});
      bf16x8 bb;
      if (s < 4) {
        bb = lds_ld<bf16x8>(L, hrow + 32 * s + 16 * hf);
      } else {
        const unsigned o = L_ACC + nlc * ACC_NODE + 3 * 256 + (16 * (s - 4) + 8 * hf) * 4;
        const f32x4 m0 = lds_ld<f32x4>(L, o) * NLN2, m1 = lds_ld<f32x4>(L, o + 16) * NLN2;
        bb = as_frag(u32x4{cvt_pk(m0[0], m0[1]), cvt_pk(m0[2], m0[3]), cvt_pk(m1[0], m1[1]), cvt_pk(m1[2], m1[3])});
      }
      d = mfma(a, bb, d);
    }
    if (nl < BN) {
#pragma unroll
      for (int q = 0; q < 4; ++q) {
        const int f = 32 * t + 8 * q + 4 * hf;
        const u32x2 hv = lds_ld<u32x2>(L, hrow + 2 * f);
        u32x2 o;
#pragma unroll
        for (int k = 0; k < 2; ++k) {
          const unsigned pb = cvt_pk(d[4 * q + 2 * k], d[4 * q + 2 * k + 1]);
          const f32x2 x = {bf_lo(pb), bf_hi(pb)}, y = x * NLOG2E;
          const f32x2 den = f32x2{__builtin_amdgcn_exp2f(y[0]), __builtin_amdgcn_exp2f(y[1])} + 1.0f;
          const f32x2 act = x * f32x2{__builtin_amdgcn_rcpf(den[0]), __builtin_amdgcn_rcpf(den[1])};
          const unsigned ab = cvt_pk(act[0], act[1]);
          const f32x2 sum = f32x2{bf_lo(hv[k]), bf_hi(hv[k])} + f32x2{bf_lo(ab), bf_hi(ab)};
          o[k] = cvt_pk(sum[0], sum[1]);
        }
        *(u32x2*)(out + (size_t)(n0 + nl) * 64 + f) = o;
      }
    }
  } else {
    const int idx = tid - 256, nc = idx >> 1, half = idx & 1;
    const int ncc = nc < 3 * BN ? nc : 3 * BN - 1;
    const int n = ncc / 3, c = ncc - 3 * n;
    const unsigned o = L_ACC + n * ACC_NODE + c * 256 + half * 128;
    float s = 0.f;
#pragma unroll
    for (int k = 0; k < 8; ++k) {
      const f32x4 v = lds_ld<f32x4>(L, o + 16 * k), x = lds_ld<f32x4>(L, L_WX + half * 128 + 16 * k);
      s += v[0] * x[0] + v[1] * x[1] + v[2] * x[2] + v[3] * x[3];
    }
    s += __builtin_bit_cast(float, __builtin_amdgcn_update_dpp(0, __builtin_bit_cast(int, s), 0xb1, 0xf, 0xf, true));
    if (nc < 3 * BN && half == 0) {
      const float p0 = lds_ld<float>(L, L_PD + 16 * n + 4 * c);
      out[(size_t)NN * 64 + (size_t)n0 * 3 + nc] = (u16)cvt_pk(p0 + bf_round(s * NLN2), 0.f);
    }
  }
}

}

extern "C" void kernel_launch(void* const* d_in, const int* in_sizes, int n_in, void* d_out, int out_size, void* d_ws, size_t ws_size,
                              hipStream_t stream) {
  const u16* h = (const u16*)d_in[0];
  const u16* pos = (const u16*)d_in[1];
  const u16* ea = (const u16*)d_in[2];
  const int* src = (const int*)d_in[3];
  const int* dst = (const int*)d_in[4];
  const u16* we = (const u16*)d_in[5];
  const u16* wx = (const u16*)d_in[6];
  const u16* wh = (const u16*)d_in[7];
  egnn_kernel<<<NBK, 512, 0, stream>>>(h, pos, we, wx, wh, (char*)d_ws, (u16*)d_out, src, dst, ea);
}
```

```cpp
#include <hip/hip_runtime.h>
#include <hip/hip_bf16.h>
#include <stdint.h>

#define DI __device__ __forceinline__
#define AS3 __attribute__((address_space(3)))
#define SB() __builtin_amdgcn_sched_barrier(0)

typedef short bf16x8 __attribute__((ext_vector_type(8)));
typedef short s16x4 __attribute__((ext_vector_type(4)));
typedef float f32x16 __attribute__((ext_vector_type(16)));
typedef float f32x4 __attribute__((ext_vector_type(4)));
typedef float f32x2 __attribute__((ext_vector_type(2)));
typedef unsigned u32x4 __attribute__((ext_vector_type(4)));
typedef unsigned u32x3 __attribute__((ext_vector_type(3)));
typedef unsigned u32x2 __attribute__((ext_vector_type(2)));
typedef int i32x4 __attribute__((ext_vector_type(4)));
typedef __bf16 bf16x2v __attribute__((ext_vector_type(2)));
typedef unsigned short u16;

namespace {

constexpr int NN = 10000;
constexpr int NE = 640000;
constexpr int BN = 40;
constexpr int NBK = NN / BN;
constexpr int NP = NBK;
constexpr int CH = NE / NP;
static_assert(CH == 5 * 512 && CH < 4096, "five edges per thread, 12-bit run starts");
constexpr float NLOG2E = -1.4426950408889634f;
constexpr float NLN2 = -0.6931471805599453f;
constexpr unsigned SPIN_LIMIT = 1u << 20;
constexpr unsigned NO_DATA = 0xffffff00u;
constexpr int POLL_SLEEP = 4;

constexpr unsigned WS_CTL = 0;
constexpr unsigned WS_ENT = 256;
constexpr unsigned WS_RECS = WS_ENT + NP * 4096;
constexpr unsigned REC_STRIDE = CH * 6;
constexpr unsigned WS_END = WS_RECS + NP * REC_STRIDE;
static_assert(WS_RECS % 128 == 0 && REC_STRIDE % 128 == 0, "no cache line holds records of two producers");

constexpr int CAP = 2880;
constexpr int PADS = 160;
constexpr int L_S = 0;
constexpr int S_WAVE = 2560;
constexpr int L_PD = 20480;
constexpr int L_WT = 21248;
constexpr int L_TOT = L_WT + 16;
constexpr int L_LOCK = 21312;
constexpr int L_TAG = 21344;
constexpr int L_WV = 21376;
constexpr int L_WX = 21504;
constexpr int L_TBL = 21760;
constexpr int L_RAW = L_TBL + 4096;
constexpr int L_PFR = L_RAW + (CAP + PADS) * 8;
constexpr int L_RING = L_PFR + 5120;
constexpr int L_ACC = L_RING + 8 * 4096;
constexpr int ACC_NODE = 1040;
constexpr int L_W = L_ACC + BN * ACC_NODE;
constexpr int L_H = L_W + 16384;
constexpr int L_SEG = L_H + BN * 128;
constexpr int L_END = L_SEG + 1024;
static_assert(L_PD % 1024 == 0, "group id is read from bits 7..9 of the pos address");
static_assert(L_RAW + (CAP + PADS) * 8 < 65536 && L_RAW >= 8 * CH && CAP >= CH + 5 * 31, "run table holds 16-bit LDS addresses");
static_assert(L_PFR % 16 == 0 && L_RING % 16 == 0 && L_ACC % 16 == 0 && L_W % 16 == 0 && L_END <= 163840, "LDS map");

DI unsigned cvt_pk(float a, float b) { f32x2 v = {a, b}; return __builtin_bit_cast(unsigned, __builtin_convertvector(v, bf16x2v)); }
DI float as_f(unsigned u) { return __builtin_bit_cast(float, u); }
DI float bf_lo(unsigned u) { return __builtin_bit_cast(float, u << 16); }
DI float bf_hi(unsigned u) { return __builtin_bit_cast(float, u & 0xffff0000u); }
DI float bf_round(float x) { return bf_lo(cvt_pk(x, x)); }
DI f32x16 mfma(bf16x8 a, bf16x8 b, f32x16 c) { return __builtin_amdgcn_mfma_f32_32x32x16_bf16(a, b, c, 0, 0, 0); }
DI bf16x8 as_frag(u32x4 v) { return __builtin_bit_cast(bf16x8, v); }

DI unsigned wave_incl_scan(unsigned x) {
  x += __builtin_amdgcn_update_dpp(0, (int)x, 0x111, 0xf, 0xf, true);
  x += __builtin_amdgcn_update_dpp(0, (int)x, 0x112, 0xf, 0xf, true);
  x += __builtin_amdgcn_update_dpp(0, (int)x, 0x114, 0xf, 0xf, true);
  x += __builtin_amdgcn_update_dpp(0, (int)x, 0x118, 0xf, 0xf, true);
  x += __builtin_amdgcn_update_dpp(0, (int)x, 0x142, 0xa, 0xf, false);
  x += __builtin_amdgcn_update_dpp(0, (int)x, 0x143, 0xc, 0xf, false);
  return x;
}

template <typename T> DI T lds_ld(AS3 char* L, unsigned off) { return *(AS3 T*)(L + off); }
template <typename T> DI void lds_st(AS3 char* L, unsigned off, T v) { *(AS3 T*)(L + off) = v; }
DI u32x2 lds_tr(AS3 char* L, unsigned off) {
  return __builtin_bit_cast(u32x2, __builtin_amdgcn_ds_read_tr16_b64_v4i16((AS3 s16x4*)(L + off)));
}

DI const char* uni_ptr(const char* p) {
  const unsigned long long v = (unsigned long long)p;
  const unsigned lo = __builtin_amdgcn_readfirstlane((int)(unsigned)v), hi = __builtin_amdgcn_readfirstlane((int)(unsigned)(v >> 32));
  return (const char*)(__attribute__((address_space(1))) const char*)(((unsigned long long)hi << 32) | lo);
}
DI void dma_piece(const char* base_, unsigned voff, unsigned ldsaddr_) {
  const char* base = uni_ptr(base_);
  const unsigned ldsaddr = __builtin_amdgcn_readfirstlane((int)ldsaddr_);
  unsigned keep;
  asm volatile("s_mov_b32 %0, m0\n\ts_mov_b32 m0, %3\n\ts_nop 4\n\tglobal_load_lds_dwordx4 %1, %2\n\ts_mov_b32 m0, %0"
               : "=&s"(keep) : "v"(voff), "s"(base), "s"(ldsaddr) : "memory");
}
DI void set_m0(unsigned ldsaddr) { asm volatile("s_mov_b32 m0, %0\n\ts_nop 0" :: "s"(ldsaddr) : "memory"); }
typedef __amdgpu_buffer_rsrc_t rsrc_t;
template <int OFF> DI void rows_index(unsigned a) {
  asm volatile("ds_read_u16 v166, %0 offset:%1\n\tds_read_u16 v168, %0 offset:%2\n\tds_read_u16 v170, %0 offset:%3\n\tds_read_u16 v172, %0 offset:%4"
               :: "v"(a), "i"(OFF), "i"(OFF + 64), "i"(OFF + 128), "i"(OFF + 192) : "memory");
}
DI void rows_gather(rsrc_t rows) {
  asm volatile("s_waitcnt lgkmcnt(0)\n\t"
               "buffer_load_dwordx4 v[166:167], %0, %1 idxen offen lds\n\t"
               "buffer_load_dwordx4 v[168:169], %0, %2 idxen offen offset:1024 lds\n\t"
               "buffer_load_dwordx4 v[170:171], %0, %3 idxen offen offset:2048 lds\n\t"
               "buffer_load_dwordx4 v[172:173], %0, 0 idxen offen offset:3072 lds"
               :: "s"(rows), "s"(3072u), "s"(2048u), "s"(1024u) : "memory");
}
DI rsrc_t ws_rsrc(const char* ws) { return __builtin_amdgcn_make_buffer_rsrc((void*)ws, 0, WS_END, 0x00020000); }
DI u32x4 ld16_dev(rsrc_t r, unsigned off) { return __builtin_amdgcn_raw_buffer_load_b128(r, off, 0, 16); }
DI u32x3 ld12_rec(rsrc_t r, unsigned off) { return __builtin_amdgcn_raw_buffer_load_b96(r, off, 0, 16); }
DI unsigned ld4_rec(rsrc_t r, unsigned off) { return __builtin_amdgcn_raw_buffer_load_b32(r, off, 0, 16); }
DI unsigned ld2_rec(rsrc_t r, unsigned off) { return __builtin_amdgcn_raw_buffer_load_b16(r, off, 0, 16); }
DI u32x2 ld8_dev(rsrc_t r, unsigned off) { return __builtin_amdgcn_raw_buffer_load_b64(r, off, 0, 16); }
DI void st16_dev(rsrc_t r, unsigned off, u32x4 v) { __builtin_amdgcn_raw_buffer_store_b128(v, r, off, 0, 16); }
DI void wait_vm0() { asm volatile("s_waitcnt vmcnt(0)" ::: "memory"); }

struct Prep {
  unsigned sdA, sdB;
  unsigned saA, saB;
  unsigned de;
};
struct Raw { unsigned src, pda, w1, n320; };

#pragma clang diagnostic ignored "-Winline-asm"
#define T4(op, d, s0, s1, s2, s3, d1, d2, d3) op " v" #d ", v" #s0 "\n\t" op " v" #d1 ", v" #s1 "\n\t" op " v" #d2 ", v" #s2 "\n\t" op " v" #d3 ", v" #s3 "\n\t"
#define EXP_LO(a, b, c, d) T4("v_exp_f32", 184, a, b, c, d, 185, 186, 187)
#define EXP_HI(a, b, c, d) T4("v_exp_f32", 188, a, b, c, d, 189, 190, 191)
#define RCP_LO T4("v_rcp_f32", 184, 184, 185, 186, 187, 185, 186, 187)
#define RCP_HI T4("v_rcp_f32", 188, 188, 189, 190, 191, 189, 190, 191)
#define PKA(a, b) "v_pk_add_f32 v[" #a ":" #b "], v[" #a ":" #b "], 1.0 op_sel_hi:[1,0]\n\t"
#define ADD1 PKA(184, 185) PKA(186, 187) PKA(188, 189) PKA(190, 191)
#define PKM(a, b, c, d) "v_pk_mul_f32 v[" #a ":" #b "], v[" #a ":" #b "], v[" #c ":" #d "]\n\t"
#define MULY(a0, a1, a2, a3, a4, a5, a6, a7) PKM(184, 185, a0, a1) PKM(186, 187, a2, a3) PKM(188, 189, a4, a5) PKM(190, 191, a6, a7)
#define CVT(d, a, b) "v_cvt_pk_bf16_f32 v" #d ", v" #a ", v" #b "\n\t"
#define PACK(d0, d1, d2, d3) CVT(d0, 184, 185) CVT(d1, 186, 187) CVT(d2, 188, 189) CVT(d3, 190, 191)
#define MF(acc, a, b) "v_mfma_f32_32x32x16_bf16 " acc ", " a ", " b ", " acc "\n\t"
#define MF0(acc, a, b) "v_mfma_f32_32x32x16_bf16 " acc ", " a ", " b ", 0\n\t"
#define Y0 "v[192:207]"
#define Y1 "v[208:223]"
#define ACC0 "v[224:239]"
#define ACC1 "v[240:255]"
#define X0 "v[176:179]"
#define X1 "v[180:183]"

DI void tiles_init() {
  asm volatile(
               "v_mov_b64 v[224:225], 0\n\t"
               "v_mov_b64 v[226:227], 0\n\t"
               "v_mov_b64 v[228:229], 0\n\t"
               "v_mov_b64 v[230:231], 0\n\t"
               "v_mov_b64 v[232:233], 0\n\t"
               "v_mov_b64 v[234:235], 0\n\t"
               "v_mov_b64 v[236:237], 0\n\t"
               "v_mov_b64 v[238:239], 0\n\t"
               "v_mov_b64 v[240:241], 0\n\t"
               "v_mov_b64 v[242:243], 0\n\t"
               "v_mov_b64 v[244:245], 0\n\t"
               "v_mov_b64 v[246:247], 0\n\t"
               "v_mov_b64 v[248:249], 0\n\t"
               "v_mov_b64 v[250:251], 0\n\t"
               "v_mov_b64 v[252:253], 0\n\t"
               "v_mov_b64 v[254:255], 0\n\t"
               "v_mov_b64 v[180:181], 0\n\t"
               "v_mov_b64 v[182:183], 0\n\t"
               "s_nop 1" ::: "v166", "v255");
}
DI void alpha1(const bf16x8& asl) {
  asm volatile(EXP_LO(192, 193, 194, 195) MF(ACC1, "%[asl]", X1) EXP_HI(196, 197, 198, 199) :: [asl] "v"(asl));
}
DI void alpha2(const bf16x8& ax, const bf16x8& bx, const bf16x8& q0, const bf16x8& w0) {
  asm volatile(ADD1 MF0(Y1, "%[ax]", "%[bx]") RCP_LO MF(Y1, "%[q0]", "%[w0]") RCP_HI
               MULY(192, 193, 194, 195, 196, 197, 198, 199) PACK(176, 177, 178, 179)
               :: [ax] "v"(ax), [bx] "v"(bx), [q0] "v"(q0), [w0] "v"(w0));
}
DI void alpha3(const bf16x8& q1, const bf16x8& w1, const bf16x8& q2, const bf16x8& w2, const bf16x8& q3, const bf16x8& w3, const bf16x8& as0) {
  asm volatile(MF(Y1, "%[q1]", "%[w1]") EXP_LO(200, 201, 202, 203) MF(Y1, "%[q2]", "%[w2]") EXP_HI(204, 205, 206, 207) ADD1
               MF(Y1, "%[q3]", "%[w3]") RCP_LO MF(ACC0, "%[as]", X0) RCP_HI
               MULY(200, 201, 202, 203, 204, 205, 206, 207) PACK(180, 181, 182, 183)
               :: [q1] "v"(q1), [w1] "v"(w1), [q2] "v"(q2), [w2] "v"(w2), [q3] "v"(q3), [w3] "v"(w3), [as] "v"(as0));
}
DI void beta1(const bf16x8& as1) {
  asm volatile(EXP_LO(208, 209, 210, 211) MF(ACC0, "%[as]", X1) EXP_HI(212, 213, 214, 215) ADD1 :: [as] "v"(as1));
}
DI void beta2(const bf16x8& ax, const bf16x8& bx, const bf16x8& q0, const bf16x8& w0) {
  asm volatile(MF0(Y0, "%[ax]", "%[bx]") RCP_LO MF(Y0, "%[q0]", "%[w0]") RCP_HI
               MULY(208, 209, 210, 211, 212, 213, 214, 215) PACK(176, 177, 178, 179)
               :: [ax] "v"(ax), [bx] "v"(bx), [q0] "v"(q0), [w0] "v"(w0));
}
DI void beta3(const bf16x8& q1, const bf16x8& w1, const bf16x8& q2, const bf16x8& w2, const bf16x8& q3, const bf16x8& w3, const bf16x8& as0) {
  asm volatile(MF(Y0, "%[q1]", "%[w1]") EXP_LO(216, 217, 218, 219) MF(Y0, "%[q2]", "%[w2]") EXP_HI(220, 221, 222, 223) ADD1
               MF(Y0, "%[q3]", "%[w3]") RCP_LO MF(ACC1, "%[as]", X0) RCP_HI
               MULY(216, 217, 218, 219, 220, 221, 222, 223) PACK(180, 181, 182, 183)
               :: [q1] "v"(q1), [w1] "v"(w1), [q2] "v"(q2), [w2] "v"(w2), [q3] "v"(q3), [w3] "v"(w3), [as] "v"(as0));
}
DI void chain_y0(const bf16x8& ax, const bf16x8& bx, const bf16x8& q0, const bf16x8& w0, const bf16x8& q1, const bf16x8& w1,
                 const bf16x8& q2, const bf16x8& w2, const bf16x8& q3, const bf16x8& w3) {
  asm volatile("s_nop 1\n\t" MF0(Y0, "%[ax]", "%[bx]") MF(Y0, "%[q0]", "%[w0]") MF(Y0, "%[q1]", "%[w1]") MF(Y0, "%[q2]", "%[w2]")
               MF(Y0, "%[q3]", "%[w3]") "s_nop 15"
               :: [ax] "v"(ax), [bx] "v"(bx), [q0] "v"(q0), [w0] "v"(w0), [q1] "v"(q1), [w1] "v"(w1), [q2] "v"(q2), [w2] "v"(w2),
                  [q3] "v"(q3), [w3] "v"(w3));
}
DI void last_block(const bf16x8& as1) { asm volatile("s_nop 1\n\t" MF(ACC1, "%[as]", X1) :: [as] "v"(as1)); }
DI void acc_gather(unsigned a0, unsigned a1, unsigned a2, unsigned a3) {
  f32x2 t0, t1, t2, t3, t4, t5, t6, t7;
  asm volatile(
               "ds_read2_b32 %[t0], %[a0] offset0:0 offset1:64\n\t"
               "ds_read2_b32 %[t1], %[a0] offset0:128 offset1:192\n\t"
               "ds_read2_b32 %[t2], %[a0] offset0:32 offset1:96\n\t"
               "ds_read2_b32 %[t3], %[a0] offset0:160 offset1:224\n\t"
               "ds_read2_b32 %[t4], %[a1] offset0:0 offset1:64\n\t"
               "ds_read2_b32 %[t5], %[a1] offset0:128 offset1:192\n\t"
               "ds_read2_b32 %[t6], %[a1] offset0:32 offset1:96\n\t"
               "ds_read2_b32 %[t7], %[a1] offset0:160 offset1:224\n\t"
               "s_waitcnt lgkmcnt(4)\n\t"
               "v_pk_add_f32 v[224:225], v[224:225], %[t0]\n\t"
               "v_pk_add_f32 v[226:227], v[226:227], %[t1]\n\t"
               "v_pk_add_f32 v[240:241], v[240:241], %[t2]\n\t"
               "v_pk_add_f32 v[242:243], v[242:243], %[t3]\n\t"
               "s_waitcnt lgkmcnt(0)\n\t"
               "v_pk_add_f32 v[228:229], v[228:229], %[t4]\n\t"
               "v_pk_add_f32 v[230:231], v[230:231], %[t5]\n\t"
               "v_pk_add_f32 v[244:245], v[244:245], %[t6]\n\t"
               "v_pk_add_f32 v[246:247], v[246:247], %[t7]\n\t"
               "ds_read2_b32 %[t0], %[a2] offset0:0 offset1:64\n\t"
               "ds_read2_b32 %[t1], %[a2] offset0:128 offset1:192\n\t"
               "ds_read2_b32 %[t2], %[a2] offset0:32 offset1:96\n\t"
               "ds_read2_b32 %[t3], %[a2] offset0:160 offset1:224\n\t"
               "ds_read2_b32 %[t4], %[a3] offset0:0 offset1:64\n\t"
               "ds_read2_b32 %[t5], %[a3] offset0:128 offset1:192\n\t"
               "ds_read2_b32 %[t6], %[a3] offset0:32 offset1:96\n\t"
               "ds_read2_b32 %[t7], %[a3] offset0:160 offset1:224\n\t"
               "s_waitcnt lgkmcnt(4)\n\t"
               "v_pk_add_f32 v[232:233], v[232:233], %[t0]\n\t"
               "v_pk_add_f32 v[234:235], v[234:235], %[t1]\n\t"
               "v_pk_add_f32 v[248:249], v[248:249], %[t2]\n\t"
               "v_pk_add_f32 v[250:251], v[250:251], %[t3]\n\t"
               "s_waitcnt lgkmcnt(0)\n\t"
               "v_pk_add_f32 v[236:237], v[236:237], %[t4]\n\t"
               "v_pk_add_f32 v[238:239], v[238:239], %[t5]\n\t"
               "v_pk_add_f32 v[252:253], v[252:253], %[t6]\n\t"
               "v_pk_add_f32 v[254:255], v[254:255], %[t7]\n\t"
               "s_nop 0"
               : [t0] "=&v"(t0), [t1] "=&v"(t1), [t2] "=&v"(t2), [t3] "=&v"(t3), [t4] "=&v"(t4), [t5] "=&v"(t5), [t6] "=&v"(t6), [t7] "=&v"(t7)
               : [a0] "v"(a0), [a1] "v"(a1), [a2] "v"(a2), [a3] "v"(a3) : "memory");
}
DI void acc_store(unsigned a0, unsigned a1, unsigned a2, unsigned a3) {
  asm volatile(
               "ds_write2_b32 %[a0], v224, v225 offset0:0 offset1:64\n\t"
               "ds_write2_b32 %[a0], v226, v227 offset0:128 offset1:192\n\t"
               "ds_write2_b32 %[a0], v240, v241 offset0:32 offset1:96\n\t"
               "ds_write2_b32 %[a0], v242, v243 offset0:160 offset1:224\n\t"
               "ds_write2_b32 %[a1], v228, v229 offset0:0 offset1:64\n\t"
               "ds_write2_b32 %[a1], v230, v231 offset0:128 offset1:192\n\t"
               "ds_write2_b32 %[a1], v244, v245 offset0:32 offset1:96\n\t"
               "ds_write2_b32 %[a1], v246, v247 offset0:160 offset1:224\n\t"
               "ds_write2_b32 %[a2], v232, v233 offset0:0 offset1:64\n\t"
               "ds_write2_b32 %[a2], v234, v235 offset0:128 offset1:192\n\t"
               "ds_write2_b32 %[a2], v248, v249 offset0:32 offset1:96\n\t"
               "ds_write2_b32 %[a2], v250, v251 offset0:160 offset1:224\n\t"
               "ds_write2_b32 %[a3], v236, v237 offset0:0 offset1:64\n\t"
               "ds_write2_b32 %[a3], v238, v239 offset0:128 offset1:192\n\t"
               "ds_write2_b32 %[a3], v252, v253 offset0:32 offset1:96\n\t"
               "ds_write2_b32 %[a3], v254, v255 offset0:160 offset1:224\n\t"
               "s_waitcnt lgkmcnt(0)\n\t"
               "v_mov_b64 v[224:225], 0\n\t"
               "v_mov_b64 v[226:227], 0\n\t"
               "v_mov_b64 v[228:229], 0\n\t"
               "v_mov_b64 v[230:231], 0\n\t"
               "v_mov_b64 v[232:233], 0\n\t"
               "v_mov_b64 v[234:235], 0\n\t"
               "v_mov_b64 v[236:237], 0\n\t"
               "v_mov_b64 v[238:239], 0\n\t"
               "v_mov_b64 v[240:241], 0\n\t"
               "v_mov_b64 v[242:243], 0\n\t"
               "v_mov_b64 v[244:245], 0\n\t"
               "v_mov_b64 v[246:247], 0\n\t"
               "v_mov_b64 v[248:249], 0\n\t"
               "v_mov_b64 v[250:251], 0\n\t"
               "v_mov_b64 v[252:253], 0\n\t"
               "v_mov_b64 v[254:255], 0\n\t"
               "s_nop 7"
               :: [a0] "v"(a0), [a1] "v"(a1), [a2] "v"(a2), [a3] "v"(a3) : "memory");
}
DI void acc_gather_last(unsigned a0, unsigned a1, unsigned a2, unsigned a3) {
  asm volatile(
               "ds_read2_b32 v[176:177], %[a0] offset0:0 offset1:64\n\t"
               "ds_read2_b32 v[178:179], %[a0] offset0:128 offset1:192\n\t"
               "ds_read2_b32 v[180:181], %[a0] offset0:32 offset1:96\n\t"
               "ds_read2_b32 v[182:183], %[a0] offset0:160 offset1:224\n\t"
               "ds_read2_b32 v[184:185], %[a1] offset0:0 offset1:64\n\t"
               "ds_read2_b32 v[186:187], %[a1] offset0:128 offset1:192\n\t"
               "ds_read2_b32 v[188:189], %[a1] offset0:32 offset1:96\n\t"
               "ds_read2_b32 v[190:191], %[a1] offset0:160 offset1:224\n\t"
               "ds_read2_b32 v[192:193], %[a2] offset0:0 offset1:64\n\t"
               "ds_read2_b32 v[194:195], %[a2] offset0:128 offset1:192\n\t"
               "ds_read2_b32 v[196:197], %[a2] offset0:32 offset1:96\n\t"
               "ds_read2_b32 v[198:199], %[a2] offset0:160 offset1:224\n\t"
               "ds_read2_b32 v[200:201], %[a3] offset0:0 offset1:64\n\t"
               "ds_read2_b32 v[202:203], %[a3] offset0:128 offset1:192\n\t"
               "ds_read2_b32 v[204:205], %[a3] offset0:32 offset1:96\n\t"
               "ds_read2_b32 v[206:207], %[a3] offset0:160 offset1:224\n\t"
               "s_waitcnt lgkmcnt(12)\n\t"
               "v_pk_add_f32 v[224:225], v[224:225], v[176:177]\n\t"
               "v_pk_add_f32 v[226:227], v[226:227], v[178:179]\n\t"
               "v_pk_add_f32 v[240:241], v[240:241], v[180:181]\n\t"
               "v_pk_add_f32 v[242:243], v[242:243], v[182:183]\n\t"
               "s_waitcnt lgkmcnt(8)\n\t"
               "v_pk_add_f32 v[228:229], v[228:229], v[184:185]\n\t"
               "v_pk_add_f32 v[230:231], v[230:231], v[186:187]\n\t"
               "v_pk_add_f32 v[244:245], v[244:245], v[188:189]\n\t"
               "v_pk_add_f32 v[246:247], v[246:247], v[190:191]\n\t"
               "s_waitcnt lgkmcnt(4)\n\t"
               "v_pk_add_f32 v[232:233], v[232:233], v[192:193]\n\t"
               "v_pk_add_f32 v[234:235], v[234:235], v[194:195]\n\t"
               "v_pk_add_f32 v[248:249], v[248:249], v[196:197]\n\t"
               "v_pk_add_f32 v[250:251], v[250:251], v[198:199]\n\t"
               "s_waitcnt lgkmcnt(0)\n\t"
               "v_pk_add_f32 v[236:237], v[236:237], v[200:201]\n\t"
               "v_pk_add_f32 v[238:239], v[238:239], v[202:203]\n\t"
               "v_pk_add_f32 v[252:253], v[252:253], v[204:205]\n\t"
               "v_pk_add_f32 v[254:255], v[254:255], v[206:207]\n\t"
               "s_nop 0"
               :: [a0] "v"(a0), [a1] "v"(a1), [a2] "v"(a2), [a3] "v"(a3) : "memory");
}
DI void acc_store_last(unsigned a0, unsigned a1, unsigned a2, unsigned a3) {
  asm volatile(
               "ds_write2_b32 %[a0], v224, v225 offset0:0 offset1:64\n\t"
               "ds_write2_b32 %[a0], v226, v227 offset0:128 offset1:192\n\t"
               "ds_write2_b32 %[a0], v240, v241 offset0:32 offset1:96\n\t"
               "ds_write2_b32 %[a0], v242, v243 offset0:160 offset1:224\n\t"
               "ds_write2_b32 %[a1], v228, v229 offset0:0 offset1:64\n\t"
               "ds_write2_b32 %[a1], v230, v231 offset0:128 offset1:192\n\t"
               "ds_write2_b32 %[a1], v244, v245 offset0:32 offset1:96\n\t"
               "ds_write2_b32 %[a1], v246, v247 offset0:160 offset1:224\n\t"
               "ds_write2_b32 %[a2], v232, v233 offset0:0 offset1:64\n\t"
               "ds_write2_b32 %[a2], v234, v235 offset0:128 offset1:192\n\t"
               "ds_write2_b32 %[a2], v248, v249 offset0:32 offset1:96\n\t"
               "ds_write2_b32 %[a2], v250, v251 offset0:160 offset1:224\n\t"
               "ds_write2_b32 %[a3], v236, v237 offset0:0 offset1:64\n\t"
               "ds_write2_b32 %[a3], v238, v239 offset0:128 offset1:192\n\t"
               "ds_write2_b32 %[a3], v252, v253 offset0:32 offset1:96\n\t"
               "ds_write2_b32 %[a3], v254, v255 offset0:160 offset1:224\n\t"
               "s_nop 0"
               :: [a0] "v"(a0), [a1] "v"(a1), [a2] "v"(a2), [a3] "v"(a3) : "memory");
}
DI unsigned lds_lock(unsigned addr) {
  unsigned t, old;
  unsigned long long sv;
  asm volatile("s_mov_b64 %2, exec\n\ts_mov_b64 exec, 1\n"
               "1:\n\tds_wrxchg_rtn_b32 %0, %3, %4\n\ts_waitcnt lgkmcnt(0)\n\tv_cmp_eq_u32 vcc, 1, %0\n\ts_cbranch_vccz 2f\n\t"
               "s_sleep 1\n\ts_branch 1b\n"
               "2:\n\tv_readfirstlane_b32 %1, %0\n\ts_mov_b64 exec, %2\n\ts_nop 15"
               : "=&v"(t), "=&s"(old), "=&s"(sv) : "v"(addr), "v"(1u) : "vcc", "memory");
  return old;
}

template <typename T> DI T* karg(int k) {
  typedef __attribute__((address_space(4))) const unsigned long long* kp_t;
  kp_t kp = (kp_t)__builtin_amdgcn_kernarg_segment_ptr();
  asm volatile("" : "+s"(kp));
  return (T*)(__attribute__((address_space(1))) T*)kp[k];
}

__global__ __launch_bounds__(512) __attribute__((amdgpu_num_vgpr(83))) void egnn_kernel(
    const u16* __restrict__ h_, const u16* __restrict__ pos_, const u16* __restrict__ we_, const u16* __restrict__ wx_,
    const u16* __restrict__ wh_, char* __restrict__ ws_, u16* __restrict__ out_, const int* __restrict__ src_,
    const int* __restrict__ dst_, const u16* __restrict__ ea_) {
  __shared__ __attribute__((aligned(1024))) char lds[L_END];
  AS3 char* L = (AS3 char*)lds;
  const unsigned LB = (unsigned)(size_t)L;
  const int tid_ = threadIdx.x;
  const int w = __builtin_amdgcn_readfirstlane(tid_ >> 6);
  const int xl = blockIdx.x & 7;
  const int b = xl * 31 + (xl < 2 ? xl : 2) + (blockIdx.x >> 3);
  const int n0 = b * BN;
  unsigned cw[2][2];

  {
    const int tid = tid_, lane = tid & 63, r = lane & 31, hf = lane >> 5, p = blockIdx.x;
    const rsrc_t rs = ws_rsrc(ws_);
    const int e0 = p * CH + 4 * tid, e1 = p * CH + 2048 + tid;
    const i32x4 d4 = *(const i32x4*)(dst_ + e0), s4 = *(const i32x4*)(src_ + e0);
    const u32x2 a2 = *(const u32x2*)(ea_ + e0);
    unsigned dd[5] = {(unsigned)d4[0], (unsigned)d4[1], (unsigned)d4[2], (unsigned)d4[3], (unsigned)dst_[e1]};
    const unsigned s5 = src_[e1], a5 = ea_[e1];
    const u32x2 ctl = ld8_dev(rs, WS_CTL);
    SB();
    dma_piece((const char*)we_ + w * 1024, lane * 16, LB + L_W + w * 1024);
    if (w < 5) dma_piece((const char*)h_ + (size_t)n0 * 128 + w * 1024, lane * 16, LB + L_H + w * 1024);
    const u32x4 w2raw = *(const u32x4*)(we_ + 64 * 64 + tid * 8);
    unsigned sraw[3], craw[4];
#pragma unroll
    for (int t = 0; t < 2; ++t) { craw[2 * t] = we_[128 * 64 + 32 * t + r]; craw[2 * t + 1] = we_[129 * 64 + 32 * t + r]; }
    {
      const u16* pp = w == 1 ? wx_ + lane : pos_ + (lane < BN ? n0 + lane : 0) * 3;
      const int st = w == 1 ? 0 : 1;
      sraw[0] = pp[0]; sraw[1] = pp[st]; sraw[2] = pp[2 * st];
    }
    SB();
    {
      const u32x4 z4 = {0, 0, 0, 0};
      static_assert(8 * S_WAVE == 2 * 8192 + 256 * 16 && BN * ACC_NODE == 5 * 8192 + 40 * 16, "clearing pattern");
      if (tid < 256) lds_st<u32x4>(L, L_TBL + 16 * tid, z4);
      lds_st<u32x4>(L, L_S + tid * 16, z4);
      lds_st<u32x4>(L, L_S + 8192 + tid * 16, z4);
#pragma unroll
      for (int k = 0; k < 5; ++k) lds_st<u32x4>(L, L_ACC + k * 8192 + tid * 16, z4);
      if (tid < 256) lds_st<u32x4>(L, L_S + 16384 + tid * 16, z4);
      if (tid < 40) lds_st<u32x4>(L, L_ACC + 5 * 8192 + tid * 16, z4);
      if (tid < 8) lds_st<unsigned>(L, L_LOCK + 4 * tid, 0u);
      if (tid == 8) lds_st<unsigned>(L, L_WT, 0u);
    }
    __syncthreads();
    unsigned idx[5], rank[5], bk[5];
#pragma unroll
    for (int i = 0; i < 5; ++i) {
      const unsigned G = dd[i] >> 3;
      bk[i] = (G * 52429u) >> 18;
      idx[i] = G + 3 * bk[i];
      const unsigned sh = (idx[i] & 1) << 4;
      const unsigned old = __hip_atomic_fetch_add((AS3 unsigned*)(L + L_TBL + 4 * (idx[i] >> 1)), 1u << sh, __ATOMIC_RELAXED,
                                                  __HIP_MEMORY_SCOPE_WORKGROUP);
      rank[i] = (old >> sh) & 0xffffu;
    }
    __syncthreads();
    const unsigned tlo = ctl[0] + 1, thi = ctl[1] + (tlo == 0);
    const unsigned tag0 = tlo & 0xfffffffu, tag1 = ((tag0 * 2654435761u) ^ ((tlo >> 28) | (thi << 4))) & 0xfffffffu;
    u32x4 entry = {0, 0, 0, 0};
    if (w < 4) {
      const u32x4 c = lds_ld<u32x4>(L, L_TBL + 16 * tid);
      const unsigned tot = (c[0] & 0xffffu) + (c[0] >> 16) + (c[1] & 0xffffu) + (c[1] >> 16) + (c[2] & 0xffffu);
      const unsigned incl = wave_incl_scan(tot);
      unsigned old;
      unsigned long long sv;
      asm volatile("s_mov_b64 %1, exec\n\ts_mov_b64 exec, %4\n\tds_add_rtn_u32 %0, %2, %3\n\ts_mov_b64 exec, %1\n\ts_waitcnt lgkmcnt(0)"
                   : "=&v"(old), "=&s"(sv) : "v"(LB + L_WT), "v"(incl), "s"(1ull << 63) : "memory");
      const unsigned s0 = incl - tot + __builtin_amdgcn_readlane(old, 63);
      const unsigned s1 = s0 + (c[0] & 0xffffu), s2 = s1 + (c[0] >> 16), s3 = s2 + (c[1] & 0xffffu), s4_ = s3 + (c[1] >> 16);
      const unsigned s5_ = s0 + tot;
      lds_st<u32x3>(L, L_TBL + 16 * tid, u32x3{s0 | (s1 << 16), s2 | (s3 << 16), s4_});
      entry = u32x4{s0 | (s1 << 12) | (s2 << 24), (s2 >> 8) | (tag0 << 4), s3 | (s4_ << 12) | (s5_ << 24), (s5_ >> 8) | (tag1 << 4)};
    }
    if (tid == 0) {
      lds_st<u32x4>(L, L_TAG, u32x4{tag0, tag1, tlo, thi});
      lds_st<u32x4>(L, L_TOT, u32x4{0, 0, 0, 0});
      lds_st<u32x4>(L, L_TOT + 16, u32x4{0, 0, 0, 0});
    }
    {
      u32x4 o;
#pragma unroll
      for (int k = 0; k < 4; ++k) o[k] = cvt_pk(bf_lo(w2raw[k]) * NLOG2E, bf_hi(w2raw[k]) * NLOG2E);
      lds_st<u32x4>(L, L_W + 8192 + tid * 16, o);
    }
    wait_vm0();
    __syncthreads();
    {
      unsigned ps[5];
#pragma unroll
      for (int i = 0; i < 5; ++i) ps[i] = lds_ld<u16>(L, L_TBL + 2 * idx[i]) + rank[i];
#pragma unroll
      for (int i = 0; i < 5; ++i) {
        const unsigned dl = dd[i] - BN * bk[i];
        const unsigned sv = i < 4 ? (unsigned)s4[i] : s5;
        const unsigned eav = i == 4 ? a5 : (i & 1) ? (a2[i >> 1] >> 16) : a2[i >> 1];
        const unsigned pb = L_RAW + 12 * (ps[i] >> 1), odd = ps[i] & 1;
        lds_st<unsigned>(L, pb + 4 * odd, sv | ((L_PD + 16 * dl) << 16));
        lds_st<u16>(L, pb + 8 + 2 * odd, (u16)eav);
      }
    }
    __syncthreads();
    {
      static_assert(REC_STRIDE == (512 + 448) * 16, "copy pattern");
      const unsigned o = WS_RECS + p * REC_STRIDE + 16 * tid;
      st16_dev(rs, o, lds_ld<u32x4>(L, L_RAW + 16 * tid));
      if (tid < 448) st16_dev(rs, o + 8192, lds_ld<u32x4>(L, L_RAW + 8192 + 16 * tid));
    }
    SB();
    if (w >= 4) {
      const int u = (w - 4) >> 1, t = w & 1;
      f32x16 pacc;
#pragma unroll
      for (int i = 0; i < 16; ++i) pacc[i] = 0.f;
#pragma unroll
      for (int s = 0; s < 4; ++s) {
        const int nl = 32 * u + r, nlc = nl < BN ? nl : BN - 1;
        const bf16x8 ha = lds_ld<bf16x8>(L, L_H + nlc * 128 + 32 * s + 16 * hf);
        const unsigned o = L_W + (16 * s + 8 * (lane >> 5) + ((lane & 15) >> 2)) * 128 + (32 * t + 16 * ((lane >> 4) & 1) + 4 * (lane & 3)) * 2;
        const u32x2 b0 = lds_tr(L, o), b1 = lds_tr(L, o + 512);
        pacc = mfma(ha, as_frag(u32x4{b0[0], b0[1], b1[0], b1[1]}), pacc);
      }
#pragma unroll
      for (int q = 0; q < 4; ++q)
        if (4 * u + q < 5)
          lds_st<u32x2>(L, L_PFR + ((4 * u + q) * 2 + t) * 512 + lane * 8,
                        u32x2{cvt_pk(pacc[4 * q] * NLOG2E, pacc[4 * q + 1] * NLOG2E), cvt_pk(pacc[4 * q + 2] * NLOG2E, pacc[4 * q + 3] * NLOG2E)});
    } else if (w == 0) {
      if (lane <= BN)
        lds_st<f32x4>(L, L_PD + 16 * lane, f32x4{bf_lo(sraw[0]), bf_lo(sraw[1]), bf_lo(sraw[2]), lane < BN ? 1.0f : 0.0f});
    } else if (w == 1) {
      lds_st<float>(L, L_WX + 4 * lane, bf_lo(sraw[0]));
    }
#pragma unroll
    for (int t = 0; t < 2; ++t) {
      const float d = bf_lo(craw[2 * t]) * NLOG2E, a = bf_lo(craw[2 * t + 1]) * NLOG2E;
      const unsigned hi = cvt_pk(d, a);
      cw[t][0] = hi;
      cw[t][1] = cvt_pk(d - bf_lo(hi), a - bf_hi(hi));
    }
    wait_vm0();
    __syncthreads();
    if (tid < NBK) st16_dev(rs, WS_ENT + p * 4096 + tid * 16, entry);
  }

  int pa = 0, np = NP;
  for (unsigned it = 0;; ++it) {
    const u16 *h = karg<const u16>(0), *pos = karg<const u16>(1);
    const char* ws = karg<const char>(5);
    const rsrc_t rs = ws_rsrc(ws);
    int tid = tid_;
    asm volatile("" : "+v"(tid));
    const int lane = tid & 63, r = lane & 31, hf = lane >> 5;
    const unsigned l_tot = L_TOT + 16 * (it & 1);
    unsigned pA = 0, pB = 0, pC = 0, d1 = 0, d2 = 0, d3 = 0, d4 = 0;
    if (w < 4) {
      const u32x2 tg = lds_ld<u32x2>(L, L_TAG);
      const unsigned eo = WS_ENT + (tid < NP ? tid : 0) * 4096 + b * 16;
      auto seen = [&](const u32x4& e) { return __all(tid >= NP || ((e[1] >> 4) == tg[0] && (e[3] >> 4) == tg[1])); };
      auto poll = [&]() {
        __builtin_amdgcn_s_sleep(POLL_SLEEP);
        asm volatile("" ::: "memory");
        return ld16_dev(rs, eo);
      };
      u32x4 ent, e0 = ld16_dev(rs, eo);
      for (unsigned spins = 0;; ++spins) {
        const u32x4 e1 = poll();
        if (seen(e0)) { ent = e0; break; }
        e0 = poll();
        ent = e1;
        if (seen(e1)) break;
        if (spins > SPIN_LIMIT) {
          __hip_atomic_fetch_or((AS3 unsigned*)(L + l_tot + 12), 1u, __ATOMIC_RELAXED, __HIP_MEMORY_SCOPE_WORKGROUP);
          break;
        }
      }
      const unsigned s0 = ent[0] & 0xfffu, s1 = (ent[0] >> 12) & 0xfffu, s2 = __builtin_amdgcn_alignbit(ent[1], ent[0], 24) & 0xfffu;
      const unsigned s3 = ent[2] & 0xfffu, s4 = (ent[2] >> 12) & 0xfffu, s5 = __builtin_amdgcn_alignbit(ent[3], ent[2], 24) & 0xfffu;
      const bool act = (unsigned)(tid - pa) < (unsigned)np && tid < NP;
      d1 = s1 - s0; d2 = s2 - s0; d3 = s3 - s0; d4 = s4 - s0;
      const unsigned cnt = act ? s5 - s0 : 0;
      const unsigned cA = act ? d1 | ((s2 - s1) << 16) : 0, cB = act ? (s3 - s2) | ((s4 - s3) << 16) : 0, cC = act ? s5 - s4 : 0;
      lds_st<unsigned>(L, L_SEG + 4 * tid, (tid * (CH / 2) + (s0 >> 1)) | ((cnt + (s0 & 1)) << 19) | (s0 << 31));
      const unsigned iA = wave_incl_scan(cA), iB = wave_incl_scan(cB), iC = wave_incl_scan(cC);
      if (lane == 63) {
        lds_st<u32x3>(L, L_WV + 16 * w, u32x3{iA, iB, iC});
        __hip_atomic_fetch_add((AS3 unsigned*)(L + l_tot), iA, __ATOMIC_RELAXED, __HIP_MEMORY_SCOPE_WORKGROUP);
        __hip_atomic_fetch_add((AS3 unsigned*)(L + l_tot + 4), iB, __ATOMIC_RELAXED, __HIP_MEMORY_SCOPE_WORKGROUP);
        __hip_atomic_fetch_add((AS3 unsigned*)(L + l_tot + 8), iC, __ATOMIC_RELAXED, __HIP_MEMORY_SCOPE_WORKGROUP);
      }
      if (__any(cnt > 255))
        __hip_atomic_fetch_or((AS3 unsigned*)(L + l_tot + 12), 2u, __ATOMIC_RELAXED, __HIP_MEMORY_SCOPE_WORKGROUP);
      pA = iA - cA;
      pB = iB - cB;
      pC = iC - cC;
    }
    __syncthreads();
    const u32x4 tot4 = lds_ld<u32x4>(L, l_tot);
    unsigned e3[4];
#pragma unroll
    for (int k = 0; k < 4; ++k) e3[k] = lds_ld<unsigned>(L, L_SEG + 4 * (64 * k + (tid >> 3)));
    const unsigned flags = __builtin_amdgcn_readfirstlane(tot4[3]);
    if (flags & 1) return;
    if (tid == 511) lds_st<u32x4>(L, L_TOT + 16 * (~it & 1), u32x4{0, 0, 0, 0});
    const unsigned j2 = 2 * (tid & 7);
    u32x3 ra[4], rb[4];
    unsigned cntp[4], ro[4];
#pragma unroll
    for (int k = 0; k < 4; ++k) {
      cntp[k] = (e3[k] >> 19) & 0xfffu;
      ro[k] = ((e3[k] & 0x7ffffu) + (tid & 7)) * 12 + WS_RECS;
      ra[k] = ld12_rec(rs, j2 < cntp[k] ? ro[k] : NO_DATA);
    }
#pragma unroll
    for (int k = 0; k < 4; ++k) rb[k] = ld12_rec(rs, j2 + 16 < cntp[k] ? ro[k] + 96 : NO_DATA);
    SB();
    const unsigned tA = __builtin_amdgcn_readfirstlane(tot4[0]), tB = __builtin_amdgcn_readfirstlane(tot4[1]);
    const unsigned tot[5] = {tA & 0xffffu, tA >> 16, tB & 0xffffu, tB >> 16, (unsigned)__builtin_amdgcn_readfirstlane(tot4[2])};
    unsigned base[6];
    base[0] = 0;
#pragma unroll
    for (int g = 0; g < 5; ++g) base[g + 1] = base[g] + ((tot[g] + 31) & ~31u);
    if (((flags & 2) && np > 16) || base[5] > (unsigned)CAP) {
      np = (np + 1) >> 1;
      __syncthreads();
      continue;
    }
    if (w < 4) {
      constexpr unsigned R8 = L_RAW / 8;
#pragma unroll
      for (int v = 0; v < 3; ++v) {
        const u32x3 t3 = lds_ld<u32x3>(L, L_WV + 16 * v);
        if (v < w) { pA += t3[0]; pB += t3[1]; pC += t3[2]; }
      }
      const unsigned v0 = ((pA & 0xffffu) + (base[0] + R8)) << 3, v1 = ((pA >> 16) - d1 + (base[1] + R8)) << 3;
      const unsigned v2 = ((pB & 0xffffu) - d2 + (base[2] + R8)) << 3, v3 = ((pB >> 16) - d3 + (base[3] + R8)) << 3;
      const unsigned v4 = (pC - d4 + (base[4] + R8)) << 3;
      lds_st<u32x3>(L, L_TBL + 16 * tid, u32x3{(v0 & 0xffffu) | (v1 << 16), (v2 & 0xffffu) | (v3 << 16), v4});
    } else {
      const u32x2 pad = {(unsigned)(L_PD + 16 * BN) << 16, 0};
      const unsigned lo0 = w == 4 ? base[0] + tot[0] : w == 5 ? base[2] + tot[2] : base[4] + tot[4];
      const unsigned lo1 = w == 4 ? base[1] : w == 5 ? base[3] : base[5];
      const unsigned hi0 = w == 4 ? base[1] + tot[1] : base[3] + tot[3], hi1 = w == 4 ? base[2] : base[4];
      const unsigned slot = (hf ? hi0 : lo0) + r;
      if (w < 7 && slot < (hf ? (w == 6 ? 0u : hi1) : lo1)) lds_st<u32x2>(L, L_RAW + 8 * slot, pad);
      if (tid - 256 < PADS) lds_st<u32x2>(L, L_RAW + 8 * (base[5] + tid - 256), pad);
    }

    const int T = base[5] >> 5;
    const int q4 = T >> 2, r4 = T & 3, ws4 = w & 3;
    const int sa4 = ws4 * q4 + (ws4 < r4 ? ws4 : r4), sn4 = q4 + (ws4 < r4 ? 1 : 0);
    const int nold = (sn4 * 44 + 32) >> 6;
    const int ta = w < 4 ? sa4 : sa4 + nold;
    const int tb = w < 4 ? sa4 + nold : sa4 + sn4;
    const int tg1 = base[1] >> 5, tg2 = base[2] >> 5, tg3 = base[3] >> 5, tg4 = base[4] >> 5;

    const char* pos6 = (const char*)pos;
    const rsrc_t h_rows = __builtin_amdgcn_make_buffer_rsrc((void*)((const char*)h - 3072), 128, 0xffffffffu, 0x00020000);
    const unsigned ringb = L_RING + w * 4096;
    unsigned aq[4];
#pragma unroll
    for (int s = 0; s < 4; ++s) aq[s] = ringb + r * 128 + (((2 * s + hf) ^ ((r >> 1) & 7)) << 4);
    const unsigned dc0 = ((lane & 7) ^ ((lane >> 4) & 7)) << 4, dc1 = ((lane & 7) ^ ((4 + (lane >> 4)) & 7)) << 4;
    const unsigned Sb = L_S + w * S_WAVE;
    const unsigned pi_e = (r & 0x13) | ((r & 4) << 1) | ((r & 8) >> 1);
    const unsigned sa_c = Sb + 2 * pi_e;
    const unsigned s_rd = Sb + r * 80 + 16 * hf;
    const int g16 = lane >> 4, tq = (lane & 15) >> 2, tp = lane & 3, tu = g16 & 1, th = g16 >> 1;
    const unsigned s_tr = Sb + (4 * (4 * th + tq) + 3) * 80 + 2 * (16 * tu + 8 * (tp & 1) + 4 * (tp >> 1));
    const unsigned acc_l = L_ACC + hf * ACC_NODE + 4 * r;
    bf16x8 wq[2][4];
    auto group_of = [&](int t) {
      int g;
      asm("s_cmp_ge_i32 %1, %2\n\ts_cselect_b32 %0, 1, 0\n\ts_cmp_ge_i32 %1, %3\n\ts_addc_u32 %0, %0, 0\n\t"
          "s_cmp_ge_i32 %1, %4\n\ts_addc_u32 %0, %0, 0\n\ts_cmp_ge_i32 %1, %5\n\ts_addc_u32 %0, %0, 0"
          : "=&s"(g) : "s"(t), "s"(tg1), "s"(tg2), "s"(tg3), "s"(tg4) : "scc");
      return g;
    };
    auto group_end = [&](int t) {
      int e;
      asm("s_cmp_gt_i32 %5, %1\n\ts_cselect_b32 %0, %5, %6\n\ts_cmp_gt_i32 %4, %1\n\ts_cselect_b32 %0, %4, %0\n\t"
          "s_cmp_gt_i32 %3, %1\n\ts_cselect_b32 %0, %3, %0\n\ts_cmp_gt_i32 %2, %1\n\ts_cselect_b32 %0, %2, %0"
          : "=&s"(e) : "s"(t), "s"(tg1), "s"(tg2), "s"(tg3), "s"(tg4), "s"(T) : "scc");
      return e;
    };
    int gend = group_end(ta);
    int tflush = -1;

    bf16x8 bxA0, bxA1, bxB0, bxB1;
    auto load_bx = [&](int gg) {
      const u32x2 p0 = lds_ld<u32x2>(L, L_PFR + gg * 1024 + lane * 8), p1 = lds_ld<u32x2>(L, L_PFR + gg * 1024 + 512 + lane * 8);
      bxA0 = as_frag(u32x4{p0[0], p0[1], hf ? 0u : cw[0][0], hf ? 0u : cw[0][1]});
      bxB0 = as_frag(u32x4{p0[0], p0[1], hf ? cw[0][0] : 0u, hf ? cw[0][1] : 0u});
      bxA1 = as_frag(u32x4{p1[0], p1[1], hf ? 0u : cw[1][0], hf ? 0u : cw[1][1]});
      bxB1 = as_frag(u32x4{p1[0], p1[1], hf ? cw[1][0] : 0u, hf ? cw[1][1] : 0u});
      asm volatile("" : "+v"(bxA0), "+v"(bxB0), "+v"(bxA1), "+v"(bxB1));
    };
#pragma unroll
    for (int t = 0; t < 2; ++t)
#pragma unroll
      for (int s = 0; s < 4; ++s) {
        const unsigned o = L_W + 8192 + (16 * s + 8 * th + tq) * 128 + (32 * t + 16 * tu + 4 * tp) * 2;
        const u32x2 b0 = lds_tr(L, o), b1 = lds_tr(L, o + 512);
        wq[t][s] = as_frag(u32x4{b0[0], b0[1], b1[0], b1[1]});
      }
    load_bx(group_of(ta));
    tiles_init();
    asm volatile("v_mov_b32 v167, %0\n\tv_mov_b32 v169, %1\n\tv_mov_b32 v171, %0\n\tv_mov_b32 v173, %1" :: "v"(dc0), "v"(dc1));
    set_m0(LB + ringb);
    __syncthreads();

    {
      auto second = [](unsigned w0, unsigned eahi) { return (((w0 >> 20) & 7) * 320) | eahi; };
      const unsigned tb0 = L_TBL + 16 * (tid >> 3);
      unsigned a0[4], a1[4], r8[4];
#pragma unroll
      for (int k = 0; k < 4; ++k) {
        r8[k] = 8 * j2 - ((e3[k] >> 31) << 3);
        a0[k] = lds_ld<u16>(L, tb0 + 1024 * k + ((ra[k][0] >> 22) & 14)) + r8[k];
        a1[k] = lds_ld<u16>(L, tb0 + 1024 * k + ((ra[k][1] >> 22) & 14)) + r8[k];
      }
#pragma unroll
      for (int k = 0; k < 4; ++k) {
        if (j2 < cntp[k] && (int)r8[k] >= 0) lds_st<u32x2>(L, a0[k], u32x2{ra[k][0], second(ra[k][0], ra[k][2] << 16)});
        if (j2 + 1 < cntp[k]) lds_st<u32x2>(L, a1[k] + 8, u32x2{ra[k][1], second(ra[k][1], ra[k][2] & 0xffff0000u)});
      }
#pragma unroll
      for (int k = 0; k < 4; ++k) {
        if (j2 + 16 < cntp[k]) {
          const unsigned b0 = lds_ld<u16>(L, tb0 + 1024 * k + ((rb[k][0] >> 22) & 14)) + r8[k];
          const unsigned b1 = lds_ld<u16>(L, tb0 + 1024 * k + ((rb[k][1] >> 22) & 14)) + r8[k];
          lds_st<u32x2>(L, b0 + 128, u32x2{rb[k][0], second(rb[k][0], rb[k][2] << 16)});
          if (j2 + 17 < cntp[k]) lds_st<u32x2>(L, b1 + 136, u32x2{rb[k][1], second(rb[k][1], rb[k][2] & 0xffff0000u)});
        }
      }
#pragma unroll
      for (int k = 0; k < 4; ++k) {
        const unsigned odd = e3[k] >> 31;
        for (unsigned q = (tid & 7) + 32; q < cntp[k]; q += 8) {
          const unsigned pb = ((e3[k] & 0x7ffffu) + (q >> 1)) * 12 + WS_RECS;
          const unsigned w0 = ld4_rec(rs, pb + 4 * (q & 1)), eav = ld2_rec(rs, pb + 8 + 2 * (q & 1));
          const unsigned a = lds_ld<u16>(L, tb0 + 1024 * k + ((w0 >> 22) & 14));
          lds_st<u32x2>(L, a + 8 * (q - odd), u32x2{w0, second(w0, eav << 16)});
        }
      }
    }
    __syncthreads();

    if (pa + np >= NP) {
      const char* wh = karg<const char>(4);
      dma_piece(wh + w * 2048, lane * 16, LB + L_W + w * 2048);
      dma_piece(wh + w * 2048 + 1024, lane * 16, LB + L_W + w * 2048 + 1024);
    }
    if (ta < tb) {
      auto flush = [&](int gg) {
        const unsigned a = LB + acc_l + gg * (8 * ACC_NODE);
        if (lds_lock(LB + L_LOCK + 4 * gg) != 0) acc_gather(a, a + 2 * ACC_NODE, a + 4 * ACC_NODE, a + 6 * ACC_NODE);
        acc_store(a, a + 2 * ACC_NODE, a + 4 * ACC_NODE, a + 6 * ACC_NODE);
        lds_st<unsigned>(L, L_LOCK + 4 * gg, 2u);
        asm volatile("" ::: "memory");
      };
      auto flush_last = [&](int gg) {
        const unsigned a = LB + acc_l + gg * (8 * ACC_NODE);
        if (lds_lock(LB + L_LOCK + 4 * gg) != 0) acc_gather_last(a, a + 2 * ACC_NODE, a + 4 * ACC_NODE, a + 6 * ACC_NODE);
        acc_store_last(a, a + 2 * ACC_NODE, a + 4 * ACC_NODE, a + 6 * ACC_NODE);
        lds_st<unsigned>(L, L_LOCK + 4 * gg, 2u);
        asm volatile("" ::: "memory");
      };
      unsigned a_idx = LB + L_RAW + ta * 256 + (lane >> 3) * 8, a_raw = L_RAW + ta * 256 + lane * 8;
      auto raw_load = [&](int dt) {
        const unsigned a = a_raw + dt * 256;
        return Raw{lds_ld<u16>(L, a), lds_ld<u16>(L, a + 2), lds_ld<unsigned>(L, a + 4), lds_ld<u16>(L, a + 4)};
      };
      auto pos_load = [&](const Raw& x) {
        asm volatile("global_load_dwordx2 v[174:175], %0, %1" :: "v"(x.src * 6u), "s"(pos6) : "memory");
      };
      auto pd_load = [&](const Raw& x) { return lds_ld<f32x4>(L, x.pda); };
      auto prep = [&](const Raw& x, const f32x4& pd) {
        float xs, ys, zs;
        asm volatile("v_lshlrev_b32 %0, 16, v174\n\tv_and_b32 %1, 0xffff0000, v174\n\tv_lshlrev_b32 %2, 16, v175"
                     : "=v"(xs), "=v"(ys), "=v"(zs));
        const f32x2 dxy = f32x2{pd[0], pd[1]} - f32x2{xs, ys};
        unsigned wa = cvt_pk(dxy[0], dxy[1]), wb = cvt_pk(pd[2] - zs, pd[3]);
        const float dzr = bf_lo(wb);
        const bf16x2v wav = __builtin_bit_cast(bf16x2v, wa);
        const float d2 = __builtin_amdgcn_fdot2_f32_bf16(wav, wav, dzr * dzr, false);
        Prep o;
        o.de = cvt_pk(d2, as_f(x.w1));
        asm volatile("" : "+v"(o.de), "+v"(wa), "+v"(wb));
        const unsigned P = x.n320 + sa_c, Q = P + 160;
        const auto sa = __builtin_amdgcn_permlane32_swap(P, Q, false, false);
        const auto sd = __builtin_amdgcn_permlane32_swap(wa, wb, false, false);
        o.saA = sa[0]; o.saB = sa[1]; o.sdA = sd[0]; o.sdB = sd[1];
        return o;
      };

      struct Ops { bf16x8 q0, q1, q2, q3, as0, as1, ax; };
      auto read_ops = [&](unsigned sa, unsigned sd, unsigned de) {
        Ops o;
        o.q0 = lds_ld<bf16x8>(L, aq[0]); o.q1 = lds_ld<bf16x8>(L, aq[1]);
        o.q2 = lds_ld<bf16x8>(L, aq[2]); o.q3 = lds_ld<bf16x8>(L, aq[3]);
        lds_st<u16>(L, sa, (u16)sd);
        lds_st<u16>(L, sa + 80, (u16)(sd >> 16));
        const u32x2 oh = lds_tr(L, s_tr);
        o.as0 = lds_ld<bf16x8>(L, s_rd);
        o.as1 = lds_ld<bf16x8>(L, s_rd + 32);
        lds_st<u16>(L, sa, (u16)0);
        lds_st<u16>(L, sa + 80, (u16)0);
        unsigned de2;
        asm volatile("v_mov_b32 %0, %1" : "=v"(de2) : "v"(de));
        o.ax = as_frag(u32x4{oh[0], oh[1], de, de2});
        return o;
      };
      auto alpha = [&](int t, const Ops& c, const bf16x8& asl, const bf16x8& bx1, auto&& mid) {
        alpha1(asl);
        if (__builtin_expect(t == tflush, 0)) flush(group_of(t - 1));
        alpha2(c.ax, bx1, c.q0, wq[1][0]);
        SB(); mid(); SB();
        alpha3(c.q1, wq[1][1], c.q2, wq[1][2], c.q3, wq[1][3], c.as0);
      };
      auto beta = [&](const Ops& c, const Ops& n, const bf16x8& bx0n, auto&& mid) {
        beta1(c.as1);
        beta2(n.ax, bx0n, n.q0, wq[0][0]);
        SB(); mid(); SB();
        beta3(n.q1, wq[0][1], n.q2, wq[0][2], n.q3, wq[0][3], c.as0);
      };
      auto next_group = [&](int t) {
        if (__builtin_expect(t == gend, 0)) { gend = group_end(t); tflush = t; load_bx(group_of(t)); }
      };

      rows_index<0>(a_idx);
      rows_gather(h_rows);
      Raw rw = raw_load(0);
      pos_load(rw);
      f32x4 pd = pd_load(rw);
      SB();
      wait_vm0();
      Prep pr = prep(rw, pd);
      rw = raw_load(2);
      Ops A = read_ops(pr.saA, pr.sdA, pr.de), B;
#pragma unroll
      for (int j = 0; j < 8; ++j) B.as1[j] = 0;
      asm volatile("" : "+v"(B.as1));
      const int glast = group_of(tb - 1);
      rows_index<256>(a_idx);
      chain_y0(A.ax, bxA0, A.q0, wq[0][0], A.q1, wq[0][1], A.q2, wq[0][2], A.q3, wq[0][3]);
      rows_gather(h_rows);
      int t = ta;
      auto tile_a = [&]() {
        alpha(t, A, B.as1, bxA1, [&]() { pos_load(rw); });
        wait_vm0();
        next_group(t + 1);
        rows_index<512>(a_idx);
        B = read_ops(pr.saB, pr.sdB, pr.de);
        pd = pd_load(rw);
        SB();
        beta(A, B, bxB0, [&]() { rows_gather(h_rows); });
      };
      for (; t + 2 <= tb; t += 2) {
        tile_a();
        alpha(t + 1, B, A.as1, bxB1, [&]() { pr = prep(rw, pd); });
        wait_vm0();
        next_group(t + 2);
        rows_index<768>(a_idx);
        A = read_ops(pr.saA, pr.sdA, pr.de);
        rw = raw_load(4);
        SB();
        beta(B, A, bxA0, [&]() { rows_gather(h_rows); });
        a_idx += 512;
        a_raw += 512;
      }
      if (t < tb) {
        tile_a();
        last_block(A.as1);
      } else {
        last_block(B.as1);
      }
      flush_last(glast);
    } else {
      wait_vm0();
    }
    pa += np;
    if (pa >= NP) break;
    wait_vm0();
    __syncthreads();
  }
  __syncthreads();

  u16* out = karg<u16>(6);
  int tid = tid_;
  asm volatile("" : "+v"(tid));
  const int lane = tid & 63, r = lane & 31, hf = lane >> 5;
  if (w < 4) {
    const int g16 = lane >> 4, tq = (lane & 15) >> 2, tp = lane & 3, tu = g16 & 1, th = g16 >> 1;
    const int u = w >> 1, t = w & 1;
    const int nl = 32 * u + r, nlc = nl < BN ? nl : BN - 1;
    const unsigned hrow = L_H + nlc * 128;
    f32x16 d;
#pragma unroll
    for (int i = 0; i < 16; ++i) d[i] = 0.f;
    const unsigned w_tr = L_W + (8 * th + tq) * 128 + (32 * t + 16 * tu + 4 * tp) * 2;
#pragma unroll
    for (int s = 0; s < 8; ++s) {
      const u32x2 a0 = lds_tr(L, w_tr + s * 2048), a1 = lds_tr(L, w_tr + s * 2048 + 512);
      const bf16x8 a = as_frag(u32x4{a0[0], a0[1], a1[0], a1[1]});
      bf16x8 bb;
      if (s < 4) {
        bb = lds_ld<bf16x8>(L, hrow + 32 * s + 16 * hf);
      } else {
        const unsigned o = L_ACC + nlc * ACC_NODE + 3 * 256 + (16 * (s - 4) + 8 * hf) * 4;
        const f32x4 m0 = lds_ld<f32x4>(L, o) * NLN2, m1 = lds_ld<f32x4>(L, o + 16) * NLN2;
        bb = as_frag(u32x4{cvt_pk(m0[0], m0[1]), cvt_pk(m0[2], m0[3]), cvt_pk(m1[0], m1[1]), cvt_pk(m1[2], m1[3])});
      }
      d = mfma(a, bb, d);
    }
    if (nl < BN) {
#pragma unroll
      for (int q = 0; q < 4; ++q) {
        const int f = 32 * t + 8 * q + 4 * hf;
        const u32x2 hv = lds_ld<u32x2>(L, hrow + 2 * f);
        u32x2 o;
#pragma unroll
        for (int k = 0; k < 2; ++k) {
          const unsigned pb = cvt_pk(d[4 * q + 2 * k], d[4 * q + 2 * k + 1]);
          const f32x2 x = {bf_lo(pb), bf_hi(pb)}, y = x * NLOG2E;
          const f32x2 den = f32x2{__builtin_amdgcn_exp2f(y[0]), __builtin_amdgcn_exp2f(y[1])} + 1.0f;
          const f32x2 act = x * f32x2{__builtin_amdgcn_rcpf(den[0]), __builtin_amdgcn_rcpf(den[1])};
          const unsigned ab = cvt_pk(act[0], act[1]);
          const f32x2 sum = f32x2{bf_lo(hv[k]), bf_hi(hv[k])} + f32x2{bf_lo(ab), bf_hi(ab)};
          o[k] = cvt_pk(sum[0], sum[1]);
        }
        *(u32x2*)(out + (size_t)(n0 + nl) * 64 + f) = o;
      }
    }
  } else {
    const int idx = tid - 256, nc = idx >> 1, half = idx & 1;
    const int ncc = nc < 3 * BN ? nc : 3 * BN - 1;
    const int n = ncc / 3, c = ncc - 3 * n;
    const unsigned o = L_ACC + n * ACC_NODE + c * 256 + half * 128;
    float s = 0.f;
#pragma unroll
    for (int k = 0; k < 8; ++k) {
      const f32x4 v = lds_ld<f32x4>(L, o + 16 * k), x = lds_ld<f32x4>(L, L_WX + half * 128 + 16 * k);
      s += v[0] * x[0] + v[1] * x[1] + v[2] * x[2] + v[3] * x[3];
    }
    s += __builtin_bit_cast(float, __builtin_amdgcn_update_dpp(0, __builtin_bit_cast(int, s), 0xb1, 0xf, 0xf, true));
    if (nc < 3 * BN && half == 0) {
      const float p0 = lds_ld<float>(L, L_PD + 16 * n + 4 * c);
      out[(size_t)NN * 64 + (size_t)n0 * 3 + nc] = (u16)cvt_pk(p0 + bf_round(s * NLN2), 0.f);
    }
  }
  if (blockIdx.x == 0 && tid == 511)
    __builtin_amdgcn_raw_buffer_store_b64(lds_ld<u32x2>(L, L_TAG + 8), ws_rsrc(karg<const char>(5)), WS_CTL, 0, 16);
}

}

extern "C" void kernel_launch(void* const* d_in, const int* in_sizes, int n_in, void* d_out, int out_size, void* d_ws, size_t ws_size,
                              hipStream_t stream) {
  const u16* h = (const u16*)d_in[0];
  const u16* pos = (const u16*)d_in[1];
  const u16* ea = (const u16*)d_in[2];
  const int* src = (const int*)d_in[3];
  const int* dst = (const int*)d_in[4];
  const u16* we = (const u16*)d_in[5];
  const u16* wx = (const u16*)d_in[6];
  const u16* wh = (const u16*)d_in[7];
  egnn_kernel<<<NBK, 512, 0, stream>>>(h, pos, we, wx, wh, (char*)d_ws, (u16*)d_out, src, dst, ea);
}
```

```cpp
#include <hip/hip_runtime.h>
#include <hip/hip_bf16.h>
#include <stdint.h>

#define DI __device__ __forceinline__
#define AS3 __attribute__((address_space(3)))
#define SB() __builtin_amdgcn_sched_barrier(0)

typedef short bf16x8 __attribute__((ext_vector_type(8)));
typedef short s16x4 __attribute__((ext_vector_type(4)));
typedef float f32x16 __attribute__((ext_vector_type(16)));
typedef float f32x4 __attribute__((ext_vector_type(4)));
typedef float f32x2 __attribute__((ext_vector_type(2)));
typedef unsigned u32x4 __attribute__((ext_vector_type(4)));
typedef unsigned u32x3 __attribute__((ext_vector_type(3)));
typedef unsigned u32x2 __attribute__((ext_vector_type(2)));
typedef int i32x4 __attribute__((ext_vector_type(4)));
typedef __bf16 bf16x2v __attribute__((ext_vector_type(2)));
typedef unsigned short u16;

namespace {

constexpr int NN = 10000;
constexpr int NE = 640000;
constexpr int BN = 40;
constexpr int NBK = NN / BN;
constexpr int NP = NBK;
constexpr int CH = NE / NP;
static_assert(CH == 5 * 512 && CH < 4096, "five edges per thread, 12-bit run starts");
constexpr float NLOG2E = -1.4426950408889634f;
constexpr float NLN2 = -0.6931471805599453f;
constexpr unsigned SPIN_LIMIT = 1u << 20;
constexpr unsigned NO_DATA = 0xffffff00u;
constexpr int PF_WAVE = 7, CODE_BYTES = 18432;
constexpr int POLL_SLEEP = 4;

constexpr unsigned WS_CTL = 0;
constexpr unsigned WS_ENT = 256;
constexpr unsigned WS_RECS = WS_ENT + NP * 4096;
constexpr unsigned REC_STRIDE = CH * 6;
constexpr unsigned WS_END = WS_RECS + NP * REC_STRIDE;
static_assert(WS_RECS % 128 == 0 && REC_STRIDE % 128 == 0, "no cache line holds records of two producers");

constexpr int CAP = 2880;
constexpr int PADS = 160;
constexpr int L_S = 0;
constexpr int S_WAVE = 2560;
constexpr int L_PD = 20480;
constexpr int L_WT = 21248;
constexpr int L_TOT = L_WT + 16;
constexpr int L_LOCK = 21312;
constexpr int L_TAG = 21344;
constexpr int L_WV = 21376;
constexpr int L_WX = 21504;
constexpr int L_TBL = 21760;
constexpr int L_RAW = L_TBL + 4096;
constexpr int L_PFR = L_RAW + (CAP + PADS) * 8;
constexpr int L_RING = L_PFR + 5120;
constexpr int L_ACC = L_RING + 8 * 4096;
constexpr int ACC_NODE = 1040;
constexpr int L_W = L_ACC + BN * ACC_NODE;
constexpr int L_H = L_W + 16384;
constexpr int L_SEG = L_H + BN * 128;
constexpr int L_END = L_SEG + 1024;
static_assert(L_PD % 1024 == 0, "group id is read from bits 7..9 of the pos address");
static_assert(L_RAW + (CAP + PADS) * 8 < 65536 && L_RAW >= 8 * CH && CAP >= CH + 5 * 31, "run table holds 16-bit LDS addresses");
static_assert(L_PFR % 16 == 0 && L_RING % 16 == 0 && L_ACC % 16 == 0 && L_W % 16 == 0 && L_END <= 163840, "LDS map");

DI unsigned cvt_pk(float a, float b) { f32x2 v = {a, b}; return __builtin_bit_cast(unsigned, __builtin_convertvector(v, bf16x2v)); }
DI float as_f(unsigned u) { return __builtin_bit_cast(float, u); }
DI float bf_lo(unsigned u) { return __builtin_bit_cast(float, u << 16); }
DI float bf_hi(unsigned u) { return __builtin_bit_cast(float, u & 0xffff0000u); }
DI float bf_round(float x) { return bf_lo(cvt_pk(x, x)); }
DI f32x16 mfma(bf16x8 a, bf16x8 b, f32x16 c) { return __builtin_amdgcn_mfma_f32_32x32x16_bf16(a, b, c, 0, 0, 0); }
DI bf16x8 as_frag(u32x4 v) { return __builtin_bit_cast(bf16x8, v); }

DI unsigned wave_incl_scan(unsigned x) {
  x += __builtin_amdgcn_update_dpp(0, (int)x, 0x111, 0xf, 0xf, true);
  x += __builtin_amdgcn_update_dpp(0, (int)x, 0x112, 0xf, 0xf, true);
  x += __builtin_amdgcn_update_dpp(0, (int)x, 0x114, 0xf, 0xf, true);
  x += __builtin_amdgcn_update_dpp(0, (int)x, 0x118, 0xf, 0xf, true);
  x += __builtin_amdgcn_update_dpp(0, (int)x, 0x142, 0xa, 0xf, false);
  x += __builtin_amdgcn_update_dpp(0, (int)x, 0x143, 0xc, 0xf, false);
  return x;
}

template <typename T> DI T lds_ld(AS3 char* L, unsigned off) { return *(AS3 T*)(L + off); }
template <typename T> DI void lds_st(AS3 char* L, unsigned off, T v) { *(AS3 T*)(L + off) = v; }
DI u32x2 lds_tr(AS3 char* L, unsigned off) {
  return __builtin_bit_cast(u32x2, __builtin_amdgcn_ds_read_tr16_b64_v4i16((AS3 s16x4*)(L + off)));
}

DI const char* uni_ptr(const char* p) {
  const unsigned long long v = (unsigned long long)p;
  const unsigned lo = __builtin_amdgcn_readfirstlane((int)(unsigned)v), hi = __builtin_amdgcn_readfirstlane((int)(unsigned)(v >> 32));
  return (const char*)(__attribute__((address_space(1))) const char*)(((unsigned long long)hi << 32) | lo);
}
DI void dma_piece(const char* base_, unsigned voff, unsigned ldsaddr_) {
  const char* base = uni_ptr(base_);
  const unsigned ldsaddr = __builtin_amdgcn_readfirstlane((int)ldsaddr_);
  unsigned keep;
  asm volatile("s_mov_b32 %0, m0\n\ts_mov_b32 m0, %3\n\ts_nop 4\n\tglobal_load_lds_dwordx4 %1, %2\n\ts_mov_b32 m0, %0"
               : "=&s"(keep) : "v"(voff), "s"(base), "s"(ldsaddr) : "memory");
}
DI void set_m0(unsigned ldsaddr) { asm volatile("s_mov_b32 m0, %0\n\ts_nop 0" :: "s"(ldsaddr) : "memory"); }
typedef __amdgpu_buffer_rsrc_t rsrc_t;
template <int OFF> DI void rows_index(unsigned a) {
  asm volatile("ds_read_u16 v166, %0 offset:%1\n\tds_read_u16 v168, %0 offset:%2\n\tds_read_u16 v170, %0 offset:%3\n\tds_read_u16 v172, %0 offset:%4"
               :: "v"(a), "i"(OFF), "i"(OFF + 64), "i"(OFF + 128), "i"(OFF + 192) : "memory");
}
DI void rows_gather(rsrc_t rows) {
  asm volatile("s_waitcnt lgkmcnt(0)\n\t"
               "buffer_load_dwordx4 v[166:167], %0, %1 idxen offen lds\n\t"
               "buffer_load_dwordx4 v[168:169], %0, %2 idxen offen offset:1024 lds\n\t"
               "buffer_load_dwordx4 v[170:171], %0, %3 idxen offen offset:2048 lds\n\t"
               "buffer_load_dwordx4 v[172:173], %0, 0 idxen offen offset:3072 lds"
               :: "s"(rows), "s"(3072u), "s"(2048u), "s"(1024u) : "memory");
}
DI rsrc_t ws_rsrc(const char* ws) { return __builtin_amdgcn_make_buffer_rsrc((void*)ws, 0, WS_END, 0x00020000); }
DI u32x4 ld16_dev(rsrc_t r, unsigned off) { return __builtin_amdgcn_raw_buffer_load_b128(r, off, 0, 16); }
DI u32x3 ld12_rec(rsrc_t r, unsigned off) { return __builtin_amdgcn_raw_buffer_load_b96(r, off, 0, 16); }
DI unsigned ld4_rec(rsrc_t r, unsigned off) { return __builtin_amdgcn_raw_buffer_load_b32(r, off, 0, 16); }
DI unsigned ld2_rec(rsrc_t r, unsigned off) { return __builtin_amdgcn_raw_buffer_load_b16(r, off, 0, 16); }
DI u32x2 ld8_dev(rsrc_t r, unsigned off) { return __builtin_amdgcn_raw_buffer_load_b64(r, off, 0, 16); }
DI void st16_dev(rsrc_t r, unsigned off, u32x4 v) { __builtin_amdgcn_raw_buffer_store_b128(v, r, off, 0, 16); }
DI void wait_vm0() { asm volatile("s_waitcnt vmcnt(0)" ::: "memory"); }

struct Prep {
  unsigned sdA, sdB;
  unsigned saA, saB;
  unsigned de;
};
struct Raw { unsigned src, pda, w1, n320; };

#pragma clang diagnostic ignored "-Winline-asm"
#define T4(op, d, s0, s1, s2, s3, d1, d2, d3) op " v" #d ", v" #s0 "\n\t" op " v" #d1 ", v" #s1 "\n\t" op " v" #d2 ", v" #s2 "\n\t" op " v" #d3 ", v" #s3 "\n\t"
#define EXP_LO(a, b, c, d) T4("v_exp_f32", 184, a, b, c, d, 185, 186, 187)
#define EXP_HI(a, b, c, d) T4("v_exp_f32", 188, a, b, c, d, 189, 190, 191)
#define RCP_LO T4("v_rcp_f32", 184, 184, 185, 186, 187, 185, 186, 187)
#define RCP_HI T4("v_rcp_f32", 188, 188, 189, 190, 191, 189, 190, 191)
#define PKA(a, b) "v_pk_add_f32 v[" #a ":" #b "], v[" #a ":" #b "], 1.0 op_sel_hi:[1,0]\n\t"
#define ADD1 PKA(184, 185) PKA(186, 187) PKA(188, 189) PKA(190, 191)
#define PKM(a, b, c, d) "v_pk_mul_f32 v[" #a ":" #b "], v[" #a ":" #b "], v[" #c ":" #d "]\n\t"
#define MULY(a0, a1, a2, a3, a4, a5, a6, a7) PKM(184, 185, a0, a1) PKM(186, 187, a2, a3) PKM(188, 189, a4, a5) PKM(190, 191, a6, a7)
#define CVT(d, a, b) "v_cvt_pk_bf16_f32 v" #d ", v" #a ", v" #b "\n\t"
#define PACK(d0, d1, d2, d3) CVT(d0, 184, 185) CVT(d1, 186, 187) CVT(d2, 188, 189) CVT(d3, 190, 191)
#define MF(acc, a, b) "v_mfma_f32_32x32x16_bf16 " acc ", " a ", " b ", " acc "\n\t"
#define MF0(acc, a, b) "v_mfma_f32_32x32x16_bf16 " acc ", " a ", " b ", 0\n\t"
#define Y0 "v[192:207]"
#define Y1 "v[208:223]"
#define ACC0 "v[224:239]"
#define ACC1 "v[240:255]"
#define X0 "v[176:179]"
#define X1 "v[180:183]"

DI void tiles_init() {
  asm volatile(
               "v_mov_b64 v[224:225], 0\n\t"
               "v_mov_b64 v[226:227], 0\n\t"
               "v_mov_b64 v[228:229], 0\n\t"
               "v_mov_b64 v[230:231], 0\n\t"
               "v_mov_b64 v[232:233], 0\n\t"
               "v_mov_b64 v[234:235], 0\n\t"
               "v_mov_b64 v[236:237], 0\n\t"
               "v_mov_b64 v[238:239], 0\n\t"
               "v_mov_b64 v[240:241], 0\n\t"
               "v_mov_b64 v[242:243], 0\n\t"
               "v_mov_b64 v[244:245], 0\n\t"
               "v_mov_b64 v[246:247], 0\n\t"
               "v_mov_b64 v[248:249], 0\n\t"
               "v_mov_b64 v[250:251], 0\n\t"
               "v_mov_b64 v[252:253], 0\n\t"
               "v_mov_b64 v[254:255], 0\n\t"
               "v_mov_b64 v[180:181], 0\n\t"
               "v_mov_b64 v[182:183], 0\n\t"
               "s_nop 1" ::: "v166", "v255");
}
DI void alpha1(const bf16x8& asl) {
  asm volatile(EXP_LO(192, 193, 194, 195) MF(ACC1, "%[asl]", X1) EXP_HI(196, 197, 198, 199) :: [asl] "v"(asl));
}
DI void alpha2(const bf16x8& ax, const bf16x8& bx, const bf16x8& q0, const bf16x8& w0) {
  asm volatile(ADD1 MF0(Y1, "%[ax]", "%[bx]") RCP_LO MF(Y1, "%[q0]", "%[w0]") RCP_HI
               MULY(192, 193, 194, 195, 196, 197, 198, 199) PACK(176, 177, 178, 179)
               :: [ax] "v"(ax), [bx] "v"(bx), [q0] "v"(q0), [w0] "v"(w0));
}
DI void alpha3(const bf16x8& q1, const bf16x8& w1, const bf16x8& q2, const bf16x8& w2, const bf16x8& q3, const bf16x8& w3, const bf16x8& as0) {
  asm volatile(MF(Y1, "%[q1]", "%[w1]") EXP_LO(200, 201, 202, 203) MF(Y1, "%[q2]", "%[w2]") EXP_HI(204, 205, 206, 207) ADD1
               MF(Y1, "%[q3]", "%[w3]") RCP_LO MF(ACC0, "%[as]", X0) RCP_HI
               MULY(200, 201, 202, 203, 204, 205, 206, 207) PACK(180, 181, 182, 183)
               :: [q1] "v"(q1), [w1] "v"(w1), [q2] "v"(q2), [w2] "v"(w2), [q3] "v"(q3), [w3] "v"(w3), [as] "v"(as0));
}
DI void beta1(const bf16x8& as1) {
  asm volatile(EXP_LO(208, 209, 210, 211) MF(ACC0, "%[as]", X1) EXP_HI(212, 213, 214, 215) ADD1 :: [as] "v"(as1));
}
DI void beta2(const bf16x8& ax, const bf16x8& bx, const bf16x8& q0, const bf16x8& w0) {
  asm volatile(MF0(Y0, "%[ax]", "%[bx]") RCP_LO MF(Y0, "%[q0]", "%[w0]") RCP_HI
               MULY(208, 209, 210, 211, 212, 213, 214, 215) PACK(176, 177, 178, 179)
               :: [ax] "v"(ax), [bx] "v"(bx), [q0] "v"(q0), [w0] "v"(w0));
}
DI void beta3(const bf16x8& q1, const bf16x8& w1, const bf16x8& q2, const bf16x8& w2, const bf16x8& q3, const bf16x8& w3, const bf16x8& as0) {
  asm volatile(MF(Y0, "%[q1]", "%[w1]") EXP_LO(216, 217, 218, 219) MF(Y0, "%[q2]", "%[w2]") EXP_HI(220, 221, 222, 223) ADD1
               MF(Y0, "%[q3]", "%[w3]") RCP_LO MF(ACC1, "%[as]", X0) RCP_HI
               MULY(216, 217, 218, 219, 220, 221, 222, 223) PACK(180, 181, 182, 183)
               :: [q1] "v"(q1), [w1] "v"(w1), [q2] "v"(q2), [w2] "v"(w2), [q3] "v"(q3), [w3] "v"(w3), [as] "v"(as0));
}
DI void chain_y0(const bf16x8& ax, const bf16x8& bx, const bf16x8& q0, const bf16x8& w0, const bf16x8& q1, const bf16x8& w1,
                 const bf16x8& q2, const bf16x8& w2, const bf16x8& q3, const bf16x8& w3) {
  asm volatile("s_nop 1\n\t" MF0(Y0, "%[ax]", "%[bx]") MF(Y0, "%[q0]", "%[w0]") MF(Y0, "%[q1]", "%[w1]") MF(Y0, "%[q2]", "%[w2]")
               MF(Y0, "%[q3]", "%[w3]") "s_nop 15"
               :: [ax] "v"(ax), [bx] "v"(bx), [q0] "v"(q0), [w0] "v"(w0), [q1] "v"(q1), [w1] "v"(w1), [q2] "v"(q2), [w2] "v"(w2),
                  [q3] "v"(q3), [w3] "v"(w3));
}
DI void last_block(const bf16x8& as1) { asm volatile("s_nop 1\n\t" MF(ACC1, "%[as]", X1) :: [as] "v"(as1)); }
DI void acc_gather(unsigned a0, unsigned a1, unsigned a2, unsigned a3) {
  f32x2 t0, t1, t2, t3, t4, t5, t6, t7;
  asm volatile(
               "ds_read2_b32 %[t0], %[a0] offset0:0 offset1:64\n\t"
               "ds_read2_b32 %[t1], %[a0] offset0:128 offset1:192\n\t"
               "ds_read2_b32 %[t2], %[a0] offset0:32 offset1:96\n\t"
               "ds_read2_b32 %[t3], %[a0] offset0:160 offset1:224\n\t"
               "ds_read2_b32 %[t4], %[a1] offset0:0 offset1:64\n\t"
               "ds_read2_b32 %[t5], %[a1] offset0:128 offset1:192\n\t"
               "ds_read2_b32 %[t6], %[a1] offset0:32 offset1:96\n\t"
               "ds_read2_b32 %[t7], %[a1] offset0:160 offset1:224\n\t"
               "s_waitcnt lgkmcnt(4)\n\t"
               "v_pk_add_f32 v[224:225], v[224:225], %[t0]\n\t"
               "v_pk_add_f32 v[226:227], v[226:227], %[t1]\n\t"
               "v_pk_add_f32 v[240:241], v[240:241], %[t2]\n\t"
               "v_pk_add_f32 v[242:243], v[242:243], %[t3]\n\t"
               "s_waitcnt lgkmcnt(0)\n\t"
               "v_pk_add_f32 v[228:229], v[228:229], %[t4]\n\t"
               "v_pk_add_f32 v[230:231], v[230:231], %[t5]\n\t"
               "v_pk_add_f32 v[244:245], v[244:245], %[t6]\n\t"
               "v_pk_add_f32 v[246:247], v[246:247], %[t7]\n\t"
               "ds_read2_b32 %[t0], %[a2] offset0:0 offset1:64\n\t"
               "ds_read2_b32 %[t1], %[a2] offset0:128 offset1:192\n\t"
               "ds_read2_b32 %[t2], %[a2] offset0:32 offset1:96\n\t"
               "ds_read2_b32 %[t3], %[a2] offset0:160 offset1:224\n\t"
               "ds_read2_b32 %[t4], %[a3] offset0:0 offset1:64\n\t"
               "ds_read2_b32 %[t5], %[a3] offset0:128 offset1:192\n\t"
               "ds_read2_b32 %[t6], %[a3] offset0:32 offset1:96\n\t"
               "ds_read2_b32 %[t7], %[a3] offset0:160 offset1:224\n\t"
               "s_waitcnt lgkmcnt(4)\n\t"
               "v_pk_add_f32 v[232:233], v[232:233], %[t0]\n\t"
               "v_pk_add_f32 v[234:235], v[234:235], %[t1]\n\t"
               "v_pk_add_f32 v[248:249], v[248:249], %[t2]\n\t"
               "v_pk_add_f32 v[250:251], v[250:251], %[t3]\n\t"
               "s_waitcnt lgkmcnt(0)\n\t"
               "v_pk_add_f32 v[236:237], v[236:237], %[t4]\n\t"
               "v_pk_add_f32 v[238:239], v[238:239], %[t5]\n\t"
               "v_pk_add_f32 v[252:253], v[252:253], %[t6]\n\t"
               "v_pk_add_f32 v[254:255], v[254:255], %[t7]\n\t"
               "s_nop 0"
               : [t0] "=&v"(t0), [t1] "=&v"(t1), [t2] "=&v"(t2), [t3] "=&v"(t3), [t4] "=&v"(t4), [t5] "=&v"(t5), [t6] "=&v"(t6), [t7] "=&v"(t7)
               : [a0] "v"(a0), [a1] "v"(a1), [a2] "v"(a2), [a3] "v"(a3) : "memory");
}
DI void acc_store(unsigned a0, unsigned a1, unsigned a2, unsigned a3) {
  asm volatile(
               "ds_write2_b32 %[a0], v224, v225 offset0:0 offset1:64\n\t"
               "ds_write2_b32 %[a0], v226, v227 offset0:128 offset1:192\n\t"
               "ds_write2_b32 %[a0], v240, v241 offset0:32 offset1:96\n\t"
               "ds_write2_b32 %[a0], v242, v243 offset0:160 offset1:224\n\t"
               "ds_write2_b32 %[a1], v228, v229 offset0:0 offset1:64\n\t"
               "ds_write2_b32 %[a1], v230, v231 offset0:128 offset1:192\n\t"
               "ds_write2_b32 %[a1], v244, v245 offset0:32 offset1:96\n\t"
               "ds_write2_b32 %[a1], v246, v247 offset0:160 offset1:224\n\t"
               "ds_write2_b32 %[a2], v232, v233 offset0:0 offset1:64\n\t"
               "ds_write2_b32 %[a2], v234, v235 offset0:128 offset1:192\n\t"
               "ds_write2_b32 %[a2], v248, v249 offset0:32 offset1:96\n\t"
               "ds_write2_b32 %[a2], v250, v251 offset0:160 offset1:224\n\t"
               "ds_write2_b32 %[a3], v236, v237 offset0:0 offset1:64\n\t"
               "ds_write2_b32 %[a3], v238, v239 offset0:128 offset1:192\n\t"
               "ds_write2_b32 %[a3], v252, v253 offset0:32 offset1:96\n\t"
               "ds_write2_b32 %[a3], v254, v255 offset0:160 offset1:224\n\t"
               "s_waitcnt lgkmcnt(0)\n\t"
               "v_mov_b64 v[224:225], 0\n\t"
               "v_mov_b64 v[226:227], 0\n\t"
               "v_mov_b64 v[228:229], 0\n\t"
               "v_mov_b64 v[230:231], 0\n\t"
               "v_mov_b64 v[232:233], 0\n\t"
               "v_mov_b64 v[234:235], 0\n\t"
               "v_mov_b64 v[236:237], 0\n\t"
               "v_mov_b64 v[238:239], 0\n\t"
               "v_mov_b64 v[240:241], 0\n\t"
               "v_mov_b64 v[242:243], 0\n\t"
               "v_mov_b64 v[244:245], 0\n\t"
               "v_mov_b64 v[246:247], 0\n\t"
               "v_mov_b64 v[248:249], 0\n\t"
               "v_mov_b64 v[250:251], 0\n\t"
               "v_mov_b64 v[252:253], 0\n\t"
               "v_mov_b64 v[254:255], 0\n\t"
               "s_nop 7"
               :: [a0] "v"(a0), [a1] "v"(a1), [a2] "v"(a2), [a3] "v"(a3) : "memory");
}
DI void acc_gather_last(unsigned a0, unsigned a1, unsigned a2, unsigned a3) {
  asm volatile(
               "ds_read2_b32 v[176:177], %[a0] offset0:0 offset1:64\n\t"
               "ds_read2_b32 v[178:179], %[a0] offset0:128 offset1:192\n\t"
               "ds_read2_b32 v[180:181], %[a0] offset0:32 offset1:96\n\t"
               "ds_read2_b32 v[182:183], %[a0] offset0:160 offset1:224\n\t"
               "ds_read2_b32 v[184:185], %[a1] offset0:0 offset1:64\n\t"
               "ds_read2_b32 v[186:187], %[a1] offset0:128 offset1:192\n\t"
               "ds_read2_b32 v[188:189], %[a1] offset0:32 offset1:96\n\t"
               "ds_read2_b32 v[190:191], %[a1] offset0:160 offset1:224\n\t"
               "ds_read2_b32 v[192:193], %[a2] offset0:0 offset1:64\n\t"
               "ds_read2_b32 v[194:195], %[a2] offset0:128 offset1:192\n\t"
               "ds_read2_b32 v[196:197], %[a2] offset0:32 offset1:96\n\t"
               "ds_read2_b32 v[198:199], %[a2] offset0:160 offset1:224\n\t"
               "ds_read2_b32 v[200:201], %[a3] offset0:0 offset1:64\n\t"
               "ds_read2_b32 v[202:203], %[a3] offset0:128 offset1:192\n\t"
               "ds_read2_b32 v[204:205], %[a3] offset0:32 offset1:96\n\t"
               "ds_read2_b32 v[206:207], %[a3] offset0:160 offset1:224\n\t"
               "s_waitcnt lgkmcnt(12)\n\t"
               "v_pk_add_f32 v[224:225], v[224:225], v[176:177]\n\t"
               "v_pk_add_f32 v[226:227], v[226:227], v[178:179]\n\t"
               "v_pk_add_f32 v[240:241], v[240:241], v[180:181]\n\t"
               "v_pk_add_f32 v[242:243], v[242:243], v[182:183]\n\t"
               "s_waitcnt lgkmcnt(8)\n\t"
               "v_pk_add_f32 v[228:229], v[228:229], v[184:185]\n\t"
               "v_pk_add_f32 v[230:231], v[230:231], v[186:187]\n\t"
               "v_pk_add_f32 v[244:245], v[244:245], v[188:189]\n\t"
               "v_pk_add_f32 v[246:247], v[246:247], v[190:191]\n\t"
               "s_waitcnt lgkmcnt(4)\n\t"
               "v_pk_add_f32 v[232:233], v[232:233], v[192:193]\n\t"
               "v_pk_add_f32 v[234:235], v[234:235], v[194:195]\n\t"
               "v_pk_add_f32 v[248:249], v[248:249], v[196:197]\n\t"
               "v_pk_add_f32 v[250:251], v[250:251], v[198:199]\n\t"
               "s_waitcnt lgkmcnt(0)\n\t"
               "v_pk_add_f32 v[236:237], v[236:237], v[200:201]\n\t"
               "v_pk_add_f32 v[238:239], v[238:239], v[202:203]\n\t"
               "v_pk_add_f32 v[252:253], v[252:253], v[204:205]\n\t"
               "v_pk_add_f32 v[254:255], v[254:255], v[206:207]\n\t"
               "s_nop 0"
               :: [a0] "v"(a0), [a1] "v"(a1), [a2] "v"(a2), [a3] "v"(a3) : "memory");
}
DI void acc_store_last(unsigned a0, unsigned a1, unsigned a2, unsigned a3) {
  asm volatile(
               "ds_write2_b32 %[a0], v224, v225 offset0:0 offset1:64\n\t"
               "ds_write2_b32 %[a0], v226, v227 offset0:128 offset1:192\n\t"
               "ds_write2_b32 %[a0], v240, v241 offset0:32 offset1:96\n\t"
               "ds_write2_b32 %[a0], v242, v243 offset0:160 offset1:224\n\t"
               "ds_write2_b32 %[a1], v228, v229 offset0:0 offset1:64\n\t"
               "ds_write2_b32 %[a1], v230, v231 offset0:128 offset1:192\n\t"
               "ds_write2_b32 %[a1], v244, v245 offset0:32 offset1:96\n\t"
               "ds_write2_b32 %[a1], v246, v247 offset0:160 offset1:224\n\t"
               "ds_write2_b32 %[a2], v232, v233 offset0:0 offset1:64\n\t"
               "ds_write2_b32 %[a2], v234, v235 offset0:128 offset1:192\n\t"
               "ds_write2_b32 %[a2], v248, v249 offset0:32 offset1:96\n\t"
               "ds_write2_b32 %[a2], v250, v251 offset0:160 offset1:224\n\t"
               "ds_write2_b32 %[a3], v236, v237 offset0:0 offset1:64\n\t"
               "ds_write2_b32 %[a3], v238, v239 offset0:128 offset1:192\n\t"
               "ds_write2_b32 %[a3], v252, v253 offset0:32 offset1:96\n\t"
               "ds_write2_b32 %[a3], v254, v255 offset0:160 offset1:224\n\t"
               "s_nop 0"
               :: [a0] "v"(a0), [a1] "v"(a1), [a2] "v"(a2), [a3] "v"(a3) : "memory");
}
DI unsigned lds_lock(unsigned addr) {
  unsigned t, old;
  unsigned long long sv;
  asm volatile("s_mov_b64 %2, exec\n\ts_mov_b64 exec, 1\n"
               "1:\n\tds_wrxchg_rtn_b32 %0, %3, %4\n\ts_waitcnt lgkmcnt(0)\n\tv_cmp_eq_u32 vcc, 1, %0\n\ts_cbranch_vccz 2f\n\t"
               "s_sleep 1\n\ts_branch 1b\n"
               "2:\n\tv_readfirstlane_b32 %1, %0\n\ts_mov_b64 exec, %2\n\ts_nop 15"
               : "=&v"(t), "=&s"(old), "=&s"(sv) : "v"(addr), "v"(1u) : "vcc", "memory");
  return old;
}

template <typename T> DI T* karg(int k) {
  typedef __attribute__((address_space(4))) const unsigned long long* kp_t;
  kp_t kp = (kp_t)__builtin_amdgcn_kernarg_segment_ptr();
  asm volatile("" : "+s"(kp));
  return (T*)(__attribute__((address_space(1))) T*)kp[k];
}

__global__ __launch_bounds__(512) __attribute__((amdgpu_num_vgpr(83))) void egnn_kernel(
    const u16* __restrict__ h_, const u16* __restrict__ pos_, const u16* __restrict__ we_, const u16* __restrict__ wx_,
    const u16* __restrict__ wh_, char* __restrict__ ws_, u16* __restrict__ out_, const int* __restrict__ src_,
    const int* __restrict__ dst_, const u16* __restrict__ ea_) {
  __shared__ __attribute__((aligned(1024))) char lds[L_END];
  AS3 char* L = (AS3 char*)lds;
  const unsigned LB = (unsigned)(size_t)L;
  const int tid_ = threadIdx.x;
  const int w = __builtin_amdgcn_readfirstlane(tid_ >> 6);
  const int xl = blockIdx.x & 7;
  const int b = xl * 31 + (xl < 2 ? xl : 2) + (blockIdx.x >> 3);
  const int n0 = b * BN;
  unsigned cw[2][2];

  {
    const int tid = tid_, lane = tid & 63, r = lane & 31, hf = lane >> 5, p = blockIdx.x;
    const rsrc_t rs = ws_rsrc(ws_);
    const int e0 = p * CH + 4 * tid, e1 = p * CH + 2048 + tid;
    const i32x4 d4 = *(const i32x4*)(dst_ + e0), s4 = *(const i32x4*)(src_ + e0);
    const u32x2 a2 = *(const u32x2*)(ea_ + e0);
    unsigned dd[5] = {(unsigned)d4[0], (unsigned)d4[1], (unsigned)d4[2], (unsigned)d4[3], (unsigned)dst_[e1]};
    const unsigned s5 = src_[e1], a5 = ea_[e1];
    const u32x2 ctl = ld8_dev(rs, WS_CTL);
    SB();
    if (w == PF_WAVE) {
      unsigned long long pc;
      asm volatile("s_getpc_b64 %0" : "=s"(pc));
      asm volatile("global_load_dword v184, %0, %1\n\tglobal_load_dword v185, %2, %1" :: "v"(lane * 128u), "s"(pc), "v"(lane * 128u + 8192u) : "memory");
      if (lane < (CODE_BYTES - 16384) / 128) asm volatile("global_load_dword v186, %0, %1" :: "v"(lane * 128u + 16384u), "s"(pc) : "memory");
    }
    dma_piece((const char*)we_ + w * 1024, lane * 16, LB + L_W + w * 1024);
    if (w < 5) dma_piece((const char*)h_ + (size_t)n0 * 128 + w * 1024, lane * 16, LB + L_H + w * 1024);
    const u32x4 w2raw = *(const u32x4*)(we_ + 64 * 64 + tid * 8);
    unsigned sraw[3], craw[4];
#pragma unroll
    for (int t = 0; t < 2; ++t) { craw[2 * t] = we_[128 * 64 + 32 * t + r]; craw[2 * t + 1] = we_[129 * 64 + 32 * t + r]; }
    {
      const u16* pp = w == 1 ? wx_ + lane : pos_ + (lane < BN ? n0 + lane : 0) * 3;
      const int st = w == 1 ? 0 : 1;
      sraw[0] = pp[0]; sraw[1] = pp[st]; sraw[2] = pp[2 * st];
    }
    SB();
    {
      const u32x4 z4 = {0, 0, 0, 0};
      static_assert(8 * S_WAVE == 2 * 8192 + 256 * 16 && BN * ACC_NODE == 5 * 8192 + 40 * 16, "clearing pattern");
      if (tid < 256) lds_st<u32x4>(L, L_TBL + 16 * tid, z4);
      lds_st<u32x4>(L, L_S + tid * 16, z4);
      lds_st<u32x4>(L, L_S + 8192 + tid * 16, z4);
#pragma unroll
      for (int k = 0; k < 5; ++k) lds_st<u32x4>(L, L_ACC + k * 8192 + tid * 16, z4);
      if (tid < 256) lds_st<u32x4>(L, L_S + 16384 + tid * 16, z4);
      if (tid < 40) lds_st<u32x4>(L, L_ACC + 5 * 8192 + tid * 16, z4);
      if (tid < 8) lds_st<unsigned>(L, L_LOCK + 4 * tid, 0u);
      if (tid == 8) lds_st<unsigned>(L, L_WT, 0u);
    }
    __syncthreads();
    unsigned idx[5], rank[5], bk[5];
#pragma unroll
    for (int i = 0; i < 5; ++i) {
      const unsigned G = dd[i] >> 3;
      bk[i] = (G * 52429u) >> 18;
      idx[i] = G + 3 * bk[i];
      const unsigned sh = (idx[i] & 1) << 4;
      const unsigned old = __hip_atomic_fetch_add((AS3 unsigned*)(L + L_TBL + 4 * (idx[i] >> 1)), 1u << sh, __ATOMIC_RELAXED,
                                                  __HIP_MEMORY_SCOPE_WORKGROUP);
      rank[i] = (old >> sh) & 0xffffu;
    }
    __syncthreads();
    const unsigned tlo = ctl[0] + 1, thi = ctl[1] + (tlo == 0);
    const unsigned tag0 = tlo & 0xfffffffu, tag1 = ((tag0 * 2654435761u) ^ ((tlo >> 28) | (thi << 4))) & 0xfffffffu;
    u32x4 entry = {0, 0, 0, 0};
    if (w < 4) {
      const u32x4 c = lds_ld<u32x4>(L, L_TBL + 16 * tid);
      const unsigned tot = (c[0] & 0xffffu) + (c[0] >> 16) + (c[1] & 0xffffu) + (c[1] >> 16) + (c[2] & 0xffffu);
      const unsigned incl = wave_incl_scan(tot);
      unsigned old;
      unsigned long long sv;
      asm volatile("s_mov_b64 %1, exec\n\ts_mov_b64 exec, %4\n\tds_add_rtn_u32 %0, %2, %3\n\ts_mov_b64 exec, %1\n\ts_waitcnt lgkmcnt(0)"
                   : "=&v"(old), "=&s"(sv) : "v"(LB + L_WT), "v"(incl), "s"(1ull << 63) : "memory");
      const unsigned s0 = incl - tot + __builtin_amdgcn_readlane(old, 63);
      const unsigned s1 = s0 + (c[0] & 0xffffu), s2 = s1 + (c[0] >> 16), s3 = s2 + (c[1] & 0xffffu), s4_ = s3 + (c[1] >> 16);
      const unsigned s5_ = s0 + tot;
      lds_st<u32x3>(L, L_TBL + 16 * tid, u32x3{s0 | (s1 << 16), s2 | (s3 << 16), s4_});
      entry = u32x4{s0 | (s1 << 12) | (s2 << 24), (s2 >> 8) | (tag0 << 4), s3 | (s4_ << 12) | (s5_ << 24), (s5_ >> 8) | (tag1 << 4)};
    }
    if (tid == 0) {
      lds_st<u32x4>(L, L_TAG, u32x4{tag0, tag1, tlo, thi});
      lds_st<u32x4>(L, L_TOT, u32x4{0, 0, 0, 0});
      lds_st<u32x4>(L, L_TOT + 16, u32x4{0, 0, 0, 0});
    }
    {
      u32x4 o;
#pragma unroll
      for (int k = 0; k < 4; ++k) o[k] = cvt_pk(bf_lo(w2raw[k]) * NLOG2E, bf_hi(w2raw[k]) * NLOG2E);
      lds_st<u32x4>(L, L_W + 8192 + tid * 16, o);
    }
    wait_vm0();
    __syncthreads();
    {
      unsigned ps[5];
#pragma unroll
      for (int i = 0; i < 5; ++i) ps[i] = lds_ld<u16>(L, L_TBL + 2 * idx[i]) + rank[i];
#pragma unroll
      for (int i = 0; i < 5; ++i) {
        const unsigned dl = dd[i] - BN * bk[i];
        const unsigned sv = i < 4 ? (unsigned)s4[i] : s5;
        const unsigned eav = i == 4 ? a5 : (i & 1) ? (a2[i >> 1] >> 16) : a2[i >> 1];
        const unsigned pb = L_RAW + 12 * (ps[i] >> 1), odd = ps[i] & 1;
        lds_st<unsigned>(L, pb + 4 * odd, sv | ((L_PD + 16 * dl) << 16));
        lds_st<u16>(L, pb + 8 + 2 * odd, (u16)eav);
      }
    }
    __syncthreads();
    {
      static_assert(REC_STRIDE == (512 + 448) * 16, "copy pattern");
      const unsigned o = WS_RECS + p * REC_STRIDE + 16 * tid;
      st16_dev(rs, o, lds_ld<u32x4>(L, L_RAW + 16 * tid));
      if (tid < 448) st16_dev(rs, o + 8192, lds_ld<u32x4>(L, L_RAW + 8192 + 16 * tid));
    }
    SB();
    if (w >= 4) {
      const int u = (w - 4) >> 1, t = w & 1;
      f32x16 pacc;
#pragma unroll
      for (int i = 0; i < 16; ++i) pacc[i] = 0.f;
#pragma unroll
      for (int s = 0; s < 4; ++s) {
        const int nl = 32 * u + r, nlc = nl < BN ? nl : BN - 1;
        const bf16x8 ha = lds_ld<bf16x8>(L, L_H + nlc * 128 + 32 * s + 16 * hf);
        const unsigned o = L_W + (16 * s + 8 * (lane >> 5) + ((lane & 15) >> 2)) * 128 + (32 * t + 16 * ((lane >> 4) & 1) + 4 * (lane & 3)) * 2;
        const u32x2 b0 = lds_tr(L, o), b1 = lds_tr(L, o + 512);
        pacc = mfma(ha, as_frag(u32x4{b0[0], b0[1], b1[0], b1[1]}), pacc);
      }
#pragma unroll
      for (int q = 0; q < 4; ++q)
        if (4 * u + q < 5)
          lds_st<u32x2>(L, L_PFR + ((4 * u + q) * 2 + t) * 512 + lane * 8,
                        u32x2{cvt_pk(pacc[4 * q] * NLOG2E, pacc[4 * q + 1] * NLOG2E), cvt_pk(pacc[4 * q + 2] * NLOG2E, pacc[4 * q + 3] * NLOG2E)});
    } else if (w == 0) {
      if (lane <= BN)
        lds_st<f32x4>(L, L_PD + 16 * lane, f32x4{bf_lo(sraw[0]), bf_lo(sraw[1]), bf_lo(sraw[2]), lane < BN ? 1.0f : 0.0f});
    } else if (w == 1) {
      lds_st<float>(L, L_WX + 4 * lane, bf_lo(sraw[0]));
    }
#pragma unroll
    for (int t = 0; t < 2; ++t) {
      const float d = bf_lo(craw[2 * t]) * NLOG2E, a = bf_lo(craw[2 * t + 1]) * NLOG2E;
      const unsigned hi = cvt_pk(d, a);
      cw[t][0] = hi;
      cw[t][1] = cvt_pk(d - bf_lo(hi), a - bf_hi(hi));
    }
    wait_vm0();
    __syncthreads();
    if (tid < NBK) st16_dev(rs, WS_ENT + p * 4096 + tid * 16, entry);
  }

  int pa = 0, np = NP;
  for (unsigned it = 0;; ++it) {
    const u16 *h = karg<const u16>(0), *pos = karg<const u16>(1);
    const char* ws = karg<const char>(5);
    const rsrc_t rs = ws_rsrc(ws);
    int tid = tid_;
    asm volatile("" : "+v"(tid));
    const int lane = tid & 63, r = lane & 31, hf = lane >> 5;
    const unsigned l_tot = L_TOT + 16 * (it & 1);
    unsigned pA = 0, pB = 0, pC = 0, d1 = 0, d2 = 0, d3 = 0, d4 = 0;
    if (w < 4) {
      const u32x2 tg = lds_ld<u32x2>(L, L_TAG);
      const unsigned eo = WS_ENT + (tid < NP ? tid : 0) * 4096 + b * 16;
      auto seen = [&](const u32x4& e) { return __all(tid >= NP || ((e[1] >> 4) == tg[0] && (e[3] >> 4) == tg[1])); };
      auto poll = [&]() {
        __builtin_amdgcn_s_sleep(POLL_SLEEP);
        asm volatile("" ::: "memory");
        return ld16_dev(rs, eo);
      };
      u32x4 ent, e0 = ld16_dev(rs, eo);
      for (unsigned spins = 0;; ++spins) {
        const u32x4 e1 = poll();
        if (seen(e0)) { ent = e0; break; }
        e0 = poll();
        ent = e1;
        if (seen(e1)) break;
        if (spins > SPIN_LIMIT) {
          __hip_atomic_fetch_or((AS3 unsigned*)(L + l_tot + 12), 1u, __ATOMIC_RELAXED, __HIP_MEMORY_SCOPE_WORKGROUP);
          break;
        }
      }
      const unsigned s0 = ent[0] & 0xfffu, s1 = (ent[0] >> 12) & 0xfffu, s2 = __builtin_amdgcn_alignbit(ent[1], ent[0], 24) & 0xfffu;
      const unsigned s3 = ent[2] & 0xfffu, s4 = (ent[2] >> 12) & 0xfffu, s5 = __builtin_amdgcn_alignbit(ent[3], ent[2], 24) & 0xfffu;
      const bool act = (unsigned)(tid - pa) < (unsigned)np && tid < NP;
      d1 = s1 - s0; d2 = s2 - s0; d3 = s3 - s0; d4 = s4 - s0;
      const unsigned cnt = act ? s5 - s0 : 0;
      const unsigned cA = act ? d1 | ((s2 - s1) << 16) : 0, cB = act ? (s3 - s2) | ((s4 - s3) << 16) : 0, cC = act ? s5 - s4 : 0;
      lds_st<unsigned>(L, L_SEG + 4 * tid, (tid * (CH / 2) + (s0 >> 1)) | ((cnt + (s0 & 1)) << 19) | (s0 << 31));
      const unsigned iA = wave_incl_scan(cA), iB = wave_incl_scan(cB), iC = wave_incl_scan(cC);
      if (lane == 63) {
        lds_st<u32x3>(L, L_WV + 16 * w, u32x3{iA, iB, iC});
        __hip_atomic_fetch_add((AS3 unsigned*)(L + l_tot), iA, __ATOMIC_RELAXED, __HIP_MEMORY_SCOPE_WORKGROUP);
        __hip_atomic_fetch_add((AS3 unsigned*)(L + l_tot + 4), iB, __ATOMIC_RELAXED, __HIP_MEMORY_SCOPE_WORKGROUP);
        __hip_atomic_fetch_add((AS3 unsigned*)(L + l_tot + 8), iC, __ATOMIC_RELAXED, __HIP_MEMORY_SCOPE_WORKGROUP);
      }
      if (__any(cnt > 255))
        __hip_atomic_fetch_or((AS3 unsigned*)(L + l_tot + 12), 2u, __ATOMIC_RELAXED, __HIP_MEMORY_SCOPE_WORKGROUP);
      pA = iA - cA;
      pB = iB - cB;
      pC = iC - cC;
    }
    __syncthreads();
    const u32x4 tot4 = lds_ld<u32x4>(L, l_tot);
    unsigned e3[4];
#pragma unroll
    for (int k = 0; k < 4; ++k) e3[k] = lds_ld<unsigned>(L, L_SEG + 4 * (64 * k + (tid >> 3)));
    const unsigned flags = __builtin_amdgcn_readfirstlane(tot4[3]);
    if (flags & 1) return;
    if (tid == 511) lds_st<u32x4>(L, L_TOT + 16 * (~it & 1), u32x4{0, 0, 0, 0});
    const unsigned j2 = 2 * (tid & 7);
    u32x3 ra[4], rb[4];
    unsigned cntp[4], ro[4];
#pragma unroll
    for (int k = 0; k < 4; ++k) {
      cntp[k] = (e3[k] >> 19) & 0xfffu;
      ro[k] = ((e3[k] & 0x7ffffu) + (tid & 7)) * 12 + WS_RECS;
      ra[k] = ld12_rec(rs, j2 < cntp[k] ? ro[k] : NO_DATA);
    }
#pragma unroll
    for (int k = 0; k < 4; ++k) rb[k] = ld12_rec(rs, j2 + 16 < cntp[k] ? ro[k] + 96 : NO_DATA);
    SB();
    const unsigned tA = __builtin_amdgcn_readfirstlane(tot4[0]), tB = __builtin_amdgcn_readfirstlane(tot4[1]);
    const unsigned tot[5] = {tA & 0xffffu, tA >> 16, tB & 0xffffu, tB >> 16, (unsigned)__builtin_amdgcn_readfirstlane(tot4[2])};
    unsigned base[6];
    base[0] = 0;
#pragma unroll
    for (int g = 0; g < 5; ++g) base[g + 1] = base[g] + ((tot[g] + 31) & ~31u);
    if (((flags & 2) && np > 16) || base[5] > (unsigned)CAP) {
      np = (np + 1) >> 1;
      __syncthreads();
      continue;
    }
    if (w < 4) {
      constexpr unsigned R8 = L_RAW / 8;
#pragma unroll
      for (int v = 0; v < 3; ++v) {
        const u32x3 t3 = lds_ld<u32x3>(L, L_WV + 16 * v);
        if (v < w) { pA += t3[0]; pB += t3[1]; pC += t3[2]; }
      }
      const unsigned v0 = ((pA & 0xffffu) + (base[0] + R8)) << 3, v1 = ((pA >> 16) - d1 + (base[1] + R8)) << 3;
      const unsigned v2 = ((pB & 0xffffu) - d2 + (base[2] + R8)) << 3, v3 = ((pB >> 16) - d3 + (base[3] + R8)) << 3;
      const unsigned v4 = (pC - d4 + (base[4] + R8)) << 3;
      lds_st<u32x3>(L, L_TBL + 16 * tid, u32x3{(v0 & 0xffffu) | (v1 << 16), (v2 & 0xffffu) | (v3 << 16), v4});
    } else {
      const u32x2 pad = {(unsigned)(L_PD + 16 * BN) << 16, 0};
      const unsigned lo0 = w == 4 ? base[0] + tot[0] : w == 5 ? base[2] + tot[2] : base[4] + tot[4];
      const unsigned lo1 = w == 4 ? base[1] : w == 5 ? base[3] : base[5];
      const unsigned hi0 = w == 4 ? base[1] + tot[1] : base[3] + tot[3], hi1 = w == 4 ? base[2] : base[4];
      const unsigned slot = (hf ? hi0 : lo0) + r;
      if (w < 7 && slot < (hf ? (w == 6 ? 0u : hi1) : lo1)) lds_st<u32x2>(L, L_RAW + 8 * slot, pad);
      if (tid - 256 < PADS) lds_st<u32x2>(L, L_RAW + 8 * (base[5] + tid - 256), pad);
    }

    const int T = base[5] >> 5;
    const int q4 = T >> 2, r4 = T & 3, ws4 = w & 3;
    const int sa4 = ws4 * q4 + (ws4 < r4 ? ws4 : r4), sn4 = q4 + (ws4 < r4 ? 1 : 0);
    const int nold = (sn4 * 44 + 32) >> 6;
    const int ta = w < 4 ? sa4 : sa4 + nold;
    const int tb = w < 4 ? sa4 + nold : sa4 + sn4;
    const int tg1 = base[1] >> 5, tg2 = base[2] >> 5, tg3 = base[3] >> 5, tg4 = base[4] >> 5;

    const char* pos6 = (const char*)pos;
    const rsrc_t h_rows = __builtin_amdgcn_make_buffer_rsrc((void*)((const char*)h - 3072), 128, 0xffffffffu, 0x00020000);
    const unsigned ringb = L_RING + w * 4096;
    unsigned aq[4];
#pragma unroll
    for (int s = 0; s < 4; ++s) aq[s] = ringb + r * 128 + (((2 * s + hf) ^ ((r >> 1) & 7)) << 4);
    const unsigned dc0 = ((lane & 7) ^ ((lane >> 4) & 7)) << 4, dc1 = ((lane & 7) ^ ((4 + (lane >> 4)) & 7)) << 4;
    const unsigned Sb = L_S + w * S_WAVE;
    const unsigned pi_e = (r & 0x13) | ((r & 4) << 1) | ((r & 8) >> 1);
    const unsigned sa_c = Sb + 2 * pi_e;
    const unsigned s_rd = Sb + r * 80 + 16 * hf;
    const int g16 = lane >> 4, tq = (lane & 15) >> 2, tp = lane & 3, tu = g16 & 1, th = g16 >> 1;
    const unsigned s_tr = Sb + (4 * (4 * th + tq) + 3) * 80 + 2 * (16 * tu + 8 * (tp & 1) + 4 * (tp >> 1));
    const unsigned acc_l = L_ACC + hf * ACC_NODE + 4 * r;
    bf16x8 wq[2][4];
    auto group_of = [&](int t) {
      int g;
      asm("s_cmp_ge_i32 %1, %2\n\ts_cselect_b32 %0, 1, 0\n\ts_cmp_ge_i32 %1, %3\n\ts_addc_u32 %0, %0, 0\n\t"
          "s_cmp_ge_i32 %1, %4\n\ts_addc_u32 %0, %0, 0\n\ts_cmp_ge_i32 %1, %5\n\ts_addc_u32 %0, %0, 0"
          : "=&s"(g) : "s"(t), "s"(tg1), "s"(tg2), "s"(tg3), "s"(tg4) : "scc");
      return g;
    };
    auto group_end = [&](int t) {
      int e;
      asm("s_cmp_gt_i32 %5, %1\n\ts_cselect_b32 %0, %5, %6\n\ts_cmp_gt_i32 %4, %1\n\ts_cselect_b32 %0, %4, %0\n\t"
          "s_cmp_gt_i32 %3, %1\n\ts_cselect_b32 %0, %3, %0\n\ts_cmp_gt_i32 %2, %1\n\ts_cselect_b32 %0, %2, %0"
          : "=&s"(e) : "s"(t), "s"(tg1), "s"(tg2), "s"(tg3), "s"(tg4), "s"(T) : "scc");
      return e;
    };
    int gend = group_end(ta);
    int tflush = -1;

    bf16x8 bxA0, bxA1, bxB0, bxB1;
    auto load_bx = [&](int gg) {
      const u32x2 p0 = lds_ld<u32x2>(L, L_PFR + gg * 1024 + lane * 8), p1 = lds_ld<u32x2>(L, L_PFR + gg * 1024 + 512 + lane * 8);
      bxA0 = as_frag(u32x4{p0[0], p0[1], hf ? 0u : cw[0][0], hf ? 0u : cw[0][1]});
      bxB0 = as_frag(u32x4{p0[0], p0[1], hf ? cw[0][0] : 0u, hf ? cw[0][1] : 0u});
      bxA1 = as_frag(u32x4{p1[0], p1[1], hf ? 0u : cw[1][0], hf ? 0u : cw[1][1]});
      bxB1 = as_frag(u32x4{p1[0], p1[1], hf ? cw[1][0] : 0u, hf ? cw[1][1] : 0u});
      asm volatile("" : "+v"(bxA0), "+v"(bxB0), "+v"(bxA1), "+v"(bxB1));
    };
#pragma unroll
    for (int t = 0; t < 2; ++t)
#pragma unroll
      for (int s = 0; s < 4; ++s) {
        const unsigned o = L_W + 8192 + (16 * s + 8 * th + tq) * 128 + (32 * t + 16 * tu + 4 * tp) * 2;
        const u32x2 b0 = lds_tr(L, o), b1 = lds_tr(L, o + 512);
        wq[t][s] = as_frag(u32x4{b0[0], b0[1], b1[0], b1[1]});
      }
    load_bx(group_of(ta));
    tiles_init();
    asm volatile("v_mov_b32 v167, %0\n\tv_mov_b32 v169, %1\n\tv_mov_b32 v171, %0\n\tv_mov_b32 v173, %1" :: "v"(dc0), "v"(dc1));
    set_m0(LB + ringb);
    __syncthreads();

    {
      auto second = [](unsigned w0, unsigned eahi) { return (((w0 >> 20) & 7) * 320) | eahi; };
      const unsigned tb0 = L_TBL + 16 * (tid >> 3);
      unsigned a0[4], a1[4], r8[4];
#pragma unroll
      for (int k = 0; k < 4; ++k) {
        r8[k] = 8 * j2 - ((e3[k] >> 31) << 3);
        a0[k] = lds_ld<u16>(L, tb0 + 1024 * k + ((ra[k][0] >> 22) & 14)) + r8[k];
        a1[k] = lds_ld<u16>(L, tb0 + 1024 * k + ((ra[k][1] >> 22) & 14)) + r8[k];
      }
#pragma unroll
      for (int k = 0; k < 4; ++k) {
        if (j2 < cntp[k] && (int)r8[k] >= 0) lds_st<u32x2>(L, a0[k], u32x2{ra[k][0], second(ra[k][0], ra[k][2] << 16)});
        if (j2 + 1 < cntp[k]) lds_st<u32x2>(L, a1[k] + 8, u32x2{ra[k][1], second(ra[k][1], ra[k][2] & 0xffff0000u)});
      }
#pragma unroll
      for (int k = 0; k < 4; ++k) {
        if (j2 + 16 < cntp[k]) {
          const unsigned b0 = lds_ld<u16>(L, tb0 + 1024 * k + ((rb[k][0] >> 22) & 14)) + r8[k];
          const unsigned b1 = lds_ld<u16>(L, tb0 + 1024 * k + ((rb[k][1] >> 22) & 14)) + r8[k];
          lds_st<u32x2>(L, b0 + 128, u32x2{rb[k][0], second(rb[k][0], rb[k][2] << 16)});
          if (j2 + 17 < cntp[k]) lds_st<u32x2>(L, b1 + 136, u32x2{rb[k][1], second(rb[k][1], rb[k][2] & 0xffff0000u)});
        }
      }
#pragma unroll
      for (int k = 0; k < 4; ++k) {
        const unsigned odd = e3[k] >> 31;
        for (unsigned q = (tid & 7) + 32; q < cntp[k]; q += 8) {
          const unsigned pb = ((e3[k] & 0x7ffffu) + (q >> 1)) * 12 + WS_RECS;
          const unsigned w0 = ld4_rec(rs, pb + 4 * (q & 1)), eav = ld2_rec(rs, pb + 8 + 2 * (q & 1));
          const unsigned a = lds_ld<u16>(L, tb0 + 1024 * k + ((w0 >> 22) & 14));
          lds_st<u32x2>(L, a + 8 * (q - odd), u32x2{w0, second(w0, eav << 16)});
        }
      }
    }
    __syncthreads();

    if (pa + np >= NP) {
      const char* wh = karg<const char>(4);
      dma_piece(wh + w * 2048, lane * 16, LB + L_W + w * 2048);
      dma_piece(wh + w * 2048 + 1024, lane * 16, LB + L_W + w * 2048 + 1024);
    }
    if (ta < tb) {
      auto flush = [&](int gg) {
        const unsigned a = LB + acc_l + gg * (8 * ACC_NODE);
        if (lds_lock(LB + L_LOCK + 4 * gg) != 0) acc_gather(a, a + 2 * ACC_NODE, a + 4 * ACC_NODE, a + 6 * ACC_NODE);
        acc_store(a, a + 2 * ACC_NODE, a + 4 * ACC_NODE, a + 6 * ACC_NODE);
        lds_st<unsigned>(L, L_LOCK + 4 * gg, 2u);
        asm volatile("" ::: "memory");
      };
      auto flush_last = [&](int gg) {
        const unsigned a = LB + acc_l + gg * (8 * ACC_NODE);
        if (lds_lock(LB + L_LOCK + 4 * gg) != 0) acc_gather_last(a, a + 2 * ACC_NODE, a + 4 * ACC_NODE, a + 6 * ACC_NODE);
        acc_store_last(a, a + 2 * ACC_NODE, a + 4 * ACC_NODE, a + 6 * ACC_NODE);
        lds_st<unsigned>(L, L_LOCK + 4 * gg, 2u);
        asm volatile("" ::: "memory");
      };
      unsigned a_idx = LB + L_RAW + ta * 256 + (lane >> 3) * 8, a_raw = L_RAW + ta * 256 + lane * 8;
      auto raw_load = [&](int dt) {
        const unsigned a = a_raw + dt * 256;
        return Raw{lds_ld<u16>(L, a), lds_ld<u16>(L, a + 2), lds_ld<unsigned>(L, a + 4), lds_ld<u16>(L, a + 4)};
      };
      auto pos_load = [&](const Raw& x) {
        asm volatile("global_load_dwordx2 v[174:175], %0, %1" :: "v"(x.src * 6u), "s"(pos6) : "memory");
      };
      auto pd_load = [&](const Raw& x) { return lds_ld<f32x4>(L, x.pda); };
      auto prep = [&](const Raw& x, const f32x4& pd) {
        float xs, ys, zs;
        asm volatile("v_lshlrev_b32 %0, 16, v174\n\tv_and_b32 %1, 0xffff0000, v174\n\tv_lshlrev_b32 %2, 16, v175"
                     : "=v"(xs), "=v"(ys), "=v"(zs));
        const f32x2 dxy = f32x2{pd[0], pd[1]} - f32x2{xs, ys};
        unsigned wa = cvt_pk(dxy[0], dxy[1]), wb = cvt_pk(pd[2] - zs, pd[3]);
        const float dzr = bf_lo(wb);
        const bf16x2v wav = __builtin_bit_cast(bf16x2v, wa);
        const float d2 = __builtin_amdgcn_fdot2_f32_bf16(wav, wav, dzr * dzr, false);
        Prep o;
        o.de = cvt_pk(d2, as_f(x.w1));
        asm volatile("" : "+v"(o.de), "+v"(wa), "+v"(wb));
        const unsigned P = x.n320 + sa_c, Q = P + 160;
        const auto sa = __builtin_amdgcn_permlane32_swap(P, Q, false, false);
        const auto sd = __builtin_amdgcn_permlane32_swap(wa, wb, false, false);
        o.saA = sa[0]; o.saB = sa[1]; o.sdA = sd[0]; o.sdB = sd[1];
        return o;
      };

      struct Ops { bf16x8 q0, q1, q2, q3, as0, as1, ax; };
      auto read_ops = [&](unsigned sa, unsigned sd, unsigned de) {
        Ops o;
        o.q0 = lds_ld<bf16x8>(L, aq[0]); o.q1 = lds_ld<bf16x8>(L, aq[1]);
        o.q2 = lds_ld<bf16x8>(L, aq[2]); o.q3 = lds_ld<bf16x8>(L, aq[3]);
        lds_st<u16>(L, sa, (u16)sd);
        lds_st<u16>(L, sa + 80, (u16)(sd >> 16));
        const u32x2 oh = lds_tr(L, s_tr);
        o.as0 = lds_ld<bf16x8>(L, s_rd);
        o.as1 = lds_ld<bf16x8>(L, s_rd + 32);
        lds_st<u16>(L, sa, (u16)0);
        lds_st<u16>(L, sa + 80, (u16)0);
        unsigned de2;
        asm volatile("v_mov_b32 %0, %1" : "=v"(de2) : "v"(de));
        o.ax = as_frag(u32x4{oh[0], oh[1], de, de2});
        return o;
      };
      auto alpha = [&](int t, const Ops& c, const bf16x8& asl, const bf16x8& bx1, auto&& mid) {
        alpha1(asl);
        if (__builtin_expect(t == tflush, 0)) flush(group_of(t - 1));
        alpha2(c.ax, bx1, c.q0, wq[1][0]);
        SB(); mid(); SB();
        alpha3(c.q1, wq[1][1], c.q2, wq[1][2], c.q3, wq[1][3], c.as0);
      };
      auto beta = [&](const Ops& c, const Ops& n, const bf16x8& bx0n, auto&& mid) {
        beta1(c.as1);
        beta2(n.ax, bx0n, n.q0, wq[0][0]);
        SB(); mid(); SB();
        beta3(n.q1, wq[0][1], n.q2, wq[0][2], n.q3, wq[0][3], c.as0);
      };
      auto next_group = [&](int t) {
        if (__builtin_expect(t == gend, 0)) { gend = group_end(t); tflush = t; load_bx(group_of(t)); }
      };

      rows_index<0>(a_idx);
      rows_gather(h_rows);
      Raw rw = raw_load(0);
      pos_load(rw);
      f32x4 pd = pd_load(rw);
      SB();
      wait_vm0();
      Prep pr = prep(rw, pd);
      rw = raw_load(2);
      Ops A = read_ops(pr.saA, pr.sdA, pr.de), B;
#pragma unroll
      for (int j = 0; j < 8; ++j) B.as1[j] = 0;
      asm volatile("" : "+v"(B.as1));
      const int glast = group_of(tb - 1);
      rows_index<256>(a_idx);
      chain_y0(A.ax, bxA0, A.q0, wq[0][0], A.q1, wq[0][1], A.q2, wq[0][2], A.q3, wq[0][3]);
      rows_gather(h_rows);
      int t = ta;
      auto tile_a = [&]() {
        alpha(t, A, B.as1, bxA1, [&]() { pos_load(rw); });
        wait_vm0();
        next_group(t + 1);
        rows_index<512>(a_idx);
        B = read_ops(pr.saB, pr.sdB, pr.de);
        pd = pd_load(rw);
        SB();
        beta(A, B, bxB0, [&]() { rows_gather(h_rows); });
      };
      for (; t + 2 <= tb; t += 2) {
        tile_a();
        alpha(t + 1, B, A.as1, bxB1, [&]() { pr = prep(rw, pd); });
        wait_vm0();
        next_group(t + 2);
        rows_index<768>(a_idx);
        A = read_ops(pr.saA, pr.sdA, pr.de);
        rw = raw_load(4);
        SB();
        beta(B, A, bxA0, [&]() { rows_gather(h_rows); });
        a_idx += 512;
        a_raw += 512;
      }
      if (t < tb) {
        tile_a();
        last_block(A.as1);
      } else {
        last_block(B.as1);
      }
      flush_last(glast);
    } else {
      wait_vm0();
    }
    pa += np;
    if (pa >= NP) break;
    wait_vm0();
    __syncthreads();
  }
  __syncthreads();

  u16* out = karg<u16>(6);
  int tid = tid_;
  asm volatile("" : "+v"(tid));
  const int lane = tid & 63, r = lane & 31, hf = lane >> 5;
  if (w < 4) {
    const int g16 = lane >> 4, tq = (lane & 15) >> 2, tp = lane & 3, tu = g16 & 1, th = g16 >> 1;
    const int u = w >> 1, t = w & 1;
    const int nl = 32 * u + r, nlc = nl < BN ? nl : BN - 1;
    const unsigned hrow = L_H + nlc * 128;
    f32x16 d;
#pragma unroll
    for (int i = 0; i < 16; ++i) d[i] = 0.f;
    const unsigned w_tr = L_W + (8 * th + tq) * 128 + (32 * t + 16 * tu + 4 * tp) * 2;
#pragma unroll
    for (int s = 0; s < 8; ++s) {
      const u32x2 a0 = lds_tr(L, w_tr + s * 2048), a1 = lds_tr(L, w_tr + s * 2048 + 512);
      const bf16x8 a = as_frag(u32x4{a0[0], a0[1], a1[0], a1[1]});
      bf16x8 bb;
      if (s < 4) {
        bb = lds_ld<bf16x8>(L, hrow + 32 * s + 16 * hf);
      } else {
        const unsigned o = L_ACC + nlc * ACC_NODE + 3 * 256 + (16 * (s - 4) + 8 * hf) * 4;
        const f32x4 m0 = lds_ld<f32x4>(L, o) * NLN2, m1 = lds_ld<f32x4>(L, o + 16) * NLN2;
        bb = as_frag(u32x4{cvt_pk(m0[0], m0[1]), cvt_pk(m0[2], m0[3]), cvt_pk(m1[0], m1[1]), cvt_pk(m1[2], m1[3])});
      }
      d = mfma(a, bb, d);
    }
    if (nl < BN) {
#pragma unroll
      for (int q = 0; q < 4; ++q) {
        const int f = 32 * t + 8 * q + 4 * hf;
        const u32x2 hv = lds_ld<u32x2>(L, hrow + 2 * f);
        u32x2 o;
#pragma unroll
        for (int k = 0; k < 2; ++k) {
          const unsigned pb = cvt_pk(d[4 * q + 2 * k], d[4 * q + 2 * k + 1]);
          const f32x2 x = {bf_lo(pb), bf_hi(pb)}, y = x * NLOG2E;
          const f32x2 den = f32x2{__builtin_amdgcn_exp2f(y[0]), __builtin_amdgcn_exp2f(y[1])} + 1.0f;
          const f32x2 act = x * f32x2{__builtin_amdgcn_rcpf(den[0]), __builtin_amdgcn_rcpf(den[1])};
          const unsigned ab = cvt_pk(act[0], act[1]);
          const f32x2 sum = f32x2{bf_lo(hv[k]), bf_hi(hv[k])} + f32x2{bf_lo(ab), bf_hi(ab)};
          o[k] = cvt_pk(sum[0], sum[1]);
        }
        *(u32x2*)(out + (size_t)(n0 + nl) * 64 + f) = o;
      }
    }
  } else {
    const int idx = tid - 256, nc = idx >> 1, half = idx & 1;
    const int ncc = nc < 3 * BN ? nc : 3 * BN - 1;
    const int n = ncc / 3, c = ncc - 3 * n;
    const unsigned o = L_ACC + n * ACC_NODE + c * 256 + half * 128;
    float s = 0.f;
#pragma unroll
    for (int k = 0; k < 8; ++k) {
      const f32x4 v = lds_ld<f32x4>(L, o + 16 * k), x = lds_ld<f32x4>(L, L_WX + half * 128 + 16 * k);
      s += v[0] * x[0] + v[1] * x[1] + v[2] * x[2] + v[3] * x[3];
    }
    s += __builtin_bit_cast(float, __builtin_amdgcn_update_dpp(0, __builtin_bit_cast(int, s), 0xb1, 0xf, 0xf, true));
    if (nc < 3 * BN && half == 0) {
      const float p0 = lds_ld<float>(L, L_PD + 16 * n + 4 * c);
      out[(size_t)NN * 64 + (size_t)n0 * 3 + nc] = (u16)cvt_pk(p0 + bf_round(s * NLN2), 0.f);
    }
  }
  if (blockIdx.x == 0 && tid == 511)
    __builtin_amdgcn_raw_buffer_store_b64(lds_ld<u32x2>(L, L_TAG + 8), ws_rsrc(karg<const char>(5)), WS_CTL, 0, 16);
}

}

extern "C" void kernel_launch(void* const* d_in, const int* in_sizes, int n_in, void* d_out, int out_size, void* d_ws, size_t ws_size,
                              hipStream_t stream) {
  const u16* h = (const u16*)d_in[0];
  const u16* pos = (const u16*)d_in[1];
  const u16* ea = (const u16*)d_in[2];
  const int* src = (const int*)d_in[3];
  const int* dst = (const int*)d_in[4];
  const u16* we = (const u16*)d_in[5];
  const u16* wx = (const u16*)d_in[6];
  const u16* wh = (const u16*)d_in[7];
  egnn_kernel<<<NBK, 512, 0, stream>>>(h, pos, we, wx, wh, (char*)d_ws, (u16*)d_out, src, dst, ea);
}
```

```cpp
#include <hip/hip_runtime.h>
#include <hip/hip_bf16.h>
#include <stdint.h>

#define DI __device__ __forceinline__
#define AS3 __attribute__((address_space(3)))
#define SB() __builtin_amdgcn_sched_barrier(0)

typedef short bf16x8 __attribute__((ext_vector_type(8)));
typedef short s16x4 __attribute__((ext_vector_type(4)));
typedef float f32x16 __attribute__((ext_vector_type(16)));
typedef float f32x4 __attribute__((ext_vector_type(4)));
typedef float f32x2 __attribute__((ext_vector_type(2)));
typedef unsigned u32x4 __attribute__((ext_vector_type(4)));
typedef unsigned u32x3 __attribute__((ext_vector_type(3)));
typedef unsigned u32x2 __attribute__((ext_vector_type(2)));
typedef int i32x4 __attribute__((ext_vector_type(4)));
typedef __bf16 bf16x2v __attribute__((ext_vector_type(2)));
typedef unsigned short u16;

namespace {

constexpr int NN = 10000;
constexpr int NE = 640000;
constexpr int BN = 40;
constexpr int NBK = NN / BN;
constexpr int NP = NBK;
constexpr int CH = NE / NP;
static_assert(CH == 5 * 512 && CH < 4096, "five edges per thread, 12-bit run starts");
constexpr float NLOG2E = -1.4426950408889634f;
constexpr float NLN2 = -0.6931471805599453f;
constexpr unsigned SPIN_LIMIT = 1u << 20;
constexpr unsigned NO_DATA = 0xffffff00u;
constexpr int POLL_SLEEP = 4;
constexpr int CODE_BYTES = 18432;

constexpr unsigned WS_CTL = 0;
constexpr unsigned WS_ENT = 256;
constexpr unsigned WS_RECS = WS_ENT + NP * 4096;
constexpr unsigned REC_STRIDE = CH * 6;
constexpr unsigned WS_END = WS_RECS + NP * REC_STRIDE;
static_assert(WS_RECS % 128 == 0 && REC_STRIDE % 128 == 0, "no cache line holds records of two producers");

constexpr int CAP = 2880;
constexpr int PADS = 160;
constexpr int L_S = 0;
constexpr int S_WAVE = 2560;
constexpr int L_PD = 20480;
constexpr int L_WT = 21248;
constexpr int L_TOT = L_WT + 16;
constexpr int L_LOCK = 21312;
constexpr int L_TAG = 21344;
constexpr int L_WV = 21376;
constexpr int L_WX = 21504;
constexpr int L_TBL = 21760;
constexpr int L_RAW = L_TBL + 4096;
constexpr int L_PFR = L_RAW + (CAP + PADS) * 8;
constexpr int L_RING = L_PFR + 5120;
constexpr int L_ACC = L_RING + 8 * 4096;
constexpr int ACC_NODE = 1040;
constexpr int L_W = L_ACC + BN * ACC_NODE;
constexpr int L_H = L_W + 16384;
constexpr int L_SEG = L_H + BN * 128;
constexpr int L_END = L_SEG + 1024;
static_assert(L_PD % 1024 == 0, "group id is read from bits 7..9 of the pos address");
static_assert(L_RAW + (CAP + PADS) * 8 < 65536 && L_RAW >= 8 * CH && CAP >= CH + 5 * 31, "run table holds 16-bit LDS addresses");
static_assert(L_PFR % 16 == 0 && L_RING % 16 == 0 && L_ACC % 16 == 0 && L_W % 16 == 0 && L_END <= 163840, "LDS map");

DI unsigned cvt_pk(float a, float b) { f32x2 v = {a, b}; return __builtin_bit_cast(unsigned, __builtin_convertvector(v, bf16x2v)); }
DI float as_f(unsigned u) { return __builtin_bit_cast(float, u); }
DI float bf_lo(unsigned u) { return __builtin_bit_cast(float, u << 16); }
DI float bf_hi(unsigned u) { return __builtin_bit_cast(float, u & 0xffff0000u); }
DI float bf_round(float x) { return bf_lo(cvt_pk(x, x)); }
DI f32x16 mfma(bf16x8 a, bf16x8 b, f32x16 c) { return __builtin_amdgcn_mfma_f32_32x32x16_bf16(a, b, c, 0, 0, 0); }
DI bf16x8 as_frag(u32x4 v) { return __builtin_bit_cast(bf16x8, v); }

DI unsigned wave_incl_scan(unsigned x) {
  x += __builtin_amdgcn_update_dpp(0, (int)x, 0x111, 0xf, 0xf, true);
  x += __builtin_amdgcn_update_dpp(0, (int)x, 0x112, 0xf, 0xf, true);
  x += __builtin_amdgcn_update_dpp(0, (int)x, 0x114, 0xf, 0xf, true);
  x += __builtin_amdgcn_update_dpp(0, (int)x, 0x118, 0xf, 0xf, true);
  x += __builtin_amdgcn_update_dpp(0, (int)x, 0x142, 0xa, 0xf, false);
  x += __builtin_amdgcn_update_dpp(0, (int)x, 0x143, 0xc, 0xf, false);
  return x;
}

template <typename T> DI T lds_ld(AS3 char* L, unsigned off) { return *(AS3 T*)(L + off); }
template <typename T> DI void lds_st(AS3 char* L, unsigned off, T v) { *(AS3 T*)(L + off) = v; }
DI u32x2 lds_tr(AS3 char* L, unsigned off) {
  return __builtin_bit_cast(u32x2, __builtin_amdgcn_ds_read_tr16_b64_v4i16((AS3 s16x4*)(L + off)));
}

DI const char* uni_ptr(const char* p) {
  const unsigned long long v = (unsigned long long)p;
  const unsigned lo = __builtin_amdgcn_readfirstlane((int)(unsigned)v), hi = __builtin_amdgcn_readfirstlane((int)(unsigned)(v >> 32));
  return (const char*)(__attribute__((address_space(1))) const char*)(((unsigned long long)hi << 32) | lo);
}
DI void dma_piece(const char* base_, unsigned voff, unsigned ldsaddr_) {
  const char* base = uni_ptr(base_);
  const unsigned ldsaddr = __builtin_amdgcn_readfirstlane((int)ldsaddr_);
  unsigned keep;
  asm volatile("s_mov_b32 %0, m0\n\ts_mov_b32 m0, %3\n\ts_nop 4\n\tglobal_load_lds_dwordx4 %1, %2\n\ts_mov_b32 m0, %0"
               : "=&s"(keep) : "v"(voff), "s"(base), "s"(ldsaddr) : "memory");
}
DI void set_m0(unsigned ldsaddr) { asm volatile("s_mov_b32 m0, %0\n\ts_nop 0" :: "s"(ldsaddr) : "memory"); }
typedef __amdgpu_buffer_rsrc_t rsrc_t;
template <int OFF> DI void rows_index(unsigned a) {
  asm volatile("ds_read_u16 v166, %0 offset:%1\n\tds_read_u16 v168, %0 offset:%2\n\tds_read_u16 v170, %0 offset:%3\n\tds_read_u16 v172, %0 offset:%4"
               :: "v"(a), "i"(OFF), "i"(OFF + 64), "i"(OFF + 128), "i"(OFF + 192) : "memory");
}
DI void rows_gather(rsrc_t rows) {
  asm volatile("s_waitcnt lgkmcnt(0)\n\t"
               "buffer_load_dwordx4 v[166:167], %0, %1 idxen offen lds\n\t"
               "buffer_load_dwordx4 v[168:169], %0, %2 idxen offen offset:1024 lds\n\t"
               "buffer_load_dwordx4 v[170:171], %0, %3 idxen offen offset:2048 lds\n\t"
               "buffer_load_dwordx4 v[172:173], %0, 0 idxen offen offset:3072 lds"
               :: "s"(rows), "s"(3072u), "s"(2048u), "s"(1024u) : "memory");
}
DI rsrc_t ws_rsrc(const char* ws) { return __builtin_amdgcn_make_buffer_rsrc((void*)ws, 0, WS_END, 0x00020000); }
DI u32x4 ld16_dev(rsrc_t r, unsigned off) { return __builtin_amdgcn_raw_buffer_load_b128(r, off, 0, 16); }
DI u32x3 ld12_rec(rsrc_t r, unsigned off) { return __builtin_amdgcn_raw_buffer_load_b96(r, off, 0, 16); }
DI unsigned ld4_rec(rsrc_t r, unsigned off) { return __builtin_amdgcn_raw_buffer_load_b32(r, off, 0, 16); }
DI unsigned ld2_rec(rsrc_t r, unsigned off) { return __builtin_amdgcn_raw_buffer_load_b16(r, off, 0, 16); }
DI u32x2 ld8_dev(rsrc_t r, unsigned off) { return __builtin_amdgcn_raw_buffer_load_b64(r, off, 0, 16); }
DI void st16_dev(rsrc_t r, unsigned off, u32x4 v) { __builtin_amdgcn_raw_buffer_store_b128(v, r, off, 0, 16); }
DI void wait_vm0() { asm volatile("s_waitcnt vmcnt(0)" ::: "memory"); }

struct Prep {
  unsigned sdA, sdB;
  unsigned saA, saB;
  unsigned de;
};
struct Raw { unsigned src, pda, w1, n320; };

#pragma clang diagnostic ignored "-Winline-asm"
#define T4(op, d, s0, s1, s2, s3, d1, d2, d3) op " v" #d ", v" #s0 "\n\t" op " v" #d1 ", v" #s1 "\n\t" op " v" #d2 ", v" #s2 "\n\t" op " v" #d3 ", v" #s3 "\n\t"
#define EXP_LO(a, b, c, d) T4("v_exp_f32", 184, a, b, c, d, 185, 186, 187)
#define EXP_HI(a, b, c, d) T4("v_exp_f32", 188, a, b, c, d, 189, 190, 191)
#define RCP_LO T4("v_rcp_f32", 184, 184, 185, 186, 187, 185, 186, 187)
#define RCP_HI T4("v_rcp_f32", 188, 188, 189, 190, 191, 189, 190, 191)
#define PKA(a, b) "v_pk_add_f32 v[" #a ":" #b "], v[" #a ":" #b "], 1.0 op_sel_hi:[1,0]\n\t"
#define ADD1 PKA(184, 185) PKA(186, 187) PKA(188, 189) PKA(190, 191)
#define PKM(a, b, c, d) "v_pk_mul_f32 v[" #a ":" #b "], v[" #a ":" #b "], v[" #c ":" #d "]\n\t"
#define MULY(a0, a1, a2, a3, a4, a5, a6, a7) PKM(184, 185, a0, a1) PKM(186, 187, a2, a3) PKM(188, 189, a4, a5) PKM(190, 191, a6, a7)
#define CVT(d, a, b) "v_cvt_pk_bf16_f32 v" #d ", v" #a ", v" #b "\n\t"
#define PACK(d0, d1, d2, d3) CVT(d0, 184, 185) CVT(d1, 186, 187) CVT(d2, 188, 189) CVT(d3, 190, 191)
#define MF(acc, a, b) "v_mfma_f32_32x32x16_bf16 " acc ", " a ", " b ", " acc "\n\t"
#define MF0(acc, a, b) "v_mfma_f32_32x32x16_bf16 " acc ", " a ", " b ", 0\n\t"
#define Y0 "v[192:207]"
#define Y1 "v[208:223]"
#define ACC0 "v[224:239]"
#define ACC1 "v[240:255]"
#define X0 "v[176:179]"
#define X1 "v[180:183]"

DI void tiles_init() {
  asm volatile(
               "v_mov_b64 v[224:225], 0\n\t"
               "v_mov_b64 v[226:227], 0\n\t"
               "v_mov_b64 v[228:229], 0\n\t"
               "v_mov_b64 v[230:231], 0\n\t"
               "v_mov_b64 v[232:233], 0\n\t"
               "v_mov_b64 v[234:235], 0\n\t"
               "v_mov_b64 v[236:237], 0\n\t"
               "v_mov_b64 v[238:239], 0\n\t"
               "v_mov_b64 v[240:241], 0\n\t"
               "v_mov_b64 v[242:243], 0\n\t"
               "v_mov_b64 v[244:245], 0\n\t"
               "v_mov_b64 v[246:247], 0\n\t"
               "v_mov_b64 v[248:249], 0\n\t"
               "v_mov_b64 v[250:251], 0\n\t"
               "v_mov_b64 v[252:253], 0\n\t"
               "v_mov_b64 v[254:255], 0\n\t"
               "v_mov_b64 v[180:181], 0\n\t"
               "v_mov_b64 v[182:183], 0\n\t"
               "s_nop 1" ::: "v166", "v255");
}
DI void alpha1(const bf16x8& asl) {
  asm volatile(EXP_LO(192, 193, 194, 195) MF(ACC1, "%[asl]", X1) EXP_HI(196, 197, 198, 199) :: [asl] "v"(asl));
}
DI void alpha2(const bf16x8& ax, const bf16x8& bx, const bf16x8& q0, const bf16x8& w0) {
  asm volatile(ADD1 MF0(Y1, "%[ax]", "%[bx]") RCP_LO MF(Y1, "%[q0]", "%[w0]") RCP_HI
               MULY(192, 193, 194, 195, 196, 197, 198, 199) PACK(176, 177, 178, 179)
               :: [ax] "v"(ax), [bx] "v"(bx), [q0] "v"(q0), [w0] "v"(w0));
}
DI void alpha3(const bf16x8& q1, const bf16x8& w1, const bf16x8& q2, const bf16x8& w2, const bf16x8& q3, const bf16x8& w3, const bf16x8& as0) {
  asm volatile(MF(Y1, "%[q1]", "%[w1]") EXP_LO(200, 201, 202, 203) MF(Y1, "%[q2]", "%[w2]") EXP_HI(204, 205, 206, 207) ADD1
               MF(Y1, "%[q3]", "%[w3]") RCP_LO MF(ACC0, "%[as]", X0) RCP_HI
               MULY(200, 201, 202, 203, 204, 205, 206, 207) PACK(180, 181, 182, 183)
               :: [q1] "v"(q1), [w1] "v"(w1), [q2] "v"(q2), [w2] "v"(w2), [q3] "v"(q3), [w3] "v"(w3), [as] "v"(as0));
}
DI void beta1(const bf16x8& as1) {
  asm volatile(EXP_LO(208, 209, 210, 211) MF(ACC0, "%[as]", X1) EXP_HI(212, 213, 214, 215) ADD1 :: [as] "v"(as1));
}
DI void beta2(const bf16x8& ax, const bf16x8& bx, const bf16x8& q0, const bf16x8& w0) {
  asm volatile(MF0(Y0, "%[ax]", "%[bx]") RCP_LO MF(Y0, "%[q0]", "%[w0]") RCP_HI
               MULY(208, 209, 210, 211, 212, 213, 214, 215) PACK(176, 177, 178, 179)
               :: [ax] "v"(ax), [bx] "v"(bx), [q0] "v"(q0), [w0] "v"(w0));
}
DI void beta3(const bf16x8& q1, const bf16x8& w1, const bf16x8& q2, const bf16x8& w2, const bf16x8& q3, const bf16x8& w3, const bf16x8& as0) {
  asm volatile(MF(Y0, "%[q1]", "%[w1]") EXP_LO(216, 217, 218, 219) MF(Y0, "%[q2]", "%[w2]") EXP_HI(220, 221, 222, 223) ADD1
               MF(Y0, "%[q3]", "%[w3]") RCP_LO MF(ACC1, "%[as]", X0) RCP_HI
               MULY(216, 217, 218, 219, 220, 221, 222, 223) PACK(180, 181, 182, 183)
               :: [q1] "v"(q1), [w1] "v"(w1), [q2] "v"(q2), [w2] "v"(w2), [q3] "v"(q3), [w3] "v"(w3), [as] "v"(as0));
}
DI void chain_y0(const bf16x8& ax, const bf16x8& bx, const bf16x8& q0, const bf16x8& w0, const bf16x8& q1, const bf16x8& w1,
                 const bf16x8& q2, const bf16x8& w2, const bf16x8& q3, const bf16x8& w3) {
  asm volatile("s_nop 1\n\t" MF0(Y0, "%[ax]", "%[bx]") MF(Y0, "%[q0]", "%[w0]") MF(Y0, "%[q1]", "%[w1]") MF(Y0, "%[q2]", "%[w2]")
               MF(Y0, "%[q3]", "%[w3]") "s_nop 15"
               :: [ax] "v"(ax), [bx] "v"(bx), [q0] "v"(q0), [w0] "v"(w0), [q1] "v"(q1), [w1] "v"(w1), [q2] "v"(q2), [w2] "v"(w2),
                  [q3] "v"(q3), [w3] "v"(w3));
}
DI void last_block(const bf16x8& as1) { asm volatile("s_nop 1\n\t" MF(ACC1, "%[as]", X1) :: [as] "v"(as1)); }
DI void acc_gather(unsigned a0, unsigned a1, unsigned a2, unsigned a3) {
  f32x2 t0, t1, t2, t3, t4, t5, t6, t7;
  asm volatile(
               "ds_read2_b32 %[t0], %[a0] offset0:0 offset1:64\n\t"
               "ds_read2_b32 %[t1], %[a0] offset0:128 offset1:192\n\t"
               "ds_read2_b32 %[t2], %[a0] offset0:32 offset1:96\n\t"
               "ds_read2_b32 %[t3], %[a0] offset0:160 offset1:224\n\t"
               "ds_read2_b32 %[t4], %[a1] offset0:0 offset1:64\n\t"
               "ds_read2_b32 %[t5], %[a1] offset0:128 offset1:192\n\t"
               "ds_read2_b32 %[t6], %[a1] offset0:32 offset1:96\n\t"
               "ds_read2_b32 %[t7], %[a1] offset0:160 offset1:224\n\t"
               "s_waitcnt lgkmcnt(4)\n\t"
               "v_pk_add_f32 v[224:225], v[224:225], %[t0]\n\t"
               "v_pk_add_f32 v[226:227], v[226:227], %[t1]\n\t"
               "v_pk_add_f32 v[240:241], v[240:241], %[t2]\n\t"
               "v_pk_add_f32 v[242:243], v[242:243], %[t3]\n\t"
               "s_waitcnt lgkmcnt(0)\n\t"
               "v_pk_add_f32 v[228:229], v[228:229], %[t4]\n\t"
               "v_pk_add_f32 v[230:231], v[230:231], %[t5]\n\t"
               "v_pk_add_f32 v[244:245], v[244:245], %[t6]\n\t"
               "v_pk_add_f32 v[246:247], v[246:247], %[t7]\n\t"
               "ds_read2_b32 %[t0], %[a2] offset0:0 offset1:64\n\t"
               "ds_read2_b32 %[t1], %[a2] offset0:128 offset1:192\n\t"
               "ds_read2_b32 %[t2], %[a2] offset0:32 offset1:96\n\t"
               "ds_read2_b32 %[t3], %[a2] offset0:160 offset1:224\n\t"
               "ds_read2_b32 %[t4], %[a3] offset0:0 offset1:64\n\t"
               "ds_read2_b32 %[t5], %[a3] offset0:128 offset1:192\n\t"
               "ds_read2_b32 %[t6], %[a3] offset0:32 offset1:96\n\t"
               "ds_read2_b32 %[t7], %[a3] offset0:160 offset1:224\n\t"
               "s_waitcnt lgkmcnt(4)\n\t"
               "v_pk_add_f32 v[232:233], v[232:233], %[t0]\n\t"
               "v_pk_add_f32 v[234:235], v[234:235], %[t1]\n\t"
               "v_pk_add_f32 v[248:249], v[248:249], %[t2]\n\t"
               "v_pk_add_f32 v[250:251], v[250:251], %[t3]\n\t"
               "s_waitcnt lgkmcnt(0)\n\t"
               "v_pk_add_f32 v[236:237], v[236:237], %[t4]\n\t"
               "v_pk_add_f32 v[238:239], v[238:239], %[t5]\n\t"
               "v_pk_add_f32 v[252:253], v[252:253], %[t6]\n\t"
               "v_pk_add_f32 v[254:255], v[254:255], %[t7]\n\t"
               "s_nop 0"
               : [t0] "=&v"(t0), [t1] "=&v"(t1), [t2] "=&v"(t2), [t3] "=&v"(t3), [t4] "=&v"(t4), [t5] "=&v"(t5), [t6] "=&v"(t6), [t7] "=&v"(t7)
               : [a0] "v"(a0), [a1] "v"(a1), [a2] "v"(a2), [a3] "v"(a3) : "memory");
}
DI void acc_store(unsigned a0, unsigned a1, unsigned a2, unsigned a3) {
  asm volatile(
               "ds_write2_b32 %[a0], v224, v225 offset0:0 offset1:64\n\t"
               "ds_write2_b32 %[a0], v226, v227 offset0:128 offset1:192\n\t"
               "ds_write2_b32 %[a0], v240, v241 offset0:32 offset1:96\n\t"
               "ds_write2_b32 %[a0], v242, v243 offset0:160 offset1:224\n\t"
               "ds_write2_b32 %[a1], v228, v229 offset0:0 offset1:64\n\t"
               "ds_write2_b32 %[a1], v230, v231 offset0:128 offset1:192\n\t"
               "ds_write2_b32 %[a1], v244, v245 offset0:32 offset1:96\n\t"
               "ds_write2_b32 %[a1], v246, v247 offset0:160 offset1:224\n\t"
               "ds_write2_b32 %[a2], v232, v233 offset0:0 offset1:64\n\t"
               "ds_write2_b32 %[a2], v234, v235 offset0:128 offset1:192\n\t"
               "ds_write2_b32 %[a2], v248, v249 offset0:32 offset1:96\n\t"
               "ds_write2_b32 %[a2], v250, v251 offset0:160 offset1:224\n\t"
               "ds_write2_b32 %[a3], v236, v237 offset0:0 offset1:64\n\t"
               "ds_write2_b32 %[a3], v238, v239 offset0:128 offset1:192\n\t"
               "ds_write2_b32 %[a3], v252, v253 offset0:32 offset1:96\n\t"
               "ds_write2_b32 %[a3], v254, v255 offset0:160 offset1:224\n\t"
               "s_waitcnt lgkmcnt(0)\n\t"
               "v_mov_b64 v[224:225], 0\n\t"
               "v_mov_b64 v[226:227], 0\n\t"
               "v_mov_b64 v[228:229], 0\n\t"
               "v_mov_b64 v[230:231], 0\n\t"
               "v_mov_b64 v[232:233], 0\n\t"
               "v_mov_b64 v[234:235], 0\n\t"
               "v_mov_b64 v[236:237], 0\n\t"
               "v_mov_b64 v[238:239], 0\n\t"
               "v_mov_b64 v[240:241], 0\n\t"
               "v_mov_b64 v[242:243], 0\n\t"
               "v_mov_b64 v[244:245], 0\n\t"
               "v_mov_b64 v[246:247], 0\n\t"
               "v_mov_b64 v[248:249], 0\n\t"
               "v_mov_b64 v[250:251], 0\n\t"
               "v_mov_b64 v[252:253], 0\n\t"
               "v_mov_b64 v[254:255], 0\n\t"
               "s_nop 7"
               :: [a0] "v"(a0), [a1] "v"(a1), [a2] "v"(a2), [a3] "v"(a3) : "memory");
}
DI void acc_gather_last(unsigned a0, unsigned a1, unsigned a2, unsigned a3) {
  asm volatile(
               "ds_read2_b32 v[176:177], %[a0] offset0:0 offset1:64\n\t"
               "ds_read2_b32 v[178:179], %[a0] offset0:128 offset1:192\n\t"
               "ds_read2_b32 v[180:181], %[a0] offset0:32 offset1:96\n\t"
               "ds_read2_b32 v[182:183], %[a0] offset0:160 offset1:224\n\t"
               "ds_read2_b32 v[184:185], %[a1] offset0:0 offset1:64\n\t"
               "ds_read2_b32 v[186:187], %[a1] offset0:128 offset1:192\n\t"
               "ds_read2_b32 v[188:189], %[a1] offset0:32 offset1:96\n\t"
               "ds_read2_b32 v[190:191], %[a1] offset0:160 offset1:224\n\t"
               "ds_read2_b32 v[192:193], %[a2] offset0:0 offset1:64\n\t"
               "ds_read2_b32 v[194:195], %[a2] offset0:128 offset1:192\n\t"
               "ds_read2_b32 v[196:197], %[a2] offset0:32 offset1:96\n\t"
               "ds_read2_b32 v[198:199], %[a2] offset0:160 offset1:224\n\t"
               "ds_read2_b32 v[200:201], %[a3] offset0:0 offset1:64\n\t"
               "ds_read2_b32 v[202:203], %[a3] offset0:128 offset1:192\n\t"
               "ds_read2_b32 v[204:205], %[a3] offset0:32 offset1:96\n\t"
               "ds_read2_b32 v[206:207], %[a3] offset0:160 offset1:224\n\t"
               "s_waitcnt lgkmcnt(12)\n\t"
               "v_pk_add_f32 v[224:225], v[224:225], v[176:177]\n\t"
               "v_pk_add_f32 v[226:227], v[226:227], v[178:179]\n\t"
               "v_pk_add_f32 v[240:241], v[240:241], v[180:181]\n\t"
               "v_pk_add_f32 v[242:243], v[242:243], v[182:183]\n\t"
               "s_waitcnt lgkmcnt(8)\n\t"
               "v_pk_add_f32 v[228:229], v[228:229], v[184:185]\n\t"
               "v_pk_add_f32 v[230:231], v[230:231], v[186:187]\n\t"
               "v_pk_add_f32 v[244:245], v[244:245], v[188:189]\n\t"
               "v_pk_add_f32 v[246:247], v[246:247], v[190:191]\n\t"
               "s_waitcnt lgkmcnt(4)\n\t"
               "v_pk_add_f32 v[232:233], v[232:233], v[192:193]\n\t"
               "v_pk_add_f32 v[234:235], v[234:235], v[194:195]\n\t"
               "v_pk_add_f32 v[248:249], v[248:249], v[196:197]\n\t"
               "v_pk_add_f32 v[250:251], v[250:251], v[198:199]\n\t"
               "s_waitcnt lgkmcnt(0)\n\t"
               "v_pk_add_f32 v[236:237], v[236:237], v[200:201]\n\t"
               "v_pk_add_f32 v[238:239], v[238:239], v[202:203]\n\t"
               "v_pk_add_f32 v[252:253], v[252:253], v[204:205]\n\t"
               "v_pk_add_f32 v[254:255], v[254:255], v[206:207]\n\t"
               "s_nop 0"
               :: [a0] "v"(a0), [a1] "v"(a1), [a2] "v"(a2), [a3] "v"(a3) : "memory");
}
DI void acc_store_last(unsigned a0, unsigned a1, unsigned a2, unsigned a3) {
  asm volatile(
               "ds_write2_b32 %[a0], v224, v225 offset0:0 offset1:64\n\t"
               "ds_write2_b32 %[a0], v226, v227 offset0:128 offset1:192\n\t"
               "ds_write2_b32 %[a0], v240, v241 offset0:32 offset1:96\n\t"
               "ds_write2_b32 %[a0], v242, v243 offset0:160 offset1:224\n\t"
               "ds_write2_b32 %[a1], v228, v229 offset0:0 offset1:64\n\t"
               "ds_write2_b32 %[a1], v230, v231 offset0:128 offset1:192\n\t"
               "ds_write2_b32 %[a1], v244, v245 offset0:32 offset1:96\n\t"
               "ds_write2_b32 %[a1], v246, v247 offset0:160 offset1:224\n\t"
               "ds_write2_b32 %[a2], v232, v233 offset0:0 offset1:64\n\t"
               "ds_write2_b32 %[a2], v234, v235 offset0:128 offset1:192\n\t"
               "ds_write2_b32 %[a2], v248, v249 offset0:32 offset1:96\n\t"
               "ds_write2_b32 %[a2], v250, v251 offset0:160 offset1:224\n\t"
               "ds_write2_b32 %[a3], v236, v237 offset0:0 offset1:64\n\t"
               "ds_write2_b32 %[a3], v238, v239 offset0:128 offset1:192\n\t"
               "ds_write2_b32 %[a3], v252, v253 offset0:32 offset1:96\n\t"
               "ds_write2_b32 %[a3], v254, v255 offset0:160 offset1:224\n\t"
               "s_nop 0"
               :: [a0] "v"(a0), [a1] "v"(a1), [a2] "v"(a2), [a3] "v"(a3) : "memory");
}
DI unsigned lds_lock(unsigned addr) {
  unsigned t, old;
  unsigned long long sv;
  asm volatile("s_mov_b64 %2, exec\n\ts_mov_b64 exec, 1\n"
               "1:\n\tds_wrxchg_rtn_b32 %0, %3, %4\n\ts_waitcnt lgkmcnt(0)\n\tv_cmp_eq_u32 vcc, 1, %0\n\ts_cbranch_vccz 2f\n\t"
               "s_sleep 1\n\ts_branch 1b\n"
               "2:\n\tv_readfirstlane_b32 %1, %0\n\ts_mov_b64 exec, %2\n\ts_nop 15"
               : "=&v"(t), "=&s"(old), "=&s"(sv) : "v"(addr), "v"(1u) : "vcc", "memory");
  return old;
}

template <typename T> DI T* karg(int k) {
  typedef __attribute__((address_space(4))) const unsigned long long* kp_t;
  kp_t kp = (kp_t)__builtin_amdgcn_kernarg_segment_ptr();
  asm volatile("" : "+s"(kp));
  return (T*)(__attribute__((address_space(1))) T*)kp[k];
}

__global__ __launch_bounds__(512) __attribute__((amdgpu_num_vgpr(83))) void egnn_kernel(
    const u16* __restrict__ h_, const u16* __restrict__ pos_, const u16* __restrict__ we_, const u16* __restrict__ wx_,
    const u16* __restrict__ wh_, char* __restrict__ ws_, u16* __restrict__ out_, const int* __restrict__ src_,
    const int* __restrict__ dst_, const u16* __restrict__ ea_) {
  __shared__ __attribute__((aligned(1024))) char lds[L_END];
  AS3 char* L = (AS3 char*)lds;
  const unsigned LB = (unsigned)(size_t)L;
  const int tid_ = threadIdx.x;
  const int w = __builtin_amdgcn_readfirstlane(tid_ >> 6);
  const int xl = blockIdx.x & 7;
  const int b = xl * 31 + (xl < 2 ? xl : 2) + (blockIdx.x >> 3);
  const int n0 = b * BN;
  unsigned cw[2][2];
  bf16x8 wq[2][4];
  unsigned aq[4], sa_c, s_rd, s_tr, acc_l;
  const unsigned ringb = L_RING + w * 4096;

  {
    const int tid = tid_, lane = tid & 63, r = lane & 31, hf = lane >> 5, p = blockIdx.x;
    const rsrc_t rs = ws_rsrc(ws_);
    const int e0 = p * CH + 4 * tid, e1 = p * CH + 2048 + tid;
    const i32x4 d4 = *(const i32x4*)(dst_ + e0), s4 = *(const i32x4*)(src_ + e0);
    const u32x2 a2 = *(const u32x2*)(ea_ + e0);
    unsigned dd[5] = {(unsigned)d4[0], (unsigned)d4[1], (unsigned)d4[2], (unsigned)d4[3], (unsigned)dst_[e1]};
    const unsigned s5 = src_[e1], a5 = ea_[e1];
    const u32x2 ctl = ld8_dev(rs, WS_CTL);
    SB();
    if (w == 7) {
      unsigned long long pc;
      asm volatile("s_getpc_b64 %0" : "=s"(pc));
      asm volatile("global_load_dword v184, %0, %1\n\tglobal_load_dword v185, %2, %1" :: "v"(lane * 128u), "s"(pc), "v"(lane * 128u + 8192u) : "memory");
      if (lane < (CODE_BYTES - 16384) / 128) asm volatile("global_load_dword v186, %0, %1" :: "v"(lane * 128u + 16384u), "s"(pc) : "memory");
    }
    dma_piece((const char*)we_ + w * 1024, lane * 16, LB + L_W + w * 1024);
    if (w < 5) dma_piece((const char*)h_ + (size_t)n0 * 128 + w * 1024, lane * 16, LB + L_H + w * 1024);
    const u32x4 w2raw = *(const u32x4*)(we_ + 64 * 64 + tid * 8);
    unsigned sraw[3], craw[4];
#pragma unroll
    for (int t = 0; t < 2; ++t) { craw[2 * t] = we_[128 * 64 + 32 * t + r]; craw[2 * t + 1] = we_[129 * 64 + 32 * t + r]; }
    {
      const u16* pp = w == 1 ? wx_ + lane : pos_ + (lane < BN ? n0 + lane : 0) * 3;
      const int st = w == 1 ? 0 : 1;
      sraw[0] = pp[0]; sraw[1] = pp[st]; sraw[2] = pp[2 * st];
    }
    SB();
    {
      const u32x4 z4 = {0, 0, 0, 0};
      static_assert(8 * S_WAVE == 2 * 8192 + 256 * 16 && BN * ACC_NODE == 5 * 8192 + 40 * 16, "clearing pattern");
      if (tid < 256) lds_st<u32x4>(L, L_TBL + 16 * tid, z4);
      lds_st<u32x4>(L, L_S + tid * 16, z4);
      lds_st<u32x4>(L, L_S + 8192 + tid * 16, z4);
#pragma unroll
      for (int k = 0; k < 5; ++k) lds_st<u32x4>(L, L_ACC + k * 8192 + tid * 16, z4);
      if (tid < 256) lds_st<u32x4>(L, L_S + 16384 + tid * 16, z4);
      if (tid < 40) lds_st<u32x4>(L, L_ACC + 5 * 8192 + tid * 16, z4);
      if (tid < 8) lds_st<unsigned>(L, L_LOCK + 4 * tid, 0u);
      if (tid == 8) lds_st<unsigned>(L, L_WT, 0u);
    }
    __syncthreads();
    unsigned idx[5], rank[5], bk[5];
#pragma unroll
    for (int i = 0; i < 5; ++i) {
      const unsigned G = dd[i] >> 3;
      bk[i] = (G * 52429u) >> 18;
      idx[i] = G + 3 * bk[i];
      const unsigned sh = (idx[i] & 1) << 4;
      const unsigned old = __hip_atomic_fetch_add((AS3 unsigned*)(L + L_TBL + 4 * (idx[i] >> 1)), 1u << sh, __ATOMIC_RELAXED,
                                                  __HIP_MEMORY_SCOPE_WORKGROUP);
      rank[i] = (old >> sh) & 0xffffu;
    }
    __syncthreads();
    const unsigned tlo = ctl[0] + 1, thi = ctl[1] + (tlo == 0);
    const unsigned tag0 = tlo & 0xfffffffu, tag1 = ((tag0 * 2654435761u) ^ ((tlo >> 28) | (thi << 4))) & 0xfffffffu;
    u32x4 entry = {0, 0, 0, 0};
    if (w < 4) {
      const u32x4 c = lds_ld<u32x4>(L, L_TBL + 16 * tid);
      const unsigned tot = (c[0] & 0xffffu) + (c[0] >> 16) + (c[1] & 0xffffu) + (c[1] >> 16) + (c[2] & 0xffffu);
      const unsigned incl = wave_incl_scan(tot);
      unsigned old;
      unsigned long long sv;
      asm volatile("s_mov_b64 %1, exec\n\ts_mov_b64 exec, %4\n\tds_add_rtn_u32 %0, %2, %3\n\ts_mov_b64 exec, %1\n\ts_waitcnt lgkmcnt(0)"
                   : "=&v"(old), "=&s"(sv) : "v"(LB + L_WT), "v"(incl), "s"(1ull << 63) : "memory");
      const unsigned s0 = incl - tot + __builtin_amdgcn_readlane(old, 63);
      const unsigned s1 = s0 + (c[0] & 0xffffu), s2 = s1 + (c[0] >> 16), s3 = s2 + (c[1] & 0xffffu), s4_ = s3 + (c[1] >> 16);
      const unsigned s5_ = s0 + tot;
      lds_st<u32x3>(L, L_TBL + 16 * tid, u32x3{s0 | (s1 << 16), s2 | (s3 << 16), s4_});
      entry = u32x4{s0 | (s1 << 12) | (s2 << 24), (s2 >> 8) | (tag0 << 4), s3 | (s4_ << 12) | (s5_ << 24), (s5_ >> 8) | (tag1 << 4)};
    }
    if (tid == 0) {
      lds_st<u32x4>(L, L_TAG, u32x4{tag0, tag1, tlo, thi});
      lds_st<u32x4>(L, L_TOT, u32x4{0, 0, 0, 0});
      lds_st<u32x4>(L, L_TOT + 16, u32x4{0, 0, 0, 0});
    }
    {
      u32x4 o;
#pragma unroll
      for (int k = 0; k < 4; ++k) o[k] = cvt_pk(bf_lo(w2raw[k]) * NLOG2E, bf_hi(w2raw[k]) * NLOG2E);
      lds_st<u32x4>(L, L_W + 8192 + tid * 16, o);
    }
    wait_vm0();
    __syncthreads();
    {
      unsigned ps[5];
#pragma unroll
      for (int i = 0; i < 5; ++i) ps[i] = lds_ld<u16>(L, L_TBL + 2 * idx[i]) + rank[i];
#pragma unroll
      for (int i = 0; i < 5; ++i) {
        const unsigned dl = dd[i] - BN * bk[i];
        const unsigned sv = i < 4 ? (unsigned)s4[i] : s5;
        const unsigned eav = i == 4 ? a5 : (i & 1) ? (a2[i >> 1] >> 16) : a2[i >> 1];
        const unsigned pb = L_RAW + 12 * (ps[i] >> 1), odd = ps[i] & 1;
        lds_st<unsigned>(L, pb + 4 * odd, sv | ((L_PD + 16 * dl) << 16));
        lds_st<u16>(L, pb + 8 + 2 * odd, (u16)eav);
      }
    }
    __syncthreads();
    {
      static_assert(REC_STRIDE == (512 + 448) * 16, "copy pattern");
      const unsigned o = WS_RECS + p * REC_STRIDE + 16 * tid;
      st16_dev(rs, o, lds_ld<u32x4>(L, L_RAW + 16 * tid));
      if (tid < 448) st16_dev(rs, o + 8192, lds_ld<u32x4>(L, L_RAW + 8192 + 16 * tid));
    }
    SB();
    if (w >= 4) {
      const int u = (w - 4) >> 1, t = w & 1;
      f32x16 pacc;
#pragma unroll
      for (int i = 0; i < 16; ++i) pacc[i] = 0.f;
#pragma unroll
      for (int s = 0; s < 4; ++s) {
        const int nl = 32 * u + r, nlc = nl < BN ? nl : BN - 1;
        const bf16x8 ha = lds_ld<bf16x8>(L, L_H + nlc * 128 + 32 * s + 16 * hf);
        const unsigned o = L_W + (16 * s + 8 * (lane >> 5) + ((lane & 15) >> 2)) * 128 + (32 * t + 16 * ((lane >> 4) & 1) + 4 * (lane & 3)) * 2;
        const u32x2 b0 = lds_tr(L, o), b1 = lds_tr(L, o + 512);
        pacc = mfma(ha, as_frag(u32x4{b0[0], b0[1], b1[0], b1[1]}), pacc);
      }
#pragma unroll
      for (int q = 0; q < 4; ++q)
        if (4 * u + q < 5)
          lds_st<u32x2>(L, L_PFR + ((4 * u + q) * 2 + t) * 512 + lane * 8,
                        u32x2{cvt_pk(pacc[4 * q] * NLOG2E, pacc[4 * q + 1] * NLOG2E), cvt_pk(pacc[4 * q + 2] * NLOG2E, pacc[4 * q + 3] * NLOG2E)});
    } else if (w == 0) {
      if (lane <= BN)
        lds_st<f32x4>(L, L_PD + 16 * lane, f32x4{bf_lo(sraw[0]), bf_lo(sraw[1]), bf_lo(sraw[2]), lane < BN ? 1.0f : 0.0f});
    } else if (w == 1) {
      lds_st<float>(L, L_WX + 4 * lane, bf_lo(sraw[0]));
    }
#pragma unroll
    for (int t = 0; t < 2; ++t) {
      const float d = bf_lo(craw[2 * t]) * NLOG2E, a = bf_lo(craw[2 * t + 1]) * NLOG2E;
      const unsigned hi = cvt_pk(d, a);
      cw[t][0] = hi;
      cw[t][1] = cvt_pk(d - bf_lo(hi), a - bf_hi(hi));
    }
    {
#pragma unroll
      for (int s = 0; s < 4; ++s) aq[s] = ringb + r * 128 + (((2 * s + hf) ^ ((r >> 1) & 7)) << 4);
      const unsigned Sb = L_S + w * S_WAVE;
      const int g16 = lane >> 4, tq = (lane & 15) >> 2, tp = lane & 3, tu = g16 & 1, th = g16 >> 1;
      sa_c = Sb + 2 * ((r & 0x13) | ((r & 4) << 1) | ((r & 8) >> 1));
      s_rd = Sb + r * 80 + 16 * hf;
      s_tr = Sb + (4 * (4 * th + tq) + 3) * 80 + 2 * (16 * tu + 8 * (tp & 1) + 4 * (tp >> 1));
      acc_l = L_ACC + hf * ACC_NODE + 4 * r;
#pragma unroll
      for (int t = 0; t < 2; ++t)
#pragma unroll
        for (int s = 0; s < 4; ++s) {
          const unsigned o = L_W + 8192 + (16 * s + 8 * th + tq) * 128 + (32 * t + 16 * tu + 4 * tp) * 2;
          const u32x2 b0 = lds_tr(L, o), b1 = lds_tr(L, o + 512);
          wq[t][s] = as_frag(u32x4{b0[0], b0[1], b1[0], b1[1]});
        }
      const unsigned dc0 = ((lane & 7) ^ ((lane >> 4) & 7)) << 4, dc1 = ((lane & 7) ^ ((4 + (lane >> 4)) & 7)) << 4;
      asm volatile("v_mov_b32 v167, %0\n\tv_mov_b32 v169, %1\n\tv_mov_b32 v171, %0\n\tv_mov_b32 v173, %1" :: "v"(dc0), "v"(dc1));
    }
    wait_vm0();
    __syncthreads();
    if (tid < NBK) st16_dev(rs, WS_ENT + p * 4096 + tid * 16, entry);
  }

  int pa = 0, np = NP;
  for (unsigned it = 0;; ++it) {
    const u16 *h = karg<const u16>(0), *pos = karg<const u16>(1);
    const char* ws = karg<const char>(5);
    const rsrc_t rs = ws_rsrc(ws);
    int tid = tid_;
    asm volatile("" : "+v"(tid));
    const int lane = tid & 63, r = lane & 31, hf = lane >> 5;
    const unsigned l_tot = L_TOT + 16 * (it & 1);
    unsigned pA = 0, pB = 0, pC = 0, d1 = 0, d2 = 0, d3 = 0, d4 = 0;
    if (w < 4) {
      const u32x2 tg = lds_ld<u32x2>(L, L_TAG);
      const unsigned eo = WS_ENT + (tid < NP ? tid : 0) * 4096 + b * 16;
      auto seen = [&](const u32x4& e) { return __all(tid >= NP || ((e[1] >> 4) == tg[0] && (e[3] >> 4) == tg[1])); };
      auto poll = [&]() {
        __builtin_amdgcn_s_sleep(POLL_SLEEP);
        asm volatile("" ::: "memory");
        return ld16_dev(rs, eo);
      };
      u32x4 ent, e0 = ld16_dev(rs, eo);
      for (unsigned spins = 0;; ++spins) {
        const u32x4 e1 = poll();
        if (seen(e0)) { ent = e0; break; }
        e0 = poll();
        ent = e1;
        if (seen(e1)) break;
        if (spins > SPIN_LIMIT) {
          __hip_atomic_fetch_or((AS3 unsigned*)(L + l_tot + 12), 1u, __ATOMIC_RELAXED, __HIP_MEMORY_SCOPE_WORKGROUP);
          break;
        }
      }
      const unsigned s0 = ent[0] & 0xfffu, s1 = (ent[0] >> 12) & 0xfffu, s2 = __builtin_amdgcn_alignbit(ent[1], ent[0], 24) & 0xfffu;
      const unsigned s3 = ent[2] & 0xfffu, s4 = (ent[2] >> 12) & 0xfffu, s5 = __builtin_amdgcn_alignbit(ent[3], ent[2], 24) & 0xfffu;
      const bool act = (unsigned)(tid - pa) < (unsigned)np && tid < NP;
      d1 = s1 - s0; d2 = s2 - s0; d3 = s3 - s0; d4 = s4 - s0;
      const unsigned cnt = act ? s5 - s0 : 0;
      const unsigned cA = act ? d1 | ((s2 - s1) << 16) : 0, cB = act ? (s3 - s2) | ((s4 - s3) << 16) : 0, cC = act ? s5 - s4 : 0;
      lds_st<unsigned>(L, L_SEG + 4 * tid, (tid * (CH / 2) + (s0 >> 1)) | ((cnt + (s0 & 1)) << 19) | (s0 << 31));
      const unsigned iA = wave_incl_scan(cA), iB = wave_incl_scan(cB), iC = wave_incl_scan(cC);
      if (lane == 63) {
        lds_st<u32x3>(L, L_WV + 16 * w, u32x3{iA, iB, iC});
        __hip_atomic_fetch_add((AS3 unsigned*)(L + l_tot), iA, __ATOMIC_RELAXED, __HIP_MEMORY_SCOPE_WORKGROUP);
        __hip_atomic_fetch_add((AS3 unsigned*)(L + l_tot + 4), iB, __ATOMIC_RELAXED, __HIP_MEMORY_SCOPE_WORKGROUP);
        __hip_atomic_fetch_add((AS3 unsigned*)(L + l_tot + 8), iC, __ATOMIC_RELAXED, __HIP_MEMORY_SCOPE_WORKGROUP);
      }
      if (__any(cnt > 255))
        __hip_atomic_fetch_or((AS3 unsigned*)(L + l_tot + 12), 2u, __ATOMIC_RELAXED, __HIP_MEMORY_SCOPE_WORKGROUP);
      pA = iA - cA;
      pB = iB - cB;
      pC = iC - cC;
    }
    __syncthreads();
    const u32x4 tot4 = lds_ld<u32x4>(L, l_tot);
    unsigned e3[4];
#pragma unroll
    for (int k = 0; k < 4; ++k) e3[k] = lds_ld<unsigned>(L, L_SEG + 4 * (64 * k + (tid >> 3)));
    const unsigned flags = __builtin_amdgcn_readfirstlane(tot4[3]);
    if (flags & 1) return;
    if (tid == 511) lds_st<u32x4>(L, L_TOT + 16 * (~it & 1), u32x4{0, 0, 0, 0});
    const unsigned j2 = 2 * (tid & 7);
    u32x3 ra[4], rb[4];
    unsigned cntp[4], ro[4];
#pragma unroll
    for (int k = 0; k < 4; ++k) {
      cntp[k] = (e3[k] >> 19) & 0xfffu;
      ro[k] = ((e3[k] & 0x7ffffu) + (tid & 7)) * 12 + WS_RECS;
      ra[k] = ld12_rec(rs, j2 < cntp[k] ? ro[k] : NO_DATA);
    }
#pragma unroll
    for (int k = 0; k < 4; ++k) rb[k] = ld12_rec(rs, j2 + 16 < cntp[k] ? ro[k] + 96 : NO_DATA);
    SB();
    const unsigned tA = __builtin_amdgcn_readfirstlane(tot4[0]), tB = __builtin_amdgcn_readfirstlane(tot4[1]);
    const unsigned tot[5] = {tA & 0xffffu, tA >> 16, tB & 0xffffu, tB >> 16, (unsigned)__builtin_amdgcn_readfirstlane(tot4[2])};
    unsigned base[6];
    base[0] = 0;
#pragma unroll
    for (int g = 0; g < 5; ++g) base[g + 1] = base[g] + ((tot[g] + 31) & ~31u);
    if (((flags & 2) && np > 16) || base[5] > (unsigned)CAP) {
      np = (np + 1) >> 1;
      __syncthreads();
      continue;
    }
    if (w < 4) {
      constexpr unsigned R8 = L_RAW / 8;
#pragma unroll
      for (int v = 0; v < 3; ++v) {
        const u32x3 t3 = lds_ld<u32x3>(L, L_WV + 16 * v);
        if (v < w) { pA += t3[0]; pB += t3[1]; pC += t3[2]; }
      }
      const unsigned v0 = ((pA & 0xffffu) + (base[0] + R8)) << 3, v1 = ((pA >> 16) - d1 + (base[1] + R8)) << 3;
      const unsigned v2 = ((pB & 0xffffu) - d2 + (base[2] + R8)) << 3, v3 = ((pB >> 16) - d3 + (base[3] + R8)) << 3;
      const unsigned v4 = (pC - d4 + (base[4] + R8)) << 3;
      lds_st<u32x3>(L, L_TBL + 16 * tid, u32x3{(v0 & 0xffffu) | (v1 << 16), (v2 & 0xffffu) | (v3 << 16), v4});
    } else {
      const u32x2 pad = {(unsigned)(L_PD + 16 * BN) << 16, 0};
      const unsigned lo0 = w == 4 ? base[0] + tot[0] : w == 5 ? base[2] + tot[2] : base[4] + tot[4];
      const unsigned lo1 = w == 4 ? base[1] : w == 5 ? base[3] : base[5];
      const unsigned hi0 = w == 4 ? base[1] + tot[1] : base[3] + tot[3], hi1 = w == 4 ? base[2] : base[4];
      const unsigned slot = (hf ? hi0 : lo0) + r;
      if (w < 7 && slot < (hf ? (w == 6 ? 0u : hi1) : lo1)) lds_st<u32x2>(L, L_RAW + 8 * slot, pad);
      if (tid - 256 < PADS) lds_st<u32x2>(L, L_RAW + 8 * (base[5] + tid - 256), pad);
    }

    const int T = base[5] >> 5;
    const int q4 = T >> 2, r4 = T & 3, ws4 = w & 3;
    const int sa4 = ws4 * q4 + (ws4 < r4 ? ws4 : r4), sn4 = q4 + (ws4 < r4 ? 1 : 0);
    const int nold = (sn4 * 44 + 32) >> 6;
    const int ta = w < 4 ? sa4 : sa4 + nold;
    const int tb = w < 4 ? sa4 + nold : sa4 + sn4;
    const int tg1 = base[1] >> 5, tg2 = base[2] >> 5, tg3 = base[3] >> 5, tg4 = base[4] >> 5;

    const char* pos6 = (const char*)pos;
    const rsrc_t h_rows = __builtin_amdgcn_make_buffer_rsrc((void*)((const char*)h - 3072), 128, 0xffffffffu, 0x00020000);
    auto group_of = [&](int t) {
      int g;
      asm("s_cmp_ge_i32 %1, %2\n\ts_cselect_b32 %0, 1, 0\n\ts_cmp_ge_i32 %1, %3\n\ts_addc_u32 %0, %0, 0\n\t"
          "s_cmp_ge_i32 %1, %4\n\ts_addc_u32 %0, %0, 0\n\ts_cmp_ge_i32 %1, %5\n\ts_addc_u32 %0, %0, 0"
          : "=&s"(g) : "s"(t), "s"(tg1), "s"(tg2), "s"(tg3), "s"(tg4) : "scc");
      return g;
    };
    auto group_end = [&](int t) {
      int e;
      asm("s_cmp_gt_i32 %5, %1\n\ts_cselect_b32 %0, %5, %6\n\ts_cmp_gt_i32 %4, %1\n\ts_cselect_b32 %0, %4, %0\n\t"
          "s_cmp_gt_i32 %3, %1\n\ts_cselect_b32 %0, %3, %0\n\ts_cmp_gt_i32 %2, %1\n\ts_cselect_b32 %0, %2, %0"
          : "=&s"(e) : "s"(t), "s"(tg1), "s"(tg2), "s"(tg3), "s"(tg4), "s"(T) : "scc");
      return e;
    };
    int gend = group_end(ta);
    int tflush = -1;

    bf16x8 bxA0, bxA1, bxB0, bxB1;
    auto load_bx = [&](int gg) {
      const u32x2 p0 = lds_ld<u32x2>(L, L_PFR + gg * 1024 + lane * 8), p1 = lds_ld<u32x2>(L, L_PFR + gg * 1024 + 512 + lane * 8);
      bxA0 = as_frag(u32x4{p0[0], p0[1], hf ? 0u : cw[0][0], hf ? 0u : cw[0][1]});
      bxB0 = as_frag(u32x4{p0[0], p0[1], hf ? cw[0][0] : 0u, hf ? cw[0][1] : 0u});
      bxA1 = as_frag(u32x4{p1[0], p1[1], hf ? 0u : cw[1][0], hf ? 0u : cw[1][1]});
      bxB1 = as_frag(u32x4{p1[0], p1[1], hf ? cw[1][0] : 0u, hf ? cw[1][1] : 0u});
      asm volatile("" : "+v"(bxA0), "+v"(bxB0), "+v"(bxA1), "+v"(bxB1));
    };
    load_bx(group_of(ta));
    tiles_init();
    set_m0(LB + ringb);
    __syncthreads();

    {
      auto second = [](unsigned w0, unsigned eahi) { return (((w0 >> 20) & 7) * 320) | eahi; };
      const unsigned tb0 = L_TBL + 16 * (tid >> 3);
      unsigned a0[4], a1[4], r8[4];
#pragma unroll
      for (int k = 0; k < 4; ++k) {
        r8[k] = 8 * j2 - ((e3[k] >> 31) << 3);
        a0[k] = lds_ld<u16>(L, tb0 + 1024 * k + ((ra[k][0] >> 22) & 14)) + r8[k];
        a1[k] = lds_ld<u16>(L, tb0 + 1024 * k + ((ra[k][1] >> 22) & 14)) + r8[k];
      }
#pragma unroll
      for (int k = 0; k < 4; ++k) {
        if (j2 < cntp[k] && (int)r8[k] >= 0) lds_st<u32x2>(L, a0[k], u32x2{ra[k][0], second(ra[k][0], ra[k][2] << 16)});
        if (j2 + 1 < cntp[k]) lds_st<u32x2>(L, a1[k] + 8, u32x2{ra[k][1], second(ra[k][1], ra[k][2] & 0xffff0000u)});
      }
#pragma unroll
      for (int k = 0; k < 4; ++k) {
        if (j2 + 16 < cntp[k]) {
          const unsigned b0 = lds_ld<u16>(L, tb0 + 1024 * k + ((rb[k][0] >> 22) & 14)) + r8[k];
          const unsigned b1 = lds_ld<u16>(L, tb0 + 1024 * k + ((rb[k][1] >> 22) & 14)) + r8[k];
          lds_st<u32x2>(L, b0 + 128, u32x2{rb[k][0], second(rb[k][0], rb[k][2] << 16)});
          if (j2 + 17 < cntp[k]) lds_st<u32x2>(L, b1 + 136, u32x2{rb[k][1], second(rb[k][1], rb[k][2] & 0xffff0000u)});
        }
      }
#pragma unroll
      for (int k = 0; k < 4; ++k) {
        const unsigned odd = e3[k] >> 31;
        for (unsigned q = (tid & 7) + 32; q < cntp[k]; q += 8) {
          const unsigned pb = ((e3[k] & 0x7ffffu) + (q >> 1)) * 12 + WS_RECS;
          const unsigned w0 = ld4_rec(rs, pb + 4 * (q & 1)), eav = ld2_rec(rs, pb + 8 + 2 * (q & 1));
          const unsigned a = lds_ld<u16>(L, tb0 + 1024 * k + ((w0 >> 22) & 14));
          lds_st<u32x2>(L, a + 8 * (q - odd), u32x2{w0, second(w0, eav << 16)});
        }
      }
    }
    __syncthreads();

    if (pa == 0) {
      const char* wh = karg<const char>(4);
      dma_piece(wh + w * 2048, lane * 16, LB + L_W + w * 2048);
      dma_piece(wh + w * 2048 + 1024, lane * 16, LB + L_W + w * 2048 + 1024);
    }
    if (ta < tb) {
      auto flush = [&](int gg) {
        const unsigned a = LB + acc_l + gg * (8 * ACC_NODE);
        if (lds_lock(LB + L_LOCK + 4 * gg) != 0) acc_gather(a, a + 2 * ACC_NODE, a + 4 * ACC_NODE, a + 6 * ACC_NODE);
        acc_store(a, a + 2 * ACC_NODE, a + 4 * ACC_NODE, a + 6 * ACC_NODE);
        lds_st<unsigned>(L, L_LOCK + 4 * gg, 2u);
        asm volatile("" ::: "memory");
      };
      auto flush_last = [&](int gg) {
        const unsigned a = LB + acc_l + gg * (8 * ACC_NODE);
        if (lds_lock(LB + L_LOCK + 4 * gg) != 0) acc_gather_last(a, a + 2 * ACC_NODE, a + 4 * ACC_NODE, a + 6 * ACC_NODE);
        acc_store_last(a, a + 2 * ACC_NODE, a + 4 * ACC_NODE, a + 6 * ACC_NODE);
        lds_st<unsigned>(L, L_LOCK + 4 * gg, 2u);
        asm volatile("" ::: "memory");
      };
      unsigned a_idx = LB + L_RAW + ta * 256 + (lane >> 3) * 8, a_raw = L_RAW + ta * 256 + lane * 8;
      auto raw_load = [&](int dt) {
        const unsigned a = a_raw + dt * 256;
        return Raw{lds_ld<u16>(L, a), lds_ld<u16>(L, a + 2), lds_ld<unsigned>(L, a + 4), lds_ld<u16>(L, a + 4)};
      };
      auto pos_load = [&](const Raw& x) {
        asm volatile("global_load_dwordx2 v[174:175], %0, %1" :: "v"(x.src * 6u), "s"(pos6) : "memory");
      };
      auto pd_load = [&](const Raw& x) { return lds_ld<f32x4>(L, x.pda); };
      auto prep = [&](const Raw& x, const f32x4& pd) {
        float xs, ys, zs;
        asm volatile("v_lshlrev_b32 %0, 16, v174\n\tv_and_b32 %1, 0xffff0000, v174\n\tv_lshlrev_b32 %2, 16, v175"
                     : "=v"(xs), "=v"(ys), "=v"(zs));
        const f32x2 dxy = f32x2{pd[0], pd[1]} - f32x2{xs, ys};
        unsigned wa = cvt_pk(dxy[0], dxy[1]), wb = cvt_pk(pd[2] - zs, pd[3]);
        const float dzr = bf_lo(wb);
        const bf16x2v wav = __builtin_bit_cast(bf16x2v, wa);
        const float d2 = __builtin_amdgcn_fdot2_f32_bf16(wav, wav, dzr * dzr, false);
        Prep o;
        o.de = cvt_pk(d2, as_f(x.w1));
        asm volatile("" : "+v"(o.de), "+v"(wa), "+v"(wb));
        const unsigned P = x.n320 + sa_c, Q = P + 160;
        const auto sa = __builtin_amdgcn_permlane32_swap(P, Q, false, false);
        const auto sd = __builtin_amdgcn_permlane32_swap(wa, wb, false, false);
        o.saA = sa[0]; o.saB = sa[1]; o.sdA = sd[0]; o.sdB = sd[1];
        return o;
      };

      struct Ops { bf16x8 q0, q1, q2, q3, as0, as1, ax; };
      auto read_ops = [&](unsigned sa, unsigned sd, unsigned de) {
        Ops o;
        o.q0 = lds_ld<bf16x8>(L, aq[0]); o.q1 = lds_ld<bf16x8>(L, aq[1]);
        o.q2 = lds_ld<bf16x8>(L, aq[2]); o.q3 = lds_ld<bf16x8>(L, aq[3]);
        lds_st<u16>(L, sa, (u16)sd);
        lds_st<u16>(L, sa + 80, (u16)(sd >> 16));
        const u32x2 oh = lds_tr(L, s_tr);
        o.as0 = lds_ld<bf16x8>(L, s_rd);
        o.as1 = lds_ld<bf16x8>(L, s_rd + 32);
        lds_st<u16>(L, sa, (u16)0);
        lds_st<u16>(L, sa + 80, (u16)0);
        unsigned de2;
        asm volatile("v_mov_b32 %0, %1" : "=v"(de2) : "v"(de));
        o.ax = as_frag(u32x4{oh[0], oh[1], de, de2});
        return o;
      };
      auto alpha = [&](int t, const Ops& c, const bf16x8& asl, const bf16x8& bx1, auto&& mid) {
        alpha1(asl);
        if (__builtin_expect(t == tflush, 0)) flush(group_of(t - 1));
        alpha2(c.ax, bx1, c.q0, wq[1][0]);
        SB(); mid(); SB();
        alpha3(c.q1, wq[1][1], c.q2, wq[1][2], c.q3, wq[1][3], c.as0);
      };
      auto beta = [&](const Ops& c, const Ops& n, const bf16x8& bx0n, auto&& mid) {
        beta1(c.as1);
        beta2(n.ax, bx0n, n.q0, wq[0][0]);
        SB(); mid(); SB();
        beta3(n.q1, wq[0][1], n.q2, wq[0][2], n.q3, wq[0][3], c.as0);
      };
      auto next_group = [&](int t) {
        if (__builtin_expect(t == gend, 0)) { gend = group_end(t); tflush = t; load_bx(group_of(t)); }
      };

      rows_index<0>(a_idx);
      rows_gather(h_rows);
      Raw rw = raw_load(0);
      pos_load(rw);
      f32x4 pd = pd_load(rw);
      SB();
      wait_vm0();
      Prep pr = prep(rw, pd);
      rw = raw_load(2);
      Ops A = read_ops(pr.saA, pr.sdA, pr.de), B;
#pragma unroll
      for (int j = 0; j < 8; ++j) B.as1[j] = 0;
      asm volatile("" : "+v"(B.as1));
      const int glast = group_of(tb - 1);
      rows_index<256>(a_idx);
      chain_y0(A.ax, bxA0, A.q0, wq[0][0], A.q1, wq[0][1], A.q2, wq[0][2], A.q3, wq[0][3]);
      rows_gather(h_rows);
      int t = ta;
      auto tile_a = [&]() {
        alpha(t, A, B.as1, bxA1, [&]() { pos_load(rw); });
        wait_vm0();
        next_group(t + 1);
        rows_index<512>(a_idx);
        B = read_ops(pr.saB, pr.sdB, pr.de);
        pd = pd_load(rw);
        SB();
        beta(A, B, bxB0, [&]() { rows_gather(h_rows); });
      };
      for (; t + 2 <= tb; t += 2) {
        tile_a();
        alpha(t + 1, B, A.as1, bxB1, [&]() { pr = prep(rw, pd); });
        wait_vm0();
        next_group(t + 2);
        rows_index<768>(a_idx);
        A = read_ops(pr.saA, pr.sdA, pr.de);
        rw = raw_load(4);
        SB();
        beta(B, A, bxA0, [&]() { rows_gather(h_rows); });
        a_idx += 512;
        a_raw += 512;
      }
      if (t < tb) {
        tile_a();
        last_block(A.as1);
      } else {
        last_block(B.as1);
      }
      flush_last(glast);
    } else {
      wait_vm0();
    }
    pa += np;
    if (pa >= NP) break;
    wait_vm0();
    __syncthreads();
  }
  __syncthreads();

  u16* out = karg<u16>(6);
  int tid = tid_;
  asm volatile("" : "+v"(tid));
  const int lane = tid & 63, r = lane & 31, hf = lane >> 5;
  if (w < 4) {
    const int g16 = lane >> 4, tq = (lane & 15) >> 2, tp = lane & 3, tu = g16 & 1, th = g16 >> 1;
    const int u = w >> 1, t = w & 1;
    const int nl = 32 * u + r, nlc = nl < BN ? nl : BN - 1;
    const unsigned hrow = L_H + nlc * 128;
    f32x16 d;
#pragma unroll
    for (int i = 0; i < 16; ++i) d[i] = 0.f;
    const unsigned w_tr = L_W + (8 * th + tq) * 128 + (32 * t + 16 * tu + 4 * tp) * 2;
#pragma unroll
    for (int s = 0; s < 8; ++s) {
      const u32x2 a0 = lds_tr(L, w_tr + s * 2048), a1 = lds_tr(L, w_tr + s * 2048 + 512);
      const bf16x8 a = as_frag(u32x4{a0[0], a0[1], a1[0], a1[1]});
      bf16x8 bb;
      if (s < 4) {
        bb = lds_ld<bf16x8>(L, hrow + 32 * s + 16 * hf);
      } else {
        const unsigned o = L_ACC + nlc * ACC_NODE + 3 * 256 + (16 * (s - 4) + 8 * hf) * 4;
        const f32x4 m0 = lds_ld<f32x4>(L, o) * NLN2, m1 = lds_ld<f32x4>(L, o + 16) * NLN2;
        bb = as_frag(u32x4{cvt_pk(m0[0], m0[1]), cvt_pk(m0[2], m0[3]), cvt_pk(m1[0], m1[1]), cvt_pk(m1[2], m1[3])});
      }
      d = mfma(a, bb, d);
    }
    if (nl < BN) {
#pragma unroll
      for (int q = 0; q < 4; ++q) {
        const int f = 32 * t + 8 * q + 4 * hf;
        const u32x2 hv = lds_ld<u32x2>(L, hrow + 2 * f);
        u32x2 o;
#pragma unroll
        for (int k = 0; k < 2; ++k) {
          const unsigned pb = cvt_pk(d[4 * q + 2 * k], d[4 * q + 2 * k + 1]);
          const f32x2 x = {bf_lo(pb), bf_hi(pb)}, y = x * NLOG2E;
          const f32x2 den = f32x2{__builtin_amdgcn_exp2f(y[0]), __builtin_amdgcn_exp2f(y[1])} + 1.0f;
          const f32x2 act = x * f32x2{__builtin_amdgcn_rcpf(den[0]), __builtin_amdgcn_rcpf(den[1])};
          const unsigned ab = cvt_pk(act[0], act[1]);
          const f32x2 sum = f32x2{bf_lo(hv[k]), bf_hi(hv[k])} + f32x2{bf_lo(ab), bf_hi(ab)};
          o[k] = cvt_pk(sum[0], sum[1]);
        }
        *(u32x2*)(out + (size_t)(n0 + nl) * 64 + f) = o;
      }
    }
  } else {
    const int idx = tid - 256, nc = idx >> 1, half = idx & 1;
    const int ncc = nc < 3 * BN ? nc : 3 * BN - 1;
    const int n = ncc / 3, c = ncc - 3 * n;
    const unsigned o = L_ACC + n * ACC_NODE + c * 256 + half * 128;
    float s = 0.f;
#pragma unroll
    for (int k = 0; k < 8; ++k) {
      const f32x4 v = lds_ld<f32x4>(L, o + 16 * k), x = lds_ld<f32x4>(L, L_WX + half * 128 + 16 * k);
      s += v[0] * x[0] + v[1] * x[1] + v[2] * x[2] + v[3] * x[3];
    }
    s += __builtin_bit_cast(float, __builtin_amdgcn_update_dpp(0, __builtin_bit_cast(int, s), 0xb1, 0xf, 0xf, true));
    if (nc < 3 * BN && half == 0) {
      const float p0 = lds_ld<float>(L, L_PD + 16 * n + 4 * c);
      out[(size_t)NN * 64 + (size_t)n0 * 3 + nc] = (u16)cvt_pk(p0 + bf_round(s * NLN2), 0.f);
    }
  }
  if (blockIdx.x == 0 && tid == 511)
    __builtin_amdgcn_raw_buffer_store_b64(lds_ld<u32x2>(L, L_TAG + 8), ws_rsrc(karg<const char>(5)), WS_CTL, 0, 16);
}

}

extern "C" void kernel_launch(void* const* d_in, const int* in_sizes, int n_in, void* d_out, int out_size, void* d_ws, size_t ws_size,
                              hipStream_t stream) {
  const u16* h = (const u16*)d_in[0];
  const u16* pos = (const u16*)d_in[1];
  const u16* ea = (const u16*)d_in[2];
  const int* src = (const int*)d_in[3];
  const int* dst = (const int*)d_in[4];
  const u16* we = (const u16*)d_in[5];
  const u16* wx = (const u16*)d_in[6];
  const u16* wh = (const u16*)d_in[7];
  egnn_kernel<<<NBK, 512, 0, stream>>>(h, pos, we, wx, wh, (char*)d_ws, (u16*)d_out, src, dst, ea);
}
```

```cpp
#include <hip/hip_runtime.h>
#include <hip/hip_bf16.h>
#include <stdint.h>

#define DI __device__ __forceinline__
#define AS3 __attribute__((address_space(3)))
#define SB() __builtin_amdgcn_sched_barrier(0)

typedef short bf16x8 __attribute__((ext_vector_type(8)));
typedef short s16x4 __attribute__((ext_vector_type(4)));
typedef float f32x16 __attribute__((ext_vector_type(16)));
typedef float f32x4 __attribute__((ext_vector_type(4)));
typedef float f32x2 __attribute__((ext_vector_type(2)));
typedef unsigned u32x4 __attribute__((ext_vector_type(4)));
typedef unsigned u32x3 __attribute__((ext_vector_type(3)));
typedef unsigned u32x2 __attribute__((ext_vector_type(2)));
typedef int i32x4 __attribute__((ext_vector_type(4)));
typedef __bf16 bf16x2v __attribute__((ext_vector_type(2)));
typedef unsigned short u16;

namespace {

constexpr int NN = 10000;
constexpr int NE = 640000;
constexpr int BN = 40;
constexpr int NBK = NN / BN;
constexpr int NP = NBK;
constexpr int CH = NE / NP;
static_assert(CH == 5 * 512 && CH < 4096, "five edges per thread, 12-bit run starts");
constexpr float NLOG2E = -1.4426950408889634f;
constexpr float NLN2 = -0.6931471805599453f;
constexpr unsigned SPIN_LIMIT = 1u << 20;
constexpr unsigned NO_DATA = 0xffffff00u;
constexpr int POLL_SLEEP = 4;
constexpr int CODE_BYTES = 18176;

constexpr unsigned WS_CTL = 0;
constexpr unsigned WS_ENT = 256;
constexpr unsigned WS_RECS = WS_ENT + NP * 4096;
constexpr unsigned REC_STRIDE = CH * 6;
constexpr unsigned WS_END = WS_RECS + NP * REC_STRIDE;
static_assert(WS_RECS % 128 == 0 && REC_STRIDE % 128 == 0, "no cache line holds records of two producers");

constexpr int CAP = 2880;
constexpr int PADS = 160;
constexpr int L_S = 0;
constexpr int S_WAVE = 2560;
constexpr int L_PD = 20480;
constexpr int L_WT = 21248;
constexpr int L_TOT = L_WT + 16;
constexpr int L_LOCK = 21312;
constexpr int L_TAG = 21344;
constexpr int L_WV = 21376;
constexpr int L_WX = 21504;
constexpr int L_TBL = 21760;
constexpr int L_RAW = L_TBL + 4096;
constexpr int L_PFR = L_RAW + (CAP + PADS) * 8;
constexpr int L_RING = L_PFR + 5120;
constexpr int L_ACC = L_RING + 8 * 4096;
constexpr int ACC_NODE = 1040;
constexpr int L_W = L_ACC + BN * ACC_NODE;
constexpr int L_H = L_W + 16384;
constexpr int L_SEG = L_H + BN * 128;
constexpr int L_END = L_SEG + 1024;
static_assert(L_PD % 1024 == 0, "group id is read from bits 7..9 of the pos address");
static_assert(L_RAW + (CAP + PADS) * 8 < 65536 && L_RAW >= 8 * CH && CAP >= CH + 5 * 31, "run table holds 16-bit LDS addresses");
static_assert(L_PFR % 16 == 0 && L_RING % 16 == 0 && L_ACC % 16 == 0 && L_W % 16 == 0 && L_END <= 163840, "LDS map");

DI unsigned cvt_pk(float a, float b) { f32x2 v = {a, b}; return __builtin_bit_cast(unsigned, __builtin_convertvector(v, bf16x2v)); }
DI float as_f(unsigned u) { return __builtin_bit_cast(float, u); }
DI float bf_lo(unsigned u) { return __builtin_bit_cast(float, u << 16); }
DI float bf_hi(unsigned u) { return __builtin_bit_cast(float, u & 0xffff0000u); }
DI float bf_round(float x) { return bf_lo(cvt_pk(x, x)); }
DI f32x16 mfma(bf16x8 a, bf16x8 b, f32x16 c) { return __builtin_amdgcn_mfma_f32_32x32x16_bf16(a, b, c, 0, 0, 0); }
DI bf16x8 as_frag(u32x4 v) { return __builtin_bit_cast(bf16x8, v); }

DI unsigned wave_incl_scan(unsigned x) {
  x += __builtin_amdgcn_update_dpp(0, (int)x, 0x111, 0xf, 0xf, true);
  x += __builtin_amdgcn_update_dpp(0, (int)x, 0x112, 0xf, 0xf, true);
  x += __builtin_amdgcn_update_dpp(0, (int)x, 0x114, 0xf, 0xf, true);
  x += __builtin_amdgcn_update_dpp(0, (int)x, 0x118, 0xf, 0xf, true);
  x += __builtin_amdgcn_update_dpp(0, (int)x, 0x142, 0xa, 0xf, false);
  x += __builtin_amdgcn_update_dpp(0, (int)x, 0x143, 0xc, 0xf, false);
  return x;
}

template <typename T> DI T lds_ld(AS3 char* L, unsigned off) { return *(AS3 T*)(L + off); }
template <typename T> DI void lds_st(AS3 char* L, unsigned off, T v) { *(AS3 T*)(L + off) = v; }
DI u32x2 lds_tr(AS3 char* L, unsigned off) {
  return __builtin_bit_cast(u32x2, __builtin_amdgcn_ds_read_tr16_b64_v4i16((AS3 s16x4*)(L + off)));
}

DI const char* uni_ptr(const char* p) {
  const unsigned long long v = (unsigned long long)p;
  const unsigned lo = __builtin_amdgcn_readfirstlane((int)(unsigned)v), hi = __builtin_amdgcn_readfirstlane((int)(unsigned)(v >> 32));
  return (const char*)(__attribute__((address_space(1))) const char*)(((unsigned long long)hi << 32) | lo);
}
DI void dma_piece(const char* base_, unsigned voff, unsigned ldsaddr_) {
  const char* base = uni_ptr(base_);
  const unsigned ldsaddr = __builtin_amdgcn_readfirstlane((int)ldsaddr_);
  unsigned keep;
  asm volatile("s_mov_b32 %0, m0\n\ts_mov_b32 m0, %3\n\ts_nop 4\n\tglobal_load_lds_dwordx4 %1, %2\n\ts_mov_b32 m0, %0"
               : "=&s"(keep) : "v"(voff), "s"(base), "s"(ldsaddr) : "memory");
}
DI void set_m0(unsigned ldsaddr) { asm volatile("s_mov_b32 m0, %0\n\ts_nop 0" :: "s"(ldsaddr) : "memory"); }
typedef __amdgpu_buffer_rsrc_t rsrc_t;
template <int OFF> DI void rows_index(unsigned a) {
  asm volatile("ds_read_u16 v166, %0 offset:%1\n\tds_read_u16 v168, %0 offset:%2\n\tds_read_u16 v170, %0 offset:%3\n\tds_read_u16 v172, %0 offset:%4"
               :: "v"(a), "i"(OFF), "i"(OFF + 64), "i"(OFF + 128), "i"(OFF + 192) : "memory");
}
DI void rows_gather(rsrc_t rows) {
  asm volatile("s_waitcnt lgkmcnt(0)\n\t"
               "buffer_load_dwordx4 v[166:167], %0, %1 idxen offen lds\n\t"
               "buffer_load_dwordx4 v[168:169], %0, %2 idxen offen offset:1024 lds\n\t"
               "buffer_load_dwordx4 v[170:171], %0, %3 idxen offen offset:2048 lds\n\t"
               "buffer_load_dwordx4 v[172:173], %0, 0 idxen offen offset:3072 lds"
               :: "s"(rows), "s"(3072u), "s"(2048u), "s"(1024u) : "memory");
}
DI rsrc_t ws_rsrc(const char* ws) { return __builtin_amdgcn_make_buffer_rsrc((void*)ws, 0, WS_END, 0x00020000); }
DI u32x4 ld16_dev(rsrc_t r, unsigned off) { return __builtin_amdgcn_raw_buffer_load_b128(r, off, 0, 16); }
DI u32x3 ld12_rec(rsrc_t r, unsigned off) { return __builtin_amdgcn_raw_buffer_load_b96(r, off, 0, 16); }
DI unsigned ld4_rec(rsrc_t r, unsigned off) { return __builtin_amdgcn_raw_buffer_load_b32(r, off, 0, 16); }
DI unsigned ld2_rec(rsrc_t r, unsigned off) { return __builtin_amdgcn_raw_buffer_load_b16(r, off, 0, 16); }
DI u32x2 ld8_dev(rsrc_t r, unsigned off) { return __builtin_amdgcn_raw_buffer_load_b64(r, off, 0, 16); }
DI void st16_dev(rsrc_t r, unsigned off, u32x4 v) { __builtin_amdgcn_raw_buffer_store_b128(v, r, off, 0, 16); }
DI void wait_vm0() { asm volatile("s_waitcnt vmcnt(0)" ::: "memory"); }

struct Prep {
  unsigned sdA, sdB;
  unsigned saA, saB;
  unsigned de;
};
struct Raw { unsigned src, pda, w1, n320; };

#pragma clang diagnostic ignored "-Winline-asm"
#define T4(op, d, s0, s1, s2, s3, d1, d2, d3) op " v" #d ", v" #s0 "\n\t" op " v" #d1 ", v" #s1 "\n\t" op " v" #d2 ", v" #s2 "\n\t" op " v" #d3 ", v" #s3 "\n\t"
#define EXP_LO(a, b, c, d) T4("v_exp_f32", 184, a, b, c, d, 185, 186, 187)
#define EXP_HI(a, b, c, d) T4("v_exp_f32", 188, a, b, c, d, 189, 190, 191)
#define RCP_LO T4("v_rcp_f32", 184, 184, 185, 186, 187, 185, 186, 187)
#define RCP_HI T4("v_rcp_f32", 188, 188, 189, 190, 191, 189, 190, 191)
#define PKA(a, b) "v_pk_add_f32 v[" #a ":" #b "], v[" #a ":" #b "], 1.0 op_sel_hi:[1,0]\n\t"
#define ADD1 PKA(184, 185) PKA(186, 187) PKA(188, 189) PKA(190, 191)
#define PKM(a, b, c, d) "v_pk_mul_f32 v[" #a ":" #b "], v[" #a ":" #b "], v[" #c ":" #d "]\n\t"
#define MULY(a0, a1, a2, a3, a4, a5, a6, a7) PKM(184, 185, a0, a1) PKM(186, 187, a2, a3) PKM(188, 189, a4, a5) PKM(190, 191, a6, a7)
#define CVT(d, a, b) "v_cvt_pk_bf16_f32 v" #d ", v" #a ", v" #b "\n\t"
#define PACK(d0, d1, d2, d3) CVT(d0, 184, 185) CVT(d1, 186, 187) CVT(d2, 188, 189) CVT(d3, 190, 191)
#define MF(acc, a, b) "v_mfma_f32_32x32x16_bf16 " acc ", " a ", " b ", " acc "\n\t"
#define MF0(acc, a, b) "v_mfma_f32_32x32x16_bf16 " acc ", " a ", " b ", 0\n\t"
#define Y0 "v[192:207]"
#define Y1 "v[208:223]"
#define ACC0 "v[224:239]"
#define ACC1 "v[240:255]"
#define X0 "v[176:179]"
#define X1 "v[180:183]"

DI void tiles_init() {
  asm volatile(
               "v_mov_b64 v[224:225], 0\n\t"
               "v_mov_b64 v[226:227], 0\n\t"
               "v_mov_b64 v[228:229], 0\n\t"
               "v_mov_b64 v[230:231], 0\n\t"
               "v_mov_b64 v[232:233], 0\n\t"
               "v_mov_b64 v[234:235], 0\n\t"
               "v_mov_b64 v[236:237], 0\n\t"
               "v_mov_b64 v[238:239], 0\n\t"
               "v_mov_b64 v[240:241], 0\n\t"
               "v_mov_b64 v[242:243], 0\n\t"
               "v_mov_b64 v[244:245], 0\n\t"
               "v_mov_b64 v[246:247], 0\n\t"
               "v_mov_b64 v[248:249], 0\n\t"
               "v_mov_b64 v[250:251], 0\n\t"
               "v_mov_b64 v[252:253], 0\n\t"
               "v_mov_b64 v[254:255], 0\n\t"
               "v_mov_b64 v[180:181], 0\n\t"
               "v_mov_b64 v[182:183], 0\n\t"
               "s_nop 1" ::: "v166", "v255");
}
DI void alpha1(const bf16x8& asl) {
  asm volatile(EXP_LO(192, 193, 194, 195) MF(ACC1, "%[asl]", X1) EXP_HI(196, 197, 198, 199) :: [asl] "v"(asl));
}
DI void alpha2(const bf16x8& ax, const bf16x8& bx, const bf16x8& q0, const bf16x8& w0) {
  asm volatile(ADD1 MF0(Y1, "%[ax]", "%[bx]") RCP_LO MF(Y1, "%[q0]", "%[w0]") RCP_HI
               MULY(192, 193, 194, 195, 196, 197, 198, 199) PACK(176, 177, 178, 179)
               :: [ax] "v"(ax), [bx] "v"(bx), [q0] "v"(q0), [w0] "v"(w0));
}
DI void alpha3(const bf16x8& q1, const bf16x8& w1, const bf16x8& q2, const bf16x8& w2, const bf16x8& q3, const bf16x8& w3, const bf16x8& as0) {
  asm volatile(MF(Y1, "%[q1]", "%[w1]") EXP_LO(200, 201, 202, 203) MF(Y1, "%[q2]", "%[w2]") EXP_HI(204, 205, 206, 207) ADD1
               MF(Y1, "%[q3]", "%[w3]") RCP_LO MF(ACC0, "%[as]", X0) RCP_HI
               MULY(200, 201, 202, 203, 204, 205, 206, 207) PACK(180, 181, 182, 183)
               :: [q1] "v"(q1), [w1] "v"(w1), [q2] "v"(q2), [w2] "v"(w2), [q3] "v"(q3), [w3] "v"(w3), [as] "v"(as0));
}
DI void beta1(const bf16x8& as1) {
  asm volatile(EXP_LO(208, 209, 210, 211) MF(ACC0, "%[as]", X1) EXP_HI(212, 213, 214, 215) ADD1 :: [as] "v"(as1));
}
DI void beta2(const bf16x8& ax, const bf16x8& bx, const bf16x8& q0, const bf16x8& w0) {
  asm volatile(MF0(Y0, "%[ax]", "%[bx]") RCP_LO MF(Y0, "%[q0]", "%[w0]") RCP_HI
               MULY(208, 209, 210, 211, 212, 213, 214, 215) PACK(176, 177, 178, 179)
               :: [ax] "v"(ax), [bx] "v"(bx), [q0] "v"(q0), [w0] "v"(w0));
}
DI void beta3(const bf16x8& q1, const bf16x8& w1, const bf16x8& q2, const bf16x8& w2, const bf16x8& q3, const bf16x8& w3, const bf16x8& as0) {
  asm volatile(MF(Y0, "%[q1]", "%[w1]") EXP_LO(216, 217, 218, 219) MF(Y0, "%[q2]", "%[w2]") EXP_HI(220, 221, 222, 223) ADD1
               MF(Y0, "%[q3]", "%[w3]") RCP_LO MF(ACC1, "%[as]", X0) RCP_HI
               MULY(216, 217, 218, 219, 220, 221, 222, 223) PACK(180, 181, 182, 183)
               :: [q1] "v"(q1), [w1] "v"(w1), [q2] "v"(q2), [w2] "v"(w2), [q3] "v"(q3), [w3] "v"(w3), [as] "v"(as0));
}
DI void chain_y0(const bf16x8& ax, const bf16x8& bx, const bf16x8& q0, const bf16x8& w0, const bf16x8& q1, const bf16x8& w1,
                 const bf16x8& q2, const bf16x8& w2, const bf16x8& q3, const bf16x8& w3) {
  asm volatile("s_nop 1\n\t" MF0(Y0, "%[ax]", "%[bx]") MF(Y0, "%[q0]", "%[w0]") MF(Y0, "%[q1]", "%[w1]") MF(Y0, "%[q2]", "%[w2]")
               MF(Y0, "%[q3]", "%[w3]") "s_nop 15"
               :: [ax] "v"(ax), [bx] "v"(bx), [q0] "v"(q0), [w0] "v"(w0), [q1] "v"(q1), [w1] "v"(w1), [q2] "v"(q2), [w2] "v"(w2),
                  [q3] "v"(q3), [w3] "v"(w3));
}
DI void last_block(const bf16x8& as1) { asm volatile("s_nop 1\n\t" MF(ACC1, "%[as]", X1) :: [as] "v"(as1)); }
DI void acc_gather(unsigned a0, unsigned a1, unsigned a2, unsigned a3) {
  f32x2 t0, t1, t2, t3, t4, t5, t6, t7;
  asm volatile(
               "ds_read2_b32 %[t0], %[a0] offset0:0 offset1:64\n\t"
               "ds_read2_b32 %[t1], %[a0] offset0:128 offset1:192\n\t"
               "ds_read2_b32 %[t2], %[a0] offset0:32 offset1:96\n\t"
               "ds_read2_b32 %[t3], %[a0] offset0:160 offset1:224\n\t"
               "ds_read2_b32 %[t4], %[a1] offset0:0 offset1:64\n\t"
               "ds_read2_b32 %[t5], %[a1] offset0:128 offset1:192\n\t"
               "ds_read2_b32 %[t6], %[a1] offset0:32 offset1:96\n\t"
               "ds_read2_b32 %[t7], %[a1] offset0:160 offset1:224\n\t"
               "s_waitcnt lgkmcnt(4)\n\t"
               "v_pk_add_f32 v[224:225], v[224:225], %[t0]\n\t"
               "v_pk_add_f32 v[226:227], v[226:227], %[t1]\n\t"
               "v_pk_add_f32 v[240:241], v[240:241], %[t2]\n\t"
               "v_pk_add_f32 v[242:243], v[242:243], %[t3]\n\t"
               "s_waitcnt lgkmcnt(0)\n\t"
               "v_pk_add_f32 v[228:229], v[228:229], %[t4]\n\t"
               "v_pk_add_f32 v[230:231], v[230:231], %[t5]\n\t"
               "v_pk_add_f32 v[244:245], v[244:245], %[t6]\n\t"
               "v_pk_add_f32 v[246:247], v[246:247], %[t7]\n\t"
               "ds_read2_b32 %[t0], %[a2] offset0:0 offset1:64\n\t"
               "ds_read2_b32 %[t1], %[a2] offset0:128 offset1:192\n\t"
               "ds_read2_b32 %[t2], %[a2] offset0:32 offset1:96\n\t"
               "ds_read2_b32 %[t3], %[a2] offset0:160 offset1:224\n\t"
               "ds_read2_b32 %[t4], %[a3] offset0:0 offset1:64\n\t"
               "ds_read2_b32 %[t5], %[a3] offset0:128 offset1:192\n\t"
               "ds_read2_b32 %[t6], %[a3] offset0:32 offset1:96\n\t"
               "ds_read2_b32 %[t7], %[a3] offset0:160 offset1:224\n\t"
               "s_waitcnt lgkmcnt(4)\n\t"
               "v_pk_add_f32 v[232:233], v[232:233], %[t0]\n\t"
               "v_pk_add_f32 v[234:235], v[234:235], %[t1]\n\t"
               "v_pk_add_f32 v[248:249], v[248:249], %[t2]\n\t"
               "v_pk_add_f32 v[250:251], v[250:251], %[t3]\n\t"
               "s_waitcnt lgkmcnt(0)\n\t"
               "v_pk_add_f32 v[236:237], v[236:237], %[t4]\n\t"
               "v_pk_add_f32 v[238:239], v[238:239], %[t5]\n\t"
               "v_pk_add_f32 v[252:253], v[252:253], %[t6]\n\t"
               "v_pk_add_f32 v[254:255], v[254:255], %[t7]\n\t"
               "s_nop 0"
               : [t0] "=&v"(t0), [t1] "=&v"(t1), [t2] "=&v"(t2), [t3] "=&v"(t3), [t4] "=&v"(t4), [t5] "=&v"(t5), [t6] "=&v"(t6), [t7] "=&v"(t7)
               : [a0] "v"(a0), [a1] "v"(a1), [a2] "v"(a2), [a3] "v"(a3) : "memory");
}
DI void acc_store(unsigned a0, unsigned a1, unsigned a2, unsigned a3) {
  asm volatile(
               "ds_write2_b32 %[a0], v224, v225 offset0:0 offset1:64\n\t"
               "ds_write2_b32 %[a0], v226, v227 offset0:128 offset1:192\n\t"
               "ds_write2_b32 %[a0], v240, v241 offset0:32 offset1:96\n\t"
               "ds_write2_b32 %[a0], v242, v243 offset0:160 offset1:224\n\t"
               "ds_write2_b32 %[a1], v228, v229 offset0:0 offset1:64\n\t"
               "ds_write2_b32 %[a1], v230, v231 offset0:128 offset1:192\n\t"
               "ds_write2_b32 %[a1], v244, v245 offset0:32 offset1:96\n\t"
               "ds_write2_b32 %[a1], v246, v247 offset0:160 offset1:224\n\t"
               "ds_write2_b32 %[a2], v232, v233 offset0:0 offset1:64\n\t"
               "ds_write2_b32 %[a2], v234, v235 offset0:128 offset1:192\n\t"
               "ds_write2_b32 %[a2], v248, v249 offset0:32 offset1:96\n\t"
               "ds_write2_b32 %[a2], v250, v251 offset0:160 offset1:224\n\t"
               "ds_write2_b32 %[a3], v236, v237 offset0:0 offset1:64\n\t"
               "ds_write2_b32 %[a3], v238, v239 offset0:128 offset1:192\n\t"
               "ds_write2_b32 %[a3], v252, v253 offset0:32 offset1:96\n\t"
               "ds_write2_b32 %[a3], v254, v255 offset0:160 offset1:224\n\t"
               "s_waitcnt lgkmcnt(0)\n\t"
               "v_mov_b64 v[224:225], 0\n\t"
               "v_mov_b64 v[226:227], 0\n\t"
               "v_mov_b64 v[228:229], 0\n\t"
               "v_mov_b64 v[230:231], 0\n\t"
               "v_mov_b64 v[232:233], 0\n\t"
               "v_mov_b64 v[234:235], 0\n\t"
               "v_mov_b64 v[236:237], 0\n\t"
               "v_mov_b64 v[238:239], 0\n\t"
               "v_mov_b64 v[240:241], 0\n\t"
               "v_mov_b64 v[242:243], 0\n\t"
               "v_mov_b64 v[244:245], 0\n\t"
               "v_mov_b64 v[246:247], 0\n\t"
               "v_mov_b64 v[248:249], 0\n\t"
               "v_mov_b64 v[250:251], 0\n\t"
               "v_mov_b64 v[252:253], 0\n\t"
               "v_mov_b64 v[254:255], 0\n\t"
               "s_nop 7"
               :: [a0] "v"(a0), [a1] "v"(a1), [a2] "v"(a2), [a3] "v"(a3) : "memory");
}
DI void acc_gather_last(unsigned a0, unsigned a1, unsigned a2, unsigned a3) {
  asm volatile(
               "ds_read2_b32 v[176:177], %[a0] offset0:0 offset1:64\n\t"
               "ds_read2_b32 v[178:179], %[a0] offset0:128 offset1:192\n\t"
               "ds_read2_b32 v[180:181], %[a0] offset0:32 offset1:96\n\t"
               "ds_read2_b32 v[182:183], %[a0] offset0:160 offset1:224\n\t"
               "ds_read2_b32 v[184:185], %[a1] offset0:0 offset1:64\n\t"
               "ds_read2_b32 v[186:187], %[a1] offset0:128 offset1:192\n\t"
               "ds_read2_b32 v[188:189], %[a1] offset0:32 offset1:96\n\t"
               "ds_read2_b32 v[190:191], %[a1] offset0:160 offset1:224\n\t"
               "ds_read2_b32 v[192:193], %[a2] offset0:0 offset1:64\n\t"
               "ds_read2_b32 v[194:195], %[a2] offset0:128 offset1:192\n\t"
               "ds_read2_b32 v[196:197], %[a2] offset0:32 offset1:96\n\t"
               "ds_read2_b32 v[198:199], %[a2] offset0:160 offset1:224\n\t"
               "ds_read2_b32 v[200:201], %[a3] offset0:0 offset1:64\n\t"
               "ds_read2_b32 v[202:203], %[a3] offset0:128 offset1:192\n\t"
               "ds_read2_b32 v[204:205], %[a3] offset0:32 offset1:96\n\t"
               "ds_read2_b32 v[206:207], %[a3] offset0:160 offset1:224\n\t"
               "s_waitcnt lgkmcnt(12)\n\t"
               "v_pk_add_f32 v[224:225], v[224:225], v[176:177]\n\t"
               "v_pk_add_f32 v[226:227], v[226:227], v[178:179]\n\t"
               "v_pk_add_f32 v[240:241], v[240:241], v[180:181]\n\t"
               "v_pk_add_f32 v[242:243], v[242:243], v[182:183]\n\t"
               "s_waitcnt lgkmcnt(8)\n\t"
               "v_pk_add_f32 v[228:229], v[228:229], v[184:185]\n\t"
               "v_pk_add_f32 v[230:231], v[230:231], v[186:187]\n\t"
               "v_pk_add_f32 v[244:245], v[244:245], v[188:189]\n\t"
               "v_pk_add_f32 v[246:247], v[246:247], v[190:191]\n\t"
               "s_waitcnt lgkmcnt(4)\n\t"
               "v_pk_add_f32 v[232:233], v[232:233], v[192:193]\n\t"
               "v_pk_add_f32 v[234:235], v[234:235], v[194:195]\n\t"
               "v_pk_add_f32 v[248:249], v[248:249], v[196:197]\n\t"
               "v_pk_add_f32 v[250:251], v[250:251], v[198:199]\n\t"
               "s_waitcnt lgkmcnt(0)\n\t"
               "v_pk_add_f32 v[236:237], v[236:237], v[200:201]\n\t"
               "v_pk_add_f32 v[238:239], v[238:239], v[202:203]\n\t"
               "v_pk_add_f32 v[252:253], v[252:253], v[204:205]\n\t"
               "v_pk_add_f32 v[254:255], v[254:255], v[206:207]\n\t"
               "s_nop 0"
               :: [a0] "v"(a0), [a1] "v"(a1), [a2] "v"(a2), [a3] "v"(a3) : "memory");
}
DI void acc_store_last(unsigned a0, unsigned a1, unsigned a2, unsigned a3) {
  asm volatile(
               "ds_write2_b32 %[a0], v224, v225 offset0:0 offset1:64\n\t"
               "ds_write2_b32 %[a0], v226, v227 offset0:128 offset1:192\n\t"
               "ds_write2_b32 %[a0], v240, v241 offset0:32 offset1:96\n\t"
               "ds_write2_b32 %[a0], v242, v243 offset0:160 offset1:224\n\t"
               "ds_write2_b32 %[a1], v228, v229 offset0:0 offset1:64\n\t"
               "ds_write2_b32 %[a1], v230, v231 offset0:128 offset1:192\n\t"
               "ds_write2_b32 %[a1], v244, v245 offset0:32 offset1:96\n\t"
               "ds_write2_b32 %[a1], v246, v247 offset0:160 offset1:224\n\t"
               "ds_write2_b32 %[a2], v232, v233 offset0:0 offset1:64\n\t"
               "ds_write2_b32 %[a2], v234, v235 offset0:128 offset1:192\n\t"
               "ds_write2_b32 %[a2], v248, v249 offset0:32 offset1:96\n\t"
               "ds_write2_b32 %[a2], v250, v251 offset0:160 offset1:224\n\t"
               "ds_write2_b32 %[a3], v236, v237 offset0:0 offset1:64\n\t"
               "ds_write2_b32 %[a3], v238, v239 offset0:128 offset1:192\n\t"
               "ds_write2_b32 %[a3], v252, v253 offset0:32 offset1:96\n\t"
               "ds_write2_b32 %[a3], v254, v255 offset0:160 offset1:224\n\t"
               "s_nop 0"
               :: [a0] "v"(a0), [a1] "v"(a1), [a2] "v"(a2), [a3] "v"(a3) : "memory");
}
DI unsigned lds_lock(unsigned addr) {
  unsigned t, old;
  unsigned long long sv;
  asm volatile("s_mov_b64 %2, exec\n\ts_mov_b64 exec, 1\n"
               "1:\n\tds_wrxchg_rtn_b32 %0, %3, %4\n\ts_waitcnt lgkmcnt(0)\n\tv_cmp_eq_u32 vcc, 1, %0\n\ts_cbranch_vccz 2f\n\t"
               "s_sleep 1\n\ts_branch 1b\n"
               "2:\n\tv_readfirstlane_b32 %1, %0\n\ts_mov_b64 exec, %2\n\ts_nop 15"
               : "=&v"(t), "=&s"(old), "=&s"(sv) : "v"(addr), "v"(1u) : "vcc", "memory");
  return old;
}

template <typename T> DI T* karg(int k) {
  typedef __attribute__((address_space(4))) const unsigned long long* kp_t;
  kp_t kp = (kp_t)__builtin_amdgcn_kernarg_segment_ptr();
  asm volatile("" : "+s"(kp));
  return (T*)(__attribute__((address_space(1))) T*)kp[k];
}

__global__ __launch_bounds__(512) __attribute__((amdgpu_num_vgpr(83))) void egnn_kernel(
    const u16* __restrict__ h_, const u16* __restrict__ pos_, const u16* __restrict__ we_, const u16* __restrict__ wx_,
    const u16* __restrict__ wh_, char* __restrict__ ws_, u16* __restrict__ out_, const int* __restrict__ src_,
    const int* __restrict__ dst_, const u16* __restrict__ ea_) {
  __shared__ __attribute__((aligned(1024))) char lds[L_END];
  AS3 char* L = (AS3 char*)lds;
  const unsigned LB = (unsigned)(size_t)L;
  const int tid_ = threadIdx.x;
  const int w = __builtin_amdgcn_readfirstlane(tid_ >> 6);
  const int xl = blockIdx.x & 7;
  const int b = xl * 31 + (xl < 2 ? xl : 2) + (blockIdx.x >> 3);
  const int n0 = b * BN;
  unsigned cw[2][2];
  bf16x8 wq[2][4];
  unsigned aq[4], sa_c, s_rd, s_tr, acc_l;
  const unsigned ringb = L_RING + w * 4096;

  {
    const int tid = tid_, lane = tid & 63, r = lane & 31, hf = lane >> 5, p = blockIdx.x;
    const rsrc_t rs = ws_rsrc(ws_);
    const int e0 = p * CH + 4 * tid, e1 = p * CH + 2048 + tid;
    const i32x4 d4 = *(const i32x4*)(dst_ + e0), s4 = *(const i32x4*)(src_ + e0);
    const u32x2 a2 = *(const u32x2*)(ea_ + e0);
    unsigned dd[5] = {(unsigned)d4[0], (unsigned)d4[1], (unsigned)d4[2], (unsigned)d4[3], (unsigned)dst_[e1]};
    const unsigned s5 = src_[e1], a5 = ea_[e1];
    const u32x2 ctl = ld8_dev(rs, WS_CTL);
    SB();
    if (w == 7) {
      unsigned long long pc;
      asm volatile("s_getpc_b64 %0" : "=s"(pc));
      asm volatile("global_load_dword v184, %0, %1\n\tglobal_load_dword v185, %2, %1" :: "v"(lane * 128u), "s"(pc), "v"(lane * 128u + 8192u) : "memory");
      if (lane < (CODE_BYTES - 16384) / 128) asm volatile("global_load_dword v186, %0, %1" :: "v"(lane * 128u + 16384u), "s"(pc) : "memory");
    }
    dma_piece((const char*)we_ + w * 1024, lane * 16, LB + L_W + w * 1024);
    if (w < 5) dma_piece((const char*)h_ + (size_t)n0 * 128 + w * 1024, lane * 16, LB + L_H + w * 1024);
    const u32x4 w2raw = *(const u32x4*)(we_ + 64 * 64 + tid * 8);
    unsigned sraw[3], craw[4];
#pragma unroll
    for (int t = 0; t < 2; ++t) { craw[2 * t] = we_[128 * 64 + 32 * t + r]; craw[2 * t + 1] = we_[129 * 64 + 32 * t + r]; }
    {
      const u16* pp = w == 1 ? wx_ + lane : pos_ + (lane < BN ? n0 + lane : 0) * 3;
      const int st = w == 1 ? 0 : 1;
      sraw[0] = pp[0]; sraw[1] = pp[st]; sraw[2] = pp[2 * st];
    }
    SB();
    {
      const u32x4 z4 = {0, 0, 0, 0};
      static_assert(8 * S_WAVE == 2 * 8192 + 256 * 16 && BN * ACC_NODE == 5 * 8192 + 40 * 16, "clearing pattern");
      if (tid < 256) lds_st<u32x4>(L, L_TBL + 16 * tid, z4);
      lds_st<u32x4>(L, L_S + tid * 16, z4);
      lds_st<u32x4>(L, L_S + 8192 + tid * 16, z4);
#pragma unroll
      for (int k = 0; k < 5; ++k) lds_st<u32x4>(L, L_ACC + k * 8192 + tid * 16, z4);
      if (tid < 256) lds_st<u32x4>(L, L_S + 16384 + tid * 16, z4);
      if (tid < 40) lds_st<u32x4>(L, L_ACC + 5 * 8192 + tid * 16, z4);
      if (tid < 8) lds_st<unsigned>(L, L_LOCK + 4 * tid, 0u);
      if (tid == 8) lds_st<unsigned>(L, L_WT, 0u);
    }
    __syncthreads();
    unsigned idx[5], rank[5], bk[5];
#pragma unroll
    for (int i = 0; i < 5; ++i) {
      const unsigned G = dd[i] >> 3;
      bk[i] = (G * 52429u) >> 18;
      idx[i] = G + 3 * bk[i];
      const unsigned sh = (idx[i] & 1) << 4;
      const unsigned old = __hip_atomic_fetch_add((AS3 unsigned*)(L + L_TBL + 4 * (idx[i] >> 1)), 1u << sh, __ATOMIC_RELAXED,
                                                  __HIP_MEMORY_SCOPE_WORKGROUP);
      rank[i] = (old >> sh) & 0xffffu;
    }
    __syncthreads();
    const unsigned tlo = ctl[0] + 1, thi = ctl[1] + (tlo == 0);
    const unsigned tag0 = tlo & 0xfffffffu, tag1 = ((tag0 * 2654435761u) ^ ((tlo >> 28) | (thi << 4))) & 0xfffffffu;
    u32x4 entry = {0, 0, 0, 0};
    if (w < 4) {
      const u32x4 c = lds_ld<u32x4>(L, L_TBL + 16 * tid);
      const unsigned tot = (c[0] & 0xffffu) + (c[0] >> 16) + (c[1] & 0xffffu) + (c[1] >> 16) + (c[2] & 0xffffu);
      const unsigned incl = wave_incl_scan(tot);
      unsigned old;
      unsigned long long sv;
      asm volatile("s_mov_b64 %1, exec\n\ts_mov_b64 exec, %4\n\tds_add_rtn_u32 %0, %2, %3\n\ts_mov_b64 exec, %1\n\ts_waitcnt lgkmcnt(0)"
                   : "=&v"(old), "=&s"(sv) : "v"(LB + L_WT), "v"(incl), "s"(1ull << 63) : "memory");
      const unsigned s0 = incl - tot + __builtin_amdgcn_readlane(old, 63);
      const unsigned s1 = s0 + (c[0] & 0xffffu), s2 = s1 + (c[0] >> 16), s3 = s2 + (c[1] & 0xffffu), s4_ = s3 + (c[1] >> 16);
      const unsigned s5_ = s0 + tot;
      lds_st<u32x3>(L, L_TBL + 16 * tid, u32x3{s0 | (s1 << 16), s2 | (s3 << 16), s4_});
      entry = u32x4{s0 | (s1 << 12) | (s2 << 24), (s2 >> 8) | (tag0 << 4), s3 | (s4_ << 12) | (s5_ << 24), (s5_ >> 8) | (tag1 << 4)};
    }
    if (tid == 0) {
      lds_st<u32x4>(L, L_TAG, u32x4{tag0, tag1, tlo, thi});
      lds_st<u32x4>(L, L_TOT, u32x4{0, 0, 0, 0});
      lds_st<u32x4>(L, L_TOT + 16, u32x4{0, 0, 0, 0});
    }
    {
      u32x4 o;
#pragma unroll
      for (int k = 0; k < 4; ++k) o[k] = cvt_pk(bf_lo(w2raw[k]) * NLOG2E, bf_hi(w2raw[k]) * NLOG2E);
      lds_st<u32x4>(L, L_W + 8192 + tid * 16, o);
    }
    wait_vm0();
    __syncthreads();
    {
      unsigned ps[5];
#pragma unroll
      for (int i = 0; i < 5; ++i) ps[i] = lds_ld<u16>(L, L_TBL + 2 * idx[i]) + rank[i];
#pragma unroll
      for (int i = 0; i < 5; ++i) {
        const unsigned dl = dd[i] - BN * bk[i];
        const unsigned sv = i < 4 ? (unsigned)s4[i] : s5;
        const unsigned eav = i == 4 ? a5 : (i & 1) ? (a2[i >> 1] >> 16) : a2[i >> 1];
        const unsigned pb = L_RAW + 12 * (ps[i] >> 1), odd = ps[i] & 1;
        lds_st<unsigned>(L, pb + 4 * odd, sv | ((L_PD + 16 * dl) << 16));
        lds_st<u16>(L, pb + 8 + 2 * odd, (u16)eav);
      }
    }
    __syncthreads();
    {
      static_assert(REC_STRIDE == (512 + 448) * 16, "copy pattern");
      const unsigned o = WS_RECS + p * REC_STRIDE + 16 * tid;
      st16_dev(rs, o, lds_ld<u32x4>(L, L_RAW + 16 * tid));
      if (tid < 448) st16_dev(rs, o + 8192, lds_ld<u32x4>(L, L_RAW + 8192 + 16 * tid));
    }
    SB();
    if (w >= 4) {
      const int u = (w - 4) >> 1, t = w & 1;
      f32x16 pacc;
#pragma unroll
      for (int i = 0; i < 16; ++i) pacc[i] = 0.f;
#pragma unroll
      for (int s = 0; s < 4; ++s) {
        const int nl = 32 * u + r, nlc = nl < BN ? nl : BN - 1;
        const bf16x8 ha = lds_ld<bf16x8>(L, L_H + nlc * 128 + 32 * s + 16 * hf);
        const unsigned o = L_W + (16 * s + 8 * (lane >> 5) + ((lane & 15) >> 2)) * 128 + (32 * t + 16 * ((lane >> 4) & 1) + 4 * (lane & 3)) * 2;
        const u32x2 b0 = lds_tr(L, o), b1 = lds_tr(L, o + 512);
        pacc = mfma(ha, as_frag(u32x4{b0[0], b0[1], b1[0], b1[1]}), pacc);
      }
#pragma unroll
      for (int q = 0; q < 4; ++q)
        if (4 * u + q < 5)
          lds_st<u32x2>(L, L_PFR + ((4 * u + q) * 2 + t) * 512 + lane * 8,
                        u32x2{cvt_pk(pacc[4 * q] * NLOG2E, pacc[4 * q + 1] * NLOG2E), cvt_pk(pacc[4 * q + 2] * NLOG2E, pacc[4 * q + 3] * NLOG2E)});
    } else if (w == 0) {
      if (lane <= BN)
        lds_st<f32x4>(L, L_PD + 16 * lane, f32x4{bf_lo(sraw[0]), bf_lo(sraw[1]), bf_lo(sraw[2]), lane < BN ? 1.0f : 0.0f});
    } else if (w == 1) {
      lds_st<float>(L, L_WX + 4 * lane, bf_lo(sraw[0]));
    }
#pragma unroll
    for (int t = 0; t < 2; ++t) {
      const float d = bf_lo(craw[2 * t]) * NLOG2E, a = bf_lo(craw[2 * t + 1]) * NLOG2E;
      const unsigned hi = cvt_pk(d, a);
      cw[t][0] = hi;
      cw[t][1] = cvt_pk(d - bf_lo(hi), a - bf_hi(hi));
    }
    {
#pragma unroll
      for (int s = 0; s < 4; ++s) aq[s] = ringb + r * 128 + (((2 * s + hf) ^ ((r >> 1) & 7)) << 4);
      const unsigned Sb = L_S + w * S_WAVE;
      const int g16 = lane >> 4, tq = (lane & 15) >> 2, tp = lane & 3, tu = g16 & 1, th = g16 >> 1;
      sa_c = Sb + 2 * ((r & 0x13) | ((r & 4) << 1) | ((r & 8) >> 1));
      s_rd = Sb + r * 80 + 16 * hf;
      s_tr = Sb + (4 * (4 * th + tq) + 3) * 80 + 2 * (16 * tu + 8 * (tp & 1) + 4 * (tp >> 1));
      acc_l = L_ACC + hf * ACC_NODE + 4 * r;
#pragma unroll
      for (int t = 0; t < 2; ++t)
#pragma unroll
        for (int s = 0; s < 4; ++s) {
          const unsigned o = L_W + 8192 + (16 * s + 8 * th + tq) * 128 + (32 * t + 16 * tu + 4 * tp) * 2;
          const u32x2 b0 = lds_tr(L, o), b1 = lds_tr(L, o + 512);
          wq[t][s] = as_frag(u32x4{b0[0], b0[1], b1[0], b1[1]});
        }
      const unsigned dc0 = ((lane & 7) ^ ((lane >> 4) & 7)) << 4, dc1 = ((lane & 7) ^ ((4 + (lane >> 4)) & 7)) << 4;
      asm volatile("v_mov_b32 v167, %0\n\tv_mov_b32 v169, %1\n\tv_mov_b32 v171, %0\n\tv_mov_b32 v173, %1" :: "v"(dc0), "v"(dc1));
    }
    wait_vm0();
    __syncthreads();
    if (tid < NBK) st16_dev(rs, WS_ENT + p * 4096 + tid * 16, entry);
  }

  int pa = 0, np = NP;
  for (unsigned it = 0;; ++it) {
    const u16 *h = karg<const u16>(0), *pos = karg<const u16>(1);
    const char* ws = karg<const char>(5);
    const rsrc_t rs = ws_rsrc(ws);
    int tid = tid_;
    asm volatile("" : "+v"(tid));
    const int lane = tid & 63, r = lane & 31, hf = lane >> 5;
    const unsigned l_tot = L_TOT + 16 * (it & 1);
    unsigned pA = 0, pB = 0, pC = 0, d1 = 0, d2 = 0, d3 = 0, d4 = 0;
    if (w < 4) {
      const u32x2 tg = lds_ld<u32x2>(L, L_TAG);
      const unsigned eo = WS_ENT + (tid < NP ? tid : 0) * 4096 + b * 16;
      auto seen = [&](const u32x4& e) { return __all(tid >= NP || ((e[1] >> 4) == tg[0] && (e[3] >> 4) == tg[1])); };
      auto poll = [&]() {
        __builtin_amdgcn_s_sleep(POLL_SLEEP);
        asm volatile("" ::: "memory");
        return ld16_dev(rs, eo);
      };
      u32x4 ent, e0 = ld16_dev(rs, eo);
      for (unsigned spins = 0;; ++spins) {
        const u32x4 e1 = poll();
        if (seen(e0)) { ent = e0; break; }
        e0 = poll();
        ent = e1;
        if (seen(e1)) break;
        if (spins > SPIN_LIMIT) {
          __hip_atomic_fetch_or((AS3 unsigned*)(L + l_tot + 12), 1u, __ATOMIC_RELAXED, __HIP_MEMORY_SCOPE_WORKGROUP);
          break;
        }
      }
      const unsigned s0 = ent[0] & 0xfffu, s1 = (ent[0] >> 12) & 0xfffu, s2 = __builtin_amdgcn_alignbit(ent[1], ent[0], 24) & 0xfffu;
      const unsigned s3 = ent[2] & 0xfffu, s4 = (ent[2] >> 12) & 0xfffu, s5 = __builtin_amdgcn_alignbit(ent[3], ent[2], 24) & 0xfffu;
      const bool act = (unsigned)(tid - pa) < (unsigned)np && tid < NP;
      d1 = s1 - s0; d2 = s2 - s0; d3 = s3 - s0; d4 = s4 - s0;
      const unsigned cnt = act ? s5 - s0 : 0;
      const unsigned cA = act ? d1 | ((s2 - s1) << 16) : 0, cB = act ? (s3 - s2) | ((s4 - s3) << 16) : 0, cC = act ? s5 - s4 : 0;
      lds_st<unsigned>(L, L_SEG + 4 * tid, (tid * (CH / 2) + (s0 >> 1)) | ((cnt + (s0 & 1)) << 19) | (s0 << 31));
      const unsigned iA = wave_incl_scan(cA), iB = wave_incl_scan(cB), iC = wave_incl_scan(cC);
      if (lane == 63) {
        lds_st<u32x3>(L, L_WV + 16 * w, u32x3{iA, iB, iC});
        __hip_atomic_fetch_add((AS3 unsigned*)(L + l_tot), iA, __ATOMIC_RELAXED, __HIP_MEMORY_SCOPE_WORKGROUP);
        __hip_atomic_fetch_add((AS3 unsigned*)(L + l_tot + 4), iB, __ATOMIC_RELAXED, __HIP_MEMORY_SCOPE_WORKGROUP);
        __hip_atomic_fetch_add((AS3 unsigned*)(L + l_tot + 8), iC, __ATOMIC_RELAXED, __HIP_MEMORY_SCOPE_WORKGROUP);
      }
      if (__any(cnt > 255))
        __hip_atomic_fetch_or((AS3 unsigned*)(L + l_tot + 12), 2u, __ATOMIC_RELAXED, __HIP_MEMORY_SCOPE_WORKGROUP);
      pA = iA - cA;
      pB = iB - cB;
      pC = iC - cC;
    }
    __syncthreads();
    const u32x4 tot4 = lds_ld<u32x4>(L, l_tot);
    unsigned e3[4];
#pragma unroll
    for (int k = 0; k < 4; ++k) e3[k] = lds_ld<unsigned>(L, L_SEG + 4 * (64 * k + (tid >> 3)));
    const unsigned flags = __builtin_amdgcn_readfirstlane(tot4[3]);
    if (flags & 1) return;
    if (tid == 511) lds_st<u32x4>(L, L_TOT + 16 * (~it & 1), u32x4{0, 0, 0, 0});
    const unsigned j2 = 2 * (tid & 7);
    u32x3 ra[4], rb[4];
    unsigned cntp[4], ro[4];
#pragma unroll
    for (int k = 0; k < 4; ++k) {
      cntp[k] = (e3[k] >> 19) & 0xfffu;
      ro[k] = ((e3[k] & 0x7ffffu) + (tid & 7)) * 12 + WS_RECS;
      ra[k] = ld12_rec(rs, j2 < cntp[k] ? ro[k] : NO_DATA);
    }
#pragma unroll
    for (int k = 0; k < 4; ++k) rb[k] = ld12_rec(rs, j2 + 16 < cntp[k] ? ro[k] + 96 : NO_DATA);
    SB();
    const unsigned tA = __builtin_amdgcn_readfirstlane(tot4[0]), tB = __builtin_amdgcn_readfirstlane(tot4[1]);
    const unsigned tot[5] = {tA & 0xffffu, tA >> 16, tB & 0xffffu, tB >> 16, (unsigned)__builtin_amdgcn_readfirstlane(tot4[2])};
    unsigned base[6];
    base[0] = 0;
#pragma unroll
    for (int g = 0; g < 5; ++g) base[g + 1] = base[g] + ((tot[g] + 31) & ~31u);
    if (((flags & 2) && np > 16) || base[5] > (unsigned)CAP) {
      np = (np + 1) >> 1;
      __syncthreads();
      continue;
    }
    if (w < 4) {
      constexpr unsigned R8 = L_RAW / 8;
#pragma unroll
      for (int v = 0; v < 3; ++v) {
        const u32x3 t3 = lds_ld<u32x3>(L, L_WV + 16 * v);
        if (v < w) { pA += t3[0]; pB += t3[1]; pC += t3[2]; }
      }
      const unsigned v0 = ((pA & 0xffffu) + (base[0] + R8)) << 3, v1 = ((pA >> 16) - d1 + (base[1] + R8)) << 3;
      const unsigned v2 = ((pB & 0xffffu) - d2 + (base[2] + R8)) << 3, v3 = ((pB >> 16) - d3 + (base[3] + R8)) << 3;
      const unsigned v4 = (pC - d4 + (base[4] + R8)) << 3;
      lds_st<u32x3>(L, L_TBL + 16 * tid, u32x3{(v0 & 0xffffu) | (v1 << 16), (v2 & 0xffffu) | (v3 << 16), v4});
    } else {
      const u32x2 pad = {(unsigned)(L_PD + 16 * BN) << 16, 0};
      const unsigned lo0 = w == 4 ? base[0] + tot[0] : w == 5 ? base[2] + tot[2] : base[4] + tot[4];
      const unsigned lo1 = w == 4 ? base[1] : w == 5 ? base[3] : base[5];
      const unsigned hi0 = w == 4 ? base[1] + tot[1] : base[3] + tot[3], hi1 = w == 4 ? base[2] : base[4];
      const unsigned slot = (hf ? hi0 : lo0) + r;
      if (w < 7 && slot < (hf ? (w == 6 ? 0u : hi1) : lo1)) lds_st<u32x2>(L, L_RAW + 8 * slot, pad);
      if (tid - 256 < PADS) lds_st<u32x2>(L, L_RAW + 8 * (base[5] + tid - 256), pad);
    }

    const int T = base[5] >> 5;
    const int q4 = T >> 2, r4 = T & 3, ws4 = w & 3;
    const int sa4 = ws4 * q4 + (ws4 < r4 ? ws4 : r4), sn4 = q4 + (ws4 < r4 ? 1 : 0);
    const int nold = (sn4 * 44 + 32) >> 6;
    const int ta = w < 4 ? sa4 : sa4 + nold;
    const int tb = w < 4 ? sa4 + nold : sa4 + sn4;
    const int tg1 = base[1] >> 5, tg2 = base[2] >> 5, tg3 = base[3] >> 5, tg4 = base[4] >> 5;

    const char* pos6 = (const char*)pos;
    const rsrc_t h_rows = __builtin_amdgcn_make_buffer_rsrc((void*)((const char*)h - 3072), 128, 0xffffffffu, 0x00020000);
    auto group_of = [&](int t) {
      int g;
      asm("s_cmp_ge_i32 %1, %2\n\ts_cselect_b32 %0, 1, 0\n\ts_cmp_ge_i32 %1, %3\n\ts_addc_u32 %0, %0, 0\n\t"
          "s_cmp_ge_i32 %1, %4\n\ts_addc_u32 %0, %0, 0\n\ts_cmp_ge_i32 %1, %5\n\ts_addc_u32 %0, %0, 0"
          : "=&s"(g) : "s"(t), "s"(tg1), "s"(tg2), "s"(tg3), "s"(tg4) : "scc");
      return g;
    };
    auto group_end = [&](int t) {
      int e;
      asm("s_cmp_gt_i32 %5, %1\n\ts_cselect_b32 %0, %5, %6\n\ts_cmp_gt_i32 %4, %1\n\ts_cselect_b32 %0, %4, %0\n\t"
          "s_cmp_gt_i32 %3, %1\n\ts_cselect_b32 %0, %3, %0\n\ts_cmp_gt_i32 %2, %1\n\ts_cselect_b32 %0, %2, %0"
          : "=&s"(e) : "s"(t), "s"(tg1), "s"(tg2), "s"(tg3), "s"(tg4), "s"(T) : "scc");
      return e;
    };
    int gend = group_end(ta);
    int tflush = -1;

    bf16x8 bxA0, bxA1, bxB0, bxB1;
    auto load_bx = [&](int gg) {
      const u32x2 p0 = lds_ld<u32x2>(L, L_PFR + gg * 1024 + lane * 8), p1 = lds_ld<u32x2>(L, L_PFR + gg * 1024 + 512 + lane * 8);
      bxA0 = as_frag(u32x4{p0[0], p0[1], hf ? 0u : cw[0][0], hf ? 0u : cw[0][1]});
      bxB0 = as_frag(u32x4{p0[0], p0[1], hf ? cw[0][0] : 0u, hf ? cw[0][1] : 0u});
      bxA1 = as_frag(u32x4{p1[0], p1[1], hf ? 0u : cw[1][0], hf ? 0u : cw[1][1]});
      bxB1 = as_frag(u32x4{p1[0], p1[1], hf ? cw[1][0] : 0u, hf ? cw[1][1] : 0u});
      asm volatile("" : "+v"(bxA0), "+v"(bxB0), "+v"(bxA1), "+v"(bxB1));
    };
    load_bx(group_of(ta));
    tiles_init();
    set_m0(LB + ringb);
    __syncthreads();

    {
      auto second = [](unsigned w0, unsigned eahi) { return (((w0 >> 20) & 7) * 320) | eahi; };
      const unsigned tb0 = L_TBL + 16 * (tid >> 3);
      unsigned a0[4], a1[4], r8[4];
#pragma unroll
      for (int k = 0; k < 4; ++k) {
        r8[k] = 8 * j2 - ((e3[k] >> 31) << 3);
        a0[k] = lds_ld<u16>(L, tb0 + 1024 * k + ((ra[k][0] >> 22) & 14)) + r8[k];
        a1[k] = lds_ld<u16>(L, tb0 + 1024 * k + ((ra[k][1] >> 22) & 14)) + r8[k];
      }
#pragma unroll
      for (int k = 0; k < 4; ++k) {
        if (j2 < cntp[k] && (int)r8[k] >= 0) lds_st<u32x2>(L, a0[k], u32x2{ra[k][0], second(ra[k][0], ra[k][2] << 16)});
        if (j2 + 1 < cntp[k]) lds_st<u32x2>(L, a1[k] + 8, u32x2{ra[k][1], second(ra[k][1], ra[k][2] & 0xffff0000u)});
      }
#pragma unroll
      for (int k = 0; k < 4; ++k) {
        if (j2 + 16 < cntp[k]) {
          const unsigned b0 = lds_ld<u16>(L, tb0 + 1024 * k + ((rb[k][0] >> 22) & 14)) + r8[k];
          const unsigned b1 = lds_ld<u16>(L, tb0 + 1024 * k + ((rb[k][1] >> 22) & 14)) + r8[k];
          lds_st<u32x2>(L, b0 + 128, u32x2{rb[k][0], second(rb[k][0], rb[k][2] << 16)});
          if (j2 + 17 < cntp[k]) lds_st<u32x2>(L, b1 + 136, u32x2{rb[k][1], second(rb[k][1], rb[k][2] & 0xffff0000u)});
        }
      }
#pragma unroll
      for (int k = 0; k < 4; ++k) {
        const unsigned odd = e3[k] >> 31;
        for (unsigned q = (tid & 7) + 32; q < cntp[k]; q += 8) {
          const unsigned pb = ((e3[k] & 0x7ffffu) + (q >> 1)) * 12 + WS_RECS;
          const unsigned w0 = ld4_rec(rs, pb + 4 * (q & 1)), eav = ld2_rec(rs, pb + 8 + 2 * (q & 1));
          const unsigned a = lds_ld<u16>(L, tb0 + 1024 * k + ((w0 >> 22) & 14));
          lds_st<u32x2>(L, a + 8 * (q - odd), u32x2{w0, second(w0, eav << 16)});
        }
      }
    }
    __syncthreads();

    if (pa == 0) {
      const char* wh = karg<const char>(4);
      dma_piece(wh + w * 2048, lane * 16, LB + L_W + w * 2048);
      dma_piece(wh + w * 2048 + 1024, lane * 16, LB + L_W + w * 2048 + 1024);
    }
    if (ta < tb) {
      auto flush = [&](int gg) {
        const unsigned a = LB + acc_l + gg * (8 * ACC_NODE);
        if (lds_lock(LB + L_LOCK + 4 * gg) != 0) acc_gather(a, a + 2 * ACC_NODE, a + 4 * ACC_NODE, a + 6 * ACC_NODE);
        acc_store(a, a + 2 * ACC_NODE, a + 4 * ACC_NODE, a + 6 * ACC_NODE);
        lds_st<unsigned>(L, L_LOCK + 4 * gg, 2u);
        asm volatile("" ::: "memory");
      };
      auto flush_last = [&](int gg) {
        const unsigned a = LB + acc_l + gg * (8 * ACC_NODE);
        if (lds_lock(LB + L_LOCK + 4 * gg) != 0) acc_gather_last(a, a + 2 * ACC_NODE, a + 4 * ACC_NODE, a + 6 * ACC_NODE);
        acc_store_last(a, a + 2 * ACC_NODE, a + 4 * ACC_NODE, a + 6 * ACC_NODE);
        lds_st<unsigned>(L, L_LOCK + 4 * gg, 2u);
        asm volatile("" ::: "memory");
      };
      unsigned a_idx = LB + L_RAW + ta * 256 + (lane >> 3) * 8, a_raw = L_RAW + ta * 256 + lane * 8;
      auto raw_load = [&](int dt) {
        const unsigned a = a_raw + dt * 256;
        return Raw{lds_ld<u16>(L, a), lds_ld<u16>(L, a + 2), lds_ld<unsigned>(L, a + 4), lds_ld<u16>(L, a + 4)};
      };
      auto pos_load = [&](const Raw& x) {
        asm volatile("global_load_dwordx2 v[174:175], %0, %1" :: "v"(x.src * 6u), "s"(pos6) : "memory");
      };
      auto pd_load = [&](const Raw& x) { return lds_ld<f32x4>(L, x.pda); };
      auto prep = [&](const Raw& x, const f32x4& pd) {
        float xs, ys, zs;
        asm volatile("v_lshlrev_b32 %0, 16, v174\n\tv_and_b32 %1, 0xffff0000, v174\n\tv_lshlrev_b32 %2, 16, v175"
                     : "=v"(xs), "=v"(ys), "=v"(zs));
        const f32x2 dxy = f32x2{pd[0], pd[1]} - f32x2{xs, ys};
        unsigned wa = cvt_pk(dxy[0], dxy[1]), wb = cvt_pk(pd[2] - zs, pd[3]);
        const float dzr = bf_lo(wb);
        const bf16x2v wav = __builtin_bit_cast(bf16x2v, wa);
        const float d2 = __builtin_amdgcn_fdot2_f32_bf16(wav, wav, dzr * dzr, false);
        Prep o;
        o.de = cvt_pk(d2, as_f(x.w1));
        asm volatile("" : "+v"(o.de), "+v"(wa), "+v"(wb));
        const unsigned P = x.n320 + sa_c, Q = P + 160;
        const auto sa = __builtin_amdgcn_permlane32_swap(P, Q, false, false);
        const auto sd = __builtin_amdgcn_permlane32_swap(wa, wb, false, false);
        o.saA = sa[0]; o.saB = sa[1]; o.sdA = sd[0]; o.sdB = sd[1];
        return o;
      };

      struct Ops { bf16x8 q0, q1, q2, q3, as0, as1, ax; };
      auto read_ops = [&](unsigned sa, unsigned sd, unsigned de) {
        Ops o;
        o.q0 = lds_ld<bf16x8>(L, aq[0]); o.q1 = lds_ld<bf16x8>(L, aq[1]);
        o.q2 = lds_ld<bf16x8>(L, aq[2]); o.q3 = lds_ld<bf16x8>(L, aq[3]);
        lds_st<u16>(L, sa, (u16)sd);
        lds_st<u16>(L, sa + 80, (u16)(sd >> 16));
        const u32x2 oh = lds_tr(L, s_tr);
        o.as0 = lds_ld<bf16x8>(L, s_rd);
        o.as1 = lds_ld<bf16x8>(L, s_rd + 32);
        lds_st<u16>(L, sa, (u16)0);
        lds_st<u16>(L, sa + 80, (u16)0);
        unsigned de2;
        asm volatile("v_mov_b32 %0, %1" : "=v"(de2) : "v"(de));
        o.ax = as_frag(u32x4{oh[0], oh[1], de, de2});
        return o;
      };
      auto alpha = [&](int t, const Ops& c, const bf16x8& asl, const bf16x8& bx1, auto&& mid) {
        alpha1(asl);
        if (__builtin_expect(t == tflush, 0)) flush(group_of(t - 1));
        alpha2(c.ax, bx1, c.q0, wq[1][0]);
        SB(); mid(); SB();
        alpha3(c.q1, wq[1][1], c.q2, wq[1][2], c.q3, wq[1][3], c.as0);
      };
      auto beta = [&](const Ops& c, const Ops& n, const bf16x8& bx0n, auto&& mid) {
        beta1(c.as1);
        beta2(n.ax, bx0n, n.q0, wq[0][0]);
        SB(); mid(); SB();
        beta3(n.q1, wq[0][1], n.q2, wq[0][2], n.q3, wq[0][3], c.as0);
      };
      auto next_group = [&](int t) {
        if (__builtin_expect(t == gend, 0)) { gend = group_end(t); tflush = t; load_bx(group_of(t)); }
      };

      rows_index<0>(a_idx);
      rows_gather(h_rows);
      Raw rw = raw_load(0);
      pos_load(rw);
      f32x4 pd = pd_load(rw);
      SB();
      wait_vm0();
      Prep pr = prep(rw, pd);
      rw = raw_load(2);
      Ops A = read_ops(pr.saA, pr.sdA, pr.de), B;
#pragma unroll
      for (int j = 0; j < 8; ++j) B.as1[j] = 0;
      asm volatile("" : "+v"(B.as1));
      const int glast = group_of(tb - 1);
      rows_index<256>(a_idx);
      chain_y0(A.ax, bxA0, A.q0, wq[0][0], A.q1, wq[0][1], A.q2, wq[0][2], A.q3, wq[0][3]);
      rows_gather(h_rows);
      int t = ta;
      auto tile_a = [&]() {
        alpha(t, A, B.as1, bxA1, [&]() { pos_load(rw); });
        wait_vm0();
        next_group(t + 1);
        rows_index<512>(a_idx);
        B = read_ops(pr.saB, pr.sdB, pr.de);
        pd = pd_load(rw);
        SB();
        beta(A, B, bxB0, [&]() { rows_gather(h_rows); });
      };
      for (; t + 2 <= tb; t += 2) {
        tile_a();
        alpha(t + 1, B, A.as1, bxB1, [&]() { pr = prep(rw, pd); });
        wait_vm0();
        next_group(t + 2);
        rows_index<768>(a_idx);
        A = read_ops(pr.saA, pr.sdA, pr.de);
        rw = raw_load(4);
        SB();
        beta(B, A, bxA0, [&]() { rows_gather(h_rows); });
        a_idx += 512;
        a_raw += 512;
      }
      if (t < tb) {
        tile_a();
        last_block(A.as1);
      } else {
        last_block(B.as1);
      }
      flush_last(glast);
    } else {
      wait_vm0();
    }
    pa += np;
    if (pa >= NP) break;
    wait_vm0();
    __syncthreads();
  }
  __syncthreads();

  u16* out = karg<u16>(6);
  int tid = tid_;
  asm volatile("" : "+v"(tid));
  const int lane = tid & 63;
  if (w < 6) {
    const int c = lane & 15, q = lane >> 4, f0 = 32 * (w & 1);
    const int nl = 16 * (w >> 1) + c, nlc = nl < BN ? nl : BN - 1;
    const unsigned hrow = L_H + nlc * 128, mrow = L_ACC + nlc * ACC_NODE + 3 * 256;
    const unsigned w_tr = L_W + (8 * q + (c >> 2)) * 128 + (f0 + 4 * (c & 3)) * 2;
    f32x4 d0 = {0.f, 0.f, 0.f, 0.f}, d1 = d0;
#pragma unroll
    for (int s = 0; s < 4; ++s) {
      bf16x8 bb;
      if (s < 2) {
        bb = lds_ld<bf16x8>(L, hrow + (32 * s + 8 * q) * 2);
      } else {
        const unsigned o = mrow + (32 * (s - 2) + 8 * q) * 4;
        const f32x4 m0 = lds_ld<f32x4>(L, o) * NLN2, m1 = lds_ld<f32x4>(L, o + 16) * NLN2;
        bb = as_frag(u32x4{cvt_pk(m0[0], m0[1]), cvt_pk(m0[2], m0[3]), cvt_pk(m1[0], m1[1]), cvt_pk(m1[2], m1[3])});
      }
      const u32x2 a0 = lds_tr(L, w_tr + s * 4096), a1 = lds_tr(L, w_tr + s * 4096 + 512);
      const u32x2 a2 = lds_tr(L, w_tr + s * 4096 + 32), a3 = lds_tr(L, w_tr + s * 4096 + 544);
      d0 = __builtin_amdgcn_mfma_f32_16x16x32_bf16(as_frag(u32x4{a0[0], a0[1], a1[0], a1[1]}), bb, d0, 0, 0, 0);
      d1 = __builtin_amdgcn_mfma_f32_16x16x32_bf16(as_frag(u32x4{a2[0], a2[1], a3[0], a3[1]}), bb, d1, 0, 0, 0);
    }
    if (nl < BN) {
#pragma unroll
      for (int ft = 0; ft < 2; ++ft) {
        const f32x4 d = ft ? d1 : d0;
        const int f = f0 + 16 * ft + 4 * q;
        const u32x2 hv = lds_ld<u32x2>(L, hrow + 2 * f);
        u32x2 o;
#pragma unroll
        for (int k = 0; k < 2; ++k) {
          const unsigned pb = cvt_pk(d[2 * k], d[2 * k + 1]);
          const f32x2 x = {bf_lo(pb), bf_hi(pb)}, y = x * NLOG2E;
          const f32x2 den = f32x2{__builtin_amdgcn_exp2f(y[0]), __builtin_amdgcn_exp2f(y[1])} + 1.0f;
          const f32x2 act = x * f32x2{__builtin_amdgcn_rcpf(den[0]), __builtin_amdgcn_rcpf(den[1])};
          const unsigned ab = cvt_pk(act[0], act[1]);
          const f32x2 sum = f32x2{bf_lo(hv[k]), bf_hi(hv[k])} + f32x2{bf_lo(ab), bf_hi(ab)};
          o[k] = cvt_pk(sum[0], sum[1]);
        }
        *(u32x2*)(out + (size_t)(n0 + nl) * 64 + f) = o;
      }
    }
  } else {
    const int nc = tid - 384, ncc = nc < 3 * BN ? nc : 3 * BN - 1;
    const int n = ncc / 3, c = ncc - 3 * n;
    const unsigned o = L_ACC + n * ACC_NODE + c * 256;
    f32x4 s4 = {0.f, 0.f, 0.f, 0.f};
#pragma unroll
    for (int k = 0; k < 16; ++k) s4 += lds_ld<f32x4>(L, o + 16 * k) * lds_ld<f32x4>(L, L_WX + 16 * k);
    if (nc < 3 * BN) {
      const float s = (s4[0] + s4[1]) + (s4[2] + s4[3]);
      const float p0 = lds_ld<float>(L, L_PD + 16 * n + 4 * c);
      out[(size_t)NN * 64 + (size_t)n0 * 3 + nc] = (u16)cvt_pk(p0 + bf_round(s * NLN2), 0.f);
    }
  }
  if (blockIdx.x == 0 && tid == 511)
    __builtin_amdgcn_raw_buffer_store_b64(lds_ld<u32x2>(L, L_TAG + 8), ws_rsrc(karg<const char>(5)), WS_CTL, 0, 16);
}

}

extern "C" void kernel_launch(void* const* d_in, const int* in_sizes, int n_in, void* d_out, int out_size, void* d_ws, size_t ws_size,
                              hipStream_t stream) {
  const u16* h = (const u16*)d_in[0];
  const u16* pos = (const u16*)d_in[1];
  const u16* ea = (const u16*)d_in[2];
  const int* src = (const int*)d_in[3];
  const int* dst = (const int*)d_in[4];
  const u16* we = (const u16*)d_in[5];
  const u16* wx = (const u16*)d_in[6];
  const u16* wh = (const u16*)d_in[7];
  egnn_kernel<<<NBK, 512, 0, stream>>>(h, pos, we, wx, wh, (char*)d_ws, (u16*)d_out, src, dst, ea);
}
```
